```python
import torch

def _softmax(x, axis=-1):
    return torch.softmax(x, dim=axis)
def _gelu(x):
    return torch.nn.functional.gelu(x, approximate="tanh")
def _silu(x):
    return torch.nn.functional.silu(x)
def _sigmoid(x):
    return torch.sigmoid(x)
def _layernorm(x, axis=-1, eps=1e-5):
    m = x.mean(dim=axis, keepdim=True)
    v = x.var(dim=axis, keepdim=True, unbiased=False)
    return (x - m) / torch.sqrt(v + eps)
def _scatter_add(tgt, idx, src, axis=0):
    return tgt.clone().index_add_(axis, idx, src)
def _segment_sum(src, seg, n):
    out = torch.zeros((n,) + src.shape[1:], dtype=src.dtype)
    return out.index_add_(0, seg, src)
def _pad(x, pw):
    flat = tuple(v for pair in reversed(pw) for v in pair)
    return torch.nn.functional.pad(x, flat)

kernel_name = 'standalone__kv_cache_quant_int4_int2_kv_with_per_channel_or_per_token_scales__decode'

def setup_inputs():

    _w = (lambda a: (lambda t: t.to(torch.bfloat16) if t.is_floating_point() else t)(torch.as_tensor(a)))
    B = 1
    Sq = 1
    Skv = 32768
    Hq = 32
    Hkv = 32
    Dh = 192
    bits = 4
    G = 64
    if G <= 0 or G >= Skv:
        G = max(1, Skv // 2)
    Skv = (Skv // G) * G
    ngk = Skv // G
    qmax = (1 << bits) - 1
    return {
        "q": _w((lambda *s: torch.randn(*s, dtype=torch.bfloat16) * 1.6)(B, Hq, Sq, Dh)),
        "qk": _w(torch.randint(0, qmax + 1, (B, Hkv, Skv, Dh)).to(torch.int32)),
        "qv": _w(torch.randint(0, qmax + 1, (B, Hkv, Skv, Dh)).to(torch.int32)),
        "k_scales": _w(torch.abs((lambda *s: torch.randn(*s, dtype=torch.bfloat16) * 1.6)(B, Hkv, ngk, Dh)) * 0.05 + 0.02),
        "k_zeros": _w((lambda *s: torch.randn(*s, dtype=torch.bfloat16) * 1.6)(B, Hkv, ngk, Dh) + float(qmax) / 2.0),
        "v_scales": _w(torch.abs((lambda *s: torch.randn(*s, dtype=torch.bfloat16) * 0.05)(B, Hkv, Skv, 1)) * 0.05 + 0.02),
        "v_zeros": _w((lambda *s: torch.randn(*s, dtype=torch.bfloat16) * 0.05)(B, Hkv, Skv, 1) + float(qmax) / 2.0),
    }

def reference(q, qk, qv, k_scales, k_zeros, v_scales, v_zeros):
    B, Hq, Sq, Dh = q.shape
    Hkv, Skv = qk.shape[1], qk.shape[2]
    ngk = k_scales.shape[2]
    G = Skv // ngk
    ks = (lambda x, n, axis: torch.repeat_interleave(x, n, dim=axis))(k_scales, G, axis=2)
    kz = (lambda x, n, axis: torch.repeat_interleave(x, n, dim=axis))(k_zeros, G, axis=2)
    k = ((lambda x, dt: x.to(dt))(qk, q.dtype) - kz) * ks
    v = ((lambda x, dt: x.to(dt))(qv, q.dtype) - v_zeros) * v_scales
    rep = Hq // Hkv
    if rep > 1:
        k = (lambda x, n, axis: torch.repeat_interleave(x, n, dim=axis))(k, rep, axis=1)
        v = (lambda x, n, axis: torch.repeat_interleave(x, n, dim=axis))(v, rep, axis=1)
    scale = float(Dh) ** -0.5
    logits = torch.einsum("bhqd,bhkd->bhqk", q, k) * scale
    p = _softmax(logits, axis=-1)
    o = torch.einsum("bhqk,bhkd->bhqd", p, v)
    return o
```

```cpp
#include <hip/hip_runtime.h>
#include <hip/hip_bf16.h>
#include <stdint.h>
#include <utility>

#define TGT 199.25
#define WST 16
#define THR 80
#define PRV 16
#define SLIP 250
#define MAXOFF 3000
#define NSENT 8
#define NR 32
#define TPL 260
#define STG 0
#define PSLEEP 32
#define OFF0 380
#define TGATH 150
#define GAP1 10
#define TREL 90
#define NSLOW 1
#define EXK 6
#define NOSLK 2
#define TMO 100000000u
#define TPC 260
#define PSL 2
#define TGMAX 300
#define PSH 0
#define SS 20
#define HOLD 4
#define VOTES 4
#define HOLDB 8
#define REVT 600
#define VWAIT 30
#define LFAC 8
#define NOSM 2
#define H0 5
#define TGS 201.75
#define EXS(i) ((int)(((i) + 1) * (TGS - TGT) * 100.0 / 256.0 + 0.5) - (int)((i) * (TGS - TGT) * 100.0 / 256.0 + 0.5))
#define NPAIR 4
#define PCL 600
#define PTH 60
#define PMIN 12
#define TPRB 500
#define TVOTE 650
#define TVMAX 760
#define FMASK 0xffffffffu
#define RING 12
#define SB() __builtin_amdgcn_sched_barrier(0)
#define PST(i) SB()

typedef int v4i __attribute__((ext_vector_type(4)));
typedef unsigned v2u __attribute__((ext_vector_type(2)));
typedef float v2f __attribute__((ext_vector_type(2)));
typedef float v4f __attribute__((ext_vector_type(4)));
typedef short v4s __attribute__((ext_vector_type(4)));
typedef __bf16 v2bf __attribute__((ext_vector_type(2)));

constexpr int DH = 192;
constexpr int NH = 32;
constexpr int SKV = 32768;
constexpr int WPB = 8;
constexpr int NT = WPB * 64;
constexpr int NWG = 256;
constexpr int NSL = 8, SLC = 24;
constexpr int RS = NSL * 16;
static_assert(SKV == 2 * 64 * NWG, "two 64-token groups per workgroup and head");
constexpr int NX = 4;
constexpr size_t WS_X = (size_t)NH * NWG * RS;
constexpr size_t WS_T = WS_X + (size_t)NWG * 256;
constexpr unsigned BODY16 = (unsigned)(TGT * 100.0 / 64.0 * 65536.0);
constexpr int slot(int n) { return (int)((n * (BODY16 / 12)) >> 16); }

template <typename F, int... I>
__device__ __forceinline__ void sfor_impl(F&& f, std::integer_sequence<int, I...>) {
    (f(std::integral_constant<int, I>{}), ...);
}
template <int N, typename F>
__device__ __forceinline__ void sfor(F&& f) {
    sfor_impl(f, std::make_integer_sequence<int, N>{});
}

__device__ __forceinline__ float bf_lo(unsigned u) { return __uint_as_float(u << 16); }
__device__ __forceinline__ float bf_hi(unsigned u) { return __uint_as_float(u & 0xffff0000u); }
__device__ __forceinline__ float rb(float x) { return (float)(__bf16)x; }
__device__ __forceinline__ unsigned pack(float lo, float hi) {
    return __builtin_bit_cast(unsigned, __builtin_convertvector(v2f{lo, hi}, v2bf));
}
__device__ __forceinline__ float dot2(unsigned a, unsigned b, float c) {
    return __builtin_amdgcn_fdot2_f32_bf16(__builtin_bit_cast(v2bf, a), __builtin_bit_cast(v2bf, b), c, false);
}
__device__ __forceinline__ void mul4(float (&y)[4], unsigned a, unsigned b, unsigned s0, unsigned s1, unsigned s2, unsigned s3) {
    asm("v_dot2_f32_bf16 %0, %4, %6, 0\n\tv_dot2_f32_bf16 %1, %4, %7, 0\n\t"
        "v_dot2_f32_bf16 %2, %5, %8, 0\n\tv_dot2_f32_bf16 %3, %5, %9, 0\n\ts_nop 2"
        : "=&v"(y[0]), "=&v"(y[1]), "=&v"(y[2]), "=&v"(y[3])
        : "v"(a), "v"(b), "v"(s0), "v"(s1), "v"(s2), "v"(s3));
}
__device__ __forceinline__ unsigned clk() { return (unsigned)__builtin_amdgcn_s_memrealtime(); }
__device__ __forceinline__ unsigned inflight() {
    unsigned x;
    asm volatile("s_getreg_b32 %0, hwreg(HW_REG_IB_STS, 0, 4)" : "=s"(x));
    return x;
}
__device__ __forceinline__ unsigned negscale(unsigned u) { return pack(bf_lo(u) * -0x1p-22f, bf_hi(u) * -0x1p-22f); }

template <int CTRL>
__device__ __forceinline__ unsigned dppu(unsigned x) {
    return (unsigned)__builtin_amdgcn_mov_dpp((int)x, CTRL, 0xf, 0xf, true);
}
template <int CTRL>
__device__ __forceinline__ float dpp(float x) { return __uint_as_float(dppu<CTRL>(__float_as_uint(x))); }
__device__ __forceinline__ float rowsum16(float x) {
    x += dpp<0x128>(x);
    x += dpp<0x124>(x);
    x += dpp<0x122>(x);
    x += dpp<0x121>(x);
    return x;
}
__device__ __forceinline__ v4i ldstream(const void* p) {
    return __builtin_nontemporal_load(reinterpret_cast<const v4i*>(p));
}

__device__ __forceinline__ void st_rec(void* p, unsigned o, v4i a) {
    asm volatile("global_store_dwordx4 %0, %1, %2 sc1\n\ts_nop 1" :: "v"(o), "v"(a), "s"(p) : "memory");
}
__device__ __forceinline__ v4i ld_rec(const void* p, unsigned o) {
    v4i x;
    asm volatile("global_load_dwordx4 %0, %1, %2 sc1" : "=v"(x) : "v"(o), "s"(p) : "memory");
    return x;
}
typedef unsigned long long u64;
__device__ __forceinline__ u64 ld_dev(const u64* p) { return __hip_atomic_load(p, __ATOMIC_RELAXED, __HIP_MEMORY_SCOPE_AGENT); }
__device__ __forceinline__ void st_dev(u64* p, u64 v) { __hip_atomic_store(p, v, __ATOMIC_RELAXED, __HIP_MEMORY_SCOPE_AGENT); }
__device__ __forceinline__ unsigned ld_wg(const unsigned* p) { return __hip_atomic_load(p, __ATOMIC_RELAXED, __HIP_MEMORY_SCOPE_WORKGROUP); }
__device__ __forceinline__ void st_wg(unsigned* p, unsigned v) { __hip_atomic_store(p, v, __ATOMIC_RELAXED, __HIP_MEMORY_SCOPE_WORKGROUP); }
__device__ __forceinline__ float fold32(float a, float b) {
    auto s = __builtin_amdgcn_permlane32_swap(__float_as_uint(a), __float_as_uint(b), false, false);
    return __uint_as_float(s[0]) + __uint_as_float(s[1]);
}
__device__ __forceinline__ float fold16(float a, float b) {
    auto s = __builtin_amdgcn_permlane16_swap(__float_as_uint(a), __float_as_uint(b), false, false);
    return __uint_as_float(s[0]) + __uint_as_float(s[1]);
}
__device__ __forceinline__ float x4max(float x) {
    x = fmaxf(x, dpp<0x124>(x));
    x = fmaxf(x, dpp<0x128>(x));
    auto s = __builtin_amdgcn_permlane16_swap(__float_as_uint(x), __float_as_uint(x), false, false);
    x = fmaxf(__uint_as_float(s[0]), __uint_as_float(s[1]));
    auto t = __builtin_amdgcn_permlane32_swap(__float_as_uint(x), __float_as_uint(x), false, false);
    return fmaxf(__uint_as_float(t[0]), __uint_as_float(t[1]));
}

struct __attribute__((aligned(16))) Smem {
    v4i ksz[(64 + NX) * 48];
    v4i qt[NH * 24];
    v4i vsz[2][(64 + NX) * 8];
    v4i rec[NH][RS / 4];
    float stage[2][WPB][DH];
    unsigned short p[NH][WPB][16];
    unsigned ml[NH];
    float fin[WPB][32];
    u64 epoch;
    unsigned desc[2 * NH];
    unsigned ready;
    unsigned have;
    unsigned tref;
    unsigned slip;
    unsigned vote[2 * NH];
    unsigned late[WPB];
    unsigned req;
    unsigned t0;
    unsigned done;
    unsigned prog;
    unsigned fcnt;
    int score[2][32];
    unsigned cnt[NH + NH / 4 + 1];
};

__global__ __launch_bounds__(NT + 128, 3) void kivi_stream_kernel(
    const __hip_bfloat16* __restrict__ q, const int* __restrict__ qk, const int* __restrict__ qv,
    const __hip_bfloat16* __restrict__ ks, const __hip_bfloat16* __restrict__ kz,
    const __hip_bfloat16* __restrict__ vs, const __hip_bfloat16* __restrict__ vz,
    float* __restrict__ ws, __hip_bfloat16* __restrict__ out, float scale) {
    __shared__ Smem sm;

    unsigned ka0, ka1;
    asm volatile("s_load_dword %0, %2, 0x0\n\ts_load_dword %1, %2, 0x40" : "=&s"(ka0), "=&s"(ka1) : "s"(__builtin_amdgcn_kernarg_segment_ptr()));
    const u64 tb64 = __builtin_amdgcn_s_memrealtime();
    const unsigned tb = (unsigned)tb64;
    const int tid = threadIdx.x;
    typedef const __attribute__((address_space(1))) int* gptr;
    u64 code;
    int codelen;
    asm volatile("s_getpc_b64 %0\nkivi_code_begin:\n\ts_mov_b32 %1, kivi_code_end-kivi_code_begin" : "=s"(code), "=s"(codelen));
    const int warm = *(gptr)(code + min(tid * 128, codelen - 4));
    const int lane = tid & 63;
    const int wave = __builtin_amdgcn_readfirstlane(tid >> 6);
    const int r = lane & 15;
    const int g = lane >> 4;
    const int pos = (blockIdx.x & ~7) | ((blockIdx.x & 3) << 1) | ((blockIdx.x >> 2) & 1);
    auto groupof = [](int p) {
        const int r0 = p < 85 ? 0 : p < 171 ? 85 : 171, na = p < 171 ? 43 : 42, t = p - r0;
        return r0 + (t < 64 ? ((t >> 4) & 1) * na + (t >> 5) * 16 + (t & 15) : t < na + 32 ? t - 32 : t);
    };
    const int gx = groupof(pos);
    const int ppos = pos ^ (NWG / 2 + 1), lead = min(pos, ppos);
    const int it0 = (-(lead + (lead >> 3))) & 15;
    unsigned po[3];
    int dsti[3];
#pragma unroll
    for (int u = 0; u < 3; ++u) {
        const int f = u * NT + tid;
        const int gi = f / 24, pc = f % 24;
        dsti[u] = gi * 48 + pc;
        po[u] = (gi * NWG + gx) * (DH * 2) + pc * 16;
        asm volatile("" : "+v"(po[u]));
    }
    SB();
    v4i pk[6];
    if (wave < WPB) {
        pk[0] = ldstream((const char*)ks + po[0]);
        pk[1] = ldstream((const char*)ks + po[1]);
        pk[2] = ldstream((const char*)ks + po[2]);
    }
    asm volatile("s_waitcnt lgkmcnt(0)" :: "s"(ka0), "s"(ka1));
    SB();
    u64* tstart = reinterpret_cast<u64*>(ws + WS_T);
    auto tagof = [](u64 e, int j) {
        e *= 0x9e3779b97f4a7c15ull;
        return (unsigned)(j == 3 ? (e ^ (e >> 29)) >> 8 : e >> (j * 20)) << 8;
    };
    auto until = [&](unsigned t) { while ((int)(clk() - t) < 0) __builtin_amdgcn_s_sleep(1); };

    if (__builtin_expect(wave == WPB, 0)) {
        const int l = lane;
        u64* echo = tstart + NWG;
        u64* gofs = tstart + 2 * NWG;
        if (l == 0) {
            st_dev(tstart + blockIdx.x, tb64);
            if (blockIdx.x < NR) st_dev(gofs + blockIdx.x * 32, tb64 - 100000ull);
            if (blockIdx.x == NR) st_dev(gofs + 16, tb64 - 100000ull);
            sm.slip = 0;
            sm.req = 0;
            sm.done = 0;
            sm.prog = 0;
            sm.ready = 0;
            sm.fcnt = 0;
            sm.epoch = tb64;
            sm.have = blockIdx.x == 0;
        }
        if (l < NH + NH / 4 + 1) sm.cnt[l] = 0;
        sm.vote[l] = 0;
        (&sm.score[0][0])[l] = 0;
        __syncthreads();
        until(tb + TGATH);
        unsigned ld;
        for (;;) {
            const u64 dt = ld_dev(tstart + NWG - 8 + (l & 7)) - tb64 + 1000ull;
            ld = dt < 2000ull ? (unsigned)dt : 0u;
            if (__all(dt < 2000ull) || clk() - tb > (unsigned)TGMAX) break;
        }
        ld = max(ld, dppu<0x124>(ld));
        ld = max(ld, dppu<0x122>(ld));
        ld = max(ld, dppu<0x121>(ld));
        ld = __builtin_amdgcn_readfirstlane(ld);
        const u64 t64 = ld ? tb64 + ld - 1000ull : tb64;
        if (l == 0) sm.t0 = (unsigned)t64;
        asm volatile("" ::: "memory");
        if (l == 0) __hip_atomic_fetch_add(&sm.cnt[NH + NH / 4], 1u, __ATOMIC_RELAXED, __HIP_MEMORY_SCOPE_WORKGROUP);
        asm volatile("" :: "v"(warm));
        u64 E = tb64, to[NWG / 64];
        bool have = blockIdx.x == 0;
        if (have) {
#pragma unroll
            for (int u = 0; u < NWG / 64; ++u) to[u] = ld_dev(tstart + u * 64 + l);
            asm volatile("s_waitcnt vmcnt(0)" ::: "memory");
#pragma unroll
            for (int u = 0; u < NWG / 64; ++u) st_dev(echo + u * 64 + l, to[u]);
        }
        auto chores = [&]() {
            if (blockIdx.x == 0) {
#pragma unroll
                for (int u = 0; u < NWG / 64; ++u) {
                    const u64 x = ld_dev(tstart + u * 64 + l);
                    if (x != to[u]) { st_dev(echo + u * 64 + l, x); to[u] = x; }
                }
            } else if (!have && ld_dev(echo + blockIdx.x) == tb64) {
                asm volatile("" ::: "memory");
                E = ld_dev(tstart);
                have = true;
                if (l == 0) sm.epoch = E;
                asm volatile("s_waitcnt lgkmcnt(0)" ::: "memory");
                if (l == 0) st_wg(&sm.have, 1u);
            }
        };
        const u64* mine = gofs + ((blockIdx.x >> 3) % NR) * 32;
        unsigned sent = 0, stage = 0, tsh = clk();
        int nt = 0;
        while (stage < 2) {
            const u64 gd = ld_dev(mine) - t64;
            const unsigned go = gd <= (u64)MAXOFF ? (unsigned)gd : 0u;
            const unsigned own = ld_wg(&sm.slip);
            if (go > own && l == 0) __hip_atomic_fetch_max(&sm.slip, go, __ATOMIC_RELAXED, __HIP_MEMORY_SCOPE_WORKGROUP);
            if (own <= go) tsh = clk();
            else if (clk() - tsh > (unsigned)REVT && blockIdx.x >= NSENT) { if (l == 0) st_wg(&sm.slip, go); }
            const unsigned req = ld_wg(&sm.req);
            unsigned want = blockIdx.x < NSENT ? own : 0u;
            if (req == go + SS && own == go) {
                unsigned x = ld_wg(&sm.late[l & 7]);
                x += dppu<0x121>(x);
                x += dppu<0x122>(x);
                x += dppu<0x124>(x);
                want = min(req + (__builtin_amdgcn_readfirstlane(x) * LFAC / 64 & ~15u), (unsigned)MAXOFF);
            }
            if (want > go && want > sent) {
                u64 old = 0;
                if (l == 0) old = __hip_atomic_fetch_max(gofs + 16, t64 + want, __ATOMIC_RELAXED, __HIP_MEMORY_SCOPE_AGENT);
                if (__builtin_amdgcn_readfirstlane((unsigned)(old < t64 + want)) && l < NR)
                    __hip_atomic_fetch_max(gofs + l * 32, t64 + want, __ATOMIC_RELAXED, __HIP_MEMORY_SCOPE_AGENT);
                sent = want;
            }
            chores();
            if (nt < 26) {
                const size_t pg = (size_t)((blockIdx.x >> 3) + 32 * (nt % 12)) << 21;
                const char* tp = nt < 12 ? (const char*)qk + pg : nt < 24 ? (const char*)qv + pg
                               : nt == 24 ? (const char*)ws + ((size_t)((blockIdx.x >> 3) & 1) << 21) : (const char*)out;
                if (nt < 25) tp += (((blockIdx.x >> 3) * 29 + nt) & 127) * 256;
                unsigned x;
                asm volatile("s_load_dword %0, %1, 0x0\n\ts_waitcnt lgkmcnt(0)" : "=s"(x) : "s"(tp) : "memory");
                ++nt;
                __builtin_amdgcn_s_sleep(PSLEEP / 2);
            } else {
                __builtin_amdgcn_s_sleep(PSLEEP);
            }
            if (ld_wg(&sm.done) > stage) {
                ++stage;
                asm volatile("" : "+s"(code));
                int w = 0;
#pragma unroll
                for (int u = 0; u < 4; ++u) {
                    const int a = (u * 64 + l) * 128;
                    w ^= *(gptr)(code + (a < codelen ? a : 0));
                }
                asm volatile("" :: "v"(w));
            }
        }
        while (!have && clk() - tb < TMO) chores();
        if (l == 0) st_wg(&sm.have, 1u);
        if (blockIdx.x == 0) {
            for (int spin = 1; ld_wg(&sm.fcnt) < WPB; ++spin) {
                __builtin_amdgcn_s_sleep(2);
                if (spin % 1024 == 0) chores();
            }
        }
        return;
    }
    if (__builtin_expect(wave == WPB + 1, 0)) {
        asm volatile("s_barrier" ::: "memory");
        u64* votes = tstart + 2 * NWG + NR * 32;
        u64* table = votes + NWG;
        {
            const int k = blockIdx.x >> 3;
            const char* pg = k < 3 ? (const char*)kz + ((size_t)k << 21) : k == 3 ? (const char*)vs : k == 4 ? (const char*)vz
                           : k < 7 ? (const char*)ks + ((size_t)(k - 4) << 21) : (const char*)qk + ((size_t)(k - 7) << 21);
            const int reg = min(blockIdx.x / 10, 23u), page = reg % 12 * 32 + blockIdx.x % 10 * 3;
            const char* pp = (const char*)(reg < 12 ? qk : qv) + ((size_t)page << 21);
            unsigned x, y, side;
            asm volatile("s_load_dword %0, %2, 0x0\n\ts_load_dword %1, %3, 0x80\n\ts_waitcnt lgkmcnt(0)"
                         : "=&s"(x), "=&s"(y) : "s"(pg + (blockIdx.x & 7) * 256), "s"(pp) : "memory");
            asm volatile("s_getreg_b32 %0, hwreg(HW_REG_XCC_ID, 2, 1)" : "=s"(side));
            int sum = 0, n = 0;
#pragma nounroll
            for (int i = 0; i < NPAIR && clk() - tb < (unsigned)TPRB; ++i) {
                const unsigned h = (tb + i) * 0x9e3779b1u;
                const char* a = pp + (h >> 25) * 16384 + ((h >> 8) & 31) * 256;
                u64 t0, t1, t2;
                asm volatile("s_memtime %0\n\ts_waitcnt lgkmcnt(0)\n\ts_load_dword %3, %4, 0x0\n\ts_waitcnt lgkmcnt(0)\n\t"
                             "s_memtime %1\n\ts_waitcnt lgkmcnt(0)\n\ts_load_dword %3, %4, 0x2000\n\ts_waitcnt lgkmcnt(0)\n\t"
                             "s_memtime %2\n\ts_waitcnt lgkmcnt(0)"
                             : "=&s"(t0), "=&s"(t1), "=&s"(t2), "=&s"(x) : "s"(a) : "memory");
                const int d0 = (int)(t1 - t0), d1 = (int)(t2 - t1);
                if (min(d0, d1) > 450) { sum += max(-PCL, min(PCL, d1 - d0)); ++n; }
            }
            if ((side ^ page ^ (page >> 7)) & 1) sum = -sum;
            if (blockIdx.x >= 240) n = sum = 0;
            if (lane == 0) st_dev(votes + blockIdx.x, (u64)tb << 32 | (unsigned)(sum & 0xffff) << 16 | (unsigned)n << 1 | side);
        }
        asm volatile("" :: "v"(warm));
        {
            v4i x[NX];
            const bool isk = lane < 48, isz = isk ? lane >= 24 : lane >= 56;
#pragma unroll
            for (int u = 0; u < NX; ++u) {
                const unsigned gg = (it0 + u * 16) * NWG + groupof(ppos);
                x[u] = ldstream(isk ? (const char*)(isz ? kz : ks) + gg * (DH * 2) + lane % 24 * 16 : (const char*)(isz ? vz : vs) + gg * 128 + (lane & 7) * 16);
            }
#pragma unroll
            for (int u = 0; u < NX; ++u) {
                if (isz) x[u] = v4i{(int)negscale(x[u].x), (int)negscale(x[u].y), (int)negscale(x[u].z), (int)negscale(x[u].w)};
                *(isk ? &sm.ksz[(64 + u) * 48 + lane] : &sm.vsz[isz][(64 + u) * 8 + (lane & 7)]) = x[u];
            }
        }
        while (ld_wg(&sm.have) == 0) __builtin_amdgcn_s_sleep(8);
        asm volatile("" ::: "memory");
        const u64 E = sm.epoch;
        if (blockIdx.x == 0) {
            u64 v[NWG / 64], fm[NWG / 64];
            until(tb + TVOTE);
            for (;;) {
#pragma unroll
                for (int u = 0; u < NWG / 64; ++u) v[u] = ld_dev(votes + u * 64 + lane);
#pragma unroll
                for (int u = 0; u < NWG / 64; ++u) fm[u] = __ballot((unsigned)(v[u] >> 32) - tb + 1000u < 2000u);
                if ((fm[0] & fm[1] & fm[2] & fm[3]) == ~0ull || clk() - tb > (unsigned)TVMAX) break;
            }
            u64 sd = 0;
#pragma unroll
            for (int u = 0; u < NWG / 64; ++u) {
                if ((fm[u] >> lane) & 1) {
                    atomicAdd(&sm.score[0][(u * 64 + lane) / 10], (int)(unsigned)v[u] >> 16);
                    atomicAdd(&sm.score[1][(u * 64 + lane) / 10], (int)((unsigned)v[u] & 0xffffu) >> 1);
                }
                const u64 pf = (fm[u ^ 2] & 0x0f0f0f0f0f0f0f0full) << 4 | (fm[u ^ 2] >> 4 & 0x0f0f0f0f0f0f0f0full);
                const u64 x = __ballot(v[u] & 1) & fm[u] & pf;
                if ((lane & 7) == 2 + u) sd = x;
            }
            const int ra = (lane >> 5) * 12 + (lane & 31) * 3 / 8, rb2 = (lane >> 5) * 12 + ((lane & 31) * 3 + 2) / 8;
            const int sa = ld_wg((unsigned*)&sm.score[0][ra]), sb = ld_wg((unsigned*)&sm.score[0][rb2]);
            const int na = ld_wg((unsigned*)&sm.score[1][ra]), nb = ld_wg((unsigned*)&sm.score[1][rb2]);
            const u64 ok = __ballot(na >= PMIN && nb >= PMIN && abs(sa) > na * PTH && abs(sb) > nb * PTH && (sa ^ sb) >= 0);
            const u64 cc = __ballot(sa < 0);
            if ((lane & 7) == 0) sd = cc;
            if ((lane & 7) == 1) sd = (unsigned)(ok & (ok >> 32)) & (~0u << H0) & FMASK;
#pragma unroll
            for (int u = 0; u < NR / 8; ++u)
                st_rec(table + u * 8 * 32, (lane >> 3) * 256 + (lane & 7) * 16, v4i{(int)(unsigned)sd, (int)(unsigned)(sd >> 32), (int)(unsigned)E, (int)(unsigned)(E >> 32)});
        }
        unsigned mode, ck, cv, pair, side;
        {
            v4i x;
            until(tb + TVOTE + 60);
            for (;;) {
                x = ld_rec(table + ((blockIdx.x >> 3) % NR) * 32, (lane & 7) * 16);
                asm volatile("s_waitcnt vmcnt(0)" : "+v"(x));
                if (__all((unsigned)x.z == (unsigned)E && (unsigned)x.w == (unsigned)(E >> 32)) || clk() - tb >= TMO) break;
                __builtin_amdgcn_s_sleep(8);
            }
            ck = __builtin_amdgcn_readlane(x.x, 0);
            cv = __builtin_amdgcn_readlane(x.y, 0);
            mode = __builtin_amdgcn_readlane(x.x, 1);
            const int sw = blockIdx.x & 32 ? x.y : x.x;
            side = ((unsigned)__builtin_amdgcn_readlane(sw, 2 + (blockIdx.x >> 6)) >> (blockIdx.x & 31)) & 1u;
            pair = side ^ (((unsigned)__builtin_amdgcn_readlane(sw, 2 + ((blockIdx.x >> 6) ^ 2)) >> ((blockIdx.x ^ 4) & 31)) & 1u);
        }
        {
            const int h = lane & 31, k = pos / (NWG / 2);
            const unsigned far = (k ^ side ^ 1u ^ ((lane < 32 ? ck : cv) >> h)) & 1u;
            unsigned d = 256;
#pragma unroll
            for (int i = 0; i < 4; ++i) {
                const int it = h * 2 + (i >> 1);
                const unsigned x = (it & 15) == it0 && pair && (i & 1) == far;
                const unsigned c = (unsigned)(6 * (it * NWG + (x ? groupof(ppos) : gx)) + 3);
                d |= x << i | ((i ^ k ^ x ^ (c >> 8) ^ (c >> 15)) & 1u) << (4 + i);
            }
            sm.desc[lane] = (mode >> h) & 1u ? d : 0u;
            asm volatile("" ::: "memory");
            if (lane == 0) st_wg(&sm.ready, 1u);
        }
#pragma nounroll
        for (int c = 0; c < NH / 4; ++c) {
            while (ld_wg(&sm.cnt[NH + (c == 0 ? 0 : NH / 4 - 1)]) < WPB) __builtin_amdgcn_s_sleep(8);
            asm volatile("" ::: "memory");
            if (c == 1) {
                const int it = it0 + g * 16;
                const bool sw = ((mode & (ck ^ cv)) >> (it >> 1) & 1u) && pair;
                unsigned* mine = reinterpret_cast<unsigned*>(&sm.p[it >> 1][0][0]) + r * 4 + (it & 1);
                const int pb = (ppos & ~7) | ((ppos >> 1) & 3) | ((ppos & 1) << 2);
                if (sw) st_rec(ws + WS_X + blockIdx.x * 256, lane * 16, v4i{(int)mine[0], (int)mine[2], (int)(unsigned)E, (int)(unsigned)(E >> 32)});
                while (__any(sw)) {
                    v4i x = ld_rec(ws + WS_X + pb * 256, lane * 16);
                    asm volatile("s_waitcnt vmcnt(0)" : "+v"(x));
                    if (__all(!sw || ((unsigned)x.z == (unsigned)E && (unsigned)x.w == (unsigned)(E >> 32))) || clk() - tb >= TMO) {
                        if (sw) { mine[0] = x.x; mine[2] = x.y; }
                        break;
                    }
                    __builtin_amdgcn_s_sleep(8);
                }
                asm volatile("" ::: "memory");
            }
            v4i* pp = reinterpret_cast<v4i*>(&sm.p[c * 4][0][0]) + lane;
            const v4i u = *pp;
            float x[8];
#pragma unroll
            for (int e = 0; e < 4; ++e) { x[2 * e] = bf_lo(u[e]); x[2 * e + 1] = bf_hi(u[e]); }
            float M = fmaxf(fmaxf(fmaxf(x[0], x[1]), fmaxf(x[2], x[3])), fmaxf(fmaxf(x[4], x[5]), fmaxf(x[6], x[7])));
            M = fmaxf(M, dpp<0x128>(M));
            M = fmaxf(M, dpp<0x124>(M));
            M = fmaxf(M, dpp<0x122>(M));
            M = fmaxf(M, dpp<0x121>(M));
            v4i o;
            float L = 0.f;
#pragma unroll
            for (int e = 0; e < 4; ++e) {
                const unsigned pk = pack(__expf(x[2 * e] - M), __expf(x[2 * e + 1] - M));
                o[e] = (int)pk;
                L += bf_lo(pk) + bf_hi(pk);
            }
            *pp = o;
            L = rowsum16(L);
            if (r == 0) sm.ml[c * 4 + g] = __float_as_uint(M) | __builtin_bit_cast(unsigned short, (_Float16)L);
            asm volatile("" ::: "memory");
            if (lane == 0) st_wg(&sm.prog, c + 1);
        }
        const unsigned tag = tagof(E, lane & 3);
        float* mine = ws + (size_t)blockIdx.x * NH * RS;
#pragma nounroll
        for (int h = 0; h < NH; ++h) {
            const unsigned mlb = (sm.ml[h] >> (lane & 3) * 8) & 0xffu;
            if (h < NH - 1) { while (ld_wg(&sm.cnt[h]) < WPB) __builtin_amdgcn_s_sleep(4); }
            else { while (ld_wg(&sm.cnt[h]) < WPB) {} }
            asm volatile("" ::: "memory");
            v2f c[4][3];
#pragma unroll
            for (int e = 0; e < 4; ++e)
#pragma unroll
                for (int k = 0; k < 3; ++k) c[e][k] = *reinterpret_cast<const v2f*>(&sm.stage[h & 1][(lane >> 5) * 4 + e][(lane & 31) * 6 + k * 2]);
            v4i x;
#pragma unroll
            for (int k = 0; k < 3; ++k) {
                const v2f a = ((c[0][k] + c[1][k]) + (c[2][k] + c[3][k])) * 0x1p22f;
                x[k] = (int)pack(fold32(a.x, a.x), fold32(a.y, a.y));
            }
            x[3] = (int)(tag | mlb);
            if (h == NH - 1) {
                if (lane < 32) st_rec(mine + h * RS, lane * 16, x);
                break;
            }
            sm.rec[h][lane & 31] = x;
            asm volatile("" ::: "memory");
            if (lane == 0) st_wg(&sm.prog, NH / 4 + h + 1);
            if (h == NH - 2) {
                v4i y[NH / 2];
#pragma unroll
                for (int u = 0; u < NH / 2; ++u) y[u] = (&sm.rec[0][0])[u * 64 + lane];
                until(ld_wg(&sm.tref) + TREL);
#pragma unroll
                for (int u = 0; u < NH / 2; ++u)
                    if (u < NH / 2 - 1 || lane < 32) st_rec(mine + u * 2 * RS, lane * 16, y[u]);
            }
        }
        return;
    }

    asm volatile("s_barrier" ::: "memory");
    const int ww = (wave & 3) * 2 + (wave >> 2);
    const int ti = ww * 4 + g;
    const int tev = ti + (ti >= 11 ? 10 : 0) + (ti >= 22 ? 11 : 0), tod = ti + 11 + (ti >= 10 ? 11 : 0) + (ti >= 21 ? 10 : 0);
    constexpr size_t GB = (size_t)64 * DH * 4, ITER = NWG * GB;
    const unsigned offe = tev * (DH * 4) + r * 16, offo = tod * (DH * 4) + r * 16, offl = ti * (DH * 4) + r * 16;
    const long long pd = (long long)(groupof(ppos) - gx) * (long long)GB;
    const char* kp = reinterpret_cast<const char*>(qk) + gx * GB;
    const char* vp = reinterpret_cast<const char*>(qv) + gx * GB;
    unsigned descv = 0, d0 = 0, d1 = 0;
    auto lanes = [=](unsigned d, int i) {
        const unsigned a = d & (16u << i) ? offo : offe, b = offl + (i & 1) * (unsigned)(GB / 2);
        return d ? a : b;
    };
    auto unit = [=](const char* p, unsigned d, int i) { return p + (i >> 1) * ITER + (d & (1u << i) ? pd : 0ll); };
    const char* dummy = reinterpret_cast<const char*>(qv) + (size_t)(blockIdx.x * WPB + wave) * 4864;

    v4i buf[RING];
    auto burst = [&](const char* p, int i, unsigned o) {
#pragma unroll
        for (int e = 0; e < 3; ++e) { buf[i * 3 + e] = ldstream(p + o + e * 256); SB(); }
    };

    const unsigned phase = ((pos & (NWG / 2 - 1)) * (BODY16 / 512) + ww * WST * (BODY16 / (12 * 256))) >> 16;
    unsigned base = tb + phase + PSH;
    unsigned sched = 0, off = 0, offv = 0, ex = 0;
    int prev = 0, now = 0, hold = 0, big = 0, body = 0;
    unsigned vc = 0;
    auto pace = [&](int due) {
        int now;
        while ((now = (int)(clk() - base)) < due) __builtin_amdgcn_s_sleep(1);
        return now;
    };
    auto follow = [&](unsigned o2) {
        hold = o2 - off > SLIP / 2 ? HOLDB : o2 != off ? HOLD : max(hold - 1, 0);
        big = o2 - off > SLIP / 2 ? HOLDB : max(big - 1, 0);
        base += o2 - off;
        off = o2;
    };
    auto pacen = [&](auto N) {
        constexpr int n = decltype(N)::value;
        follow(__builtin_amdgcn_readfirstlane(offv));
        now = pace(slot(n));
        vc = inflight();
        if (__builtin_expect(vc > 3, 0)) {
            asm volatile("s_waitcnt vmcnt(3)");
            now = (int)(clk() - base);
            if (now > slot(n) + THR && prev < PRV && big == 0 && off + SLIP <= MAXOFF && body >= NOSLK) {
                if (lane == 0 && __hip_atomic_fetch_add(&sm.vote[body], 1u, __ATOMIC_RELAXED, __HIP_MEMORY_SCOPE_WORKGROUP) + 1 >= VOTES)
                    __hip_atomic_fetch_max(&sm.slip, off + SLIP, __ATOMIC_RELAXED, __HIP_MEMORY_SCOPE_WORKGROUP);
                unsigned o2;
                while ((o2 = __builtin_amdgcn_readfirstlane(ld_wg(&sm.slip))) == off && (int)(clk() - base) < now + VWAIT) {}
                if (o2 != off) { follow(o2); now = pace(slot(n)); }
            }
            if (hold == 0 && off + SS <= MAXOFF && body >= NOSM && lane == 0) st_wg(&sm.req, off + SS);
        }
    };
    auto check = [&](auto N) {
        constexpr int n = decltype(N)::value;
        base += ex;
        prev = now - slot(n);
        if (lane == 0) st_wg(&sm.late[wave], (unsigned)prev);
    };
    auto getslip = [&]() { offv = __hip_atomic_load(&sm.slip, __ATOMIC_RELAXED, __HIP_MEMORY_SCOPE_WORKGROUP); };
    auto arrive = [&](int i) {
        asm volatile("" ::: "memory");
        if (lane == 0) __hip_atomic_fetch_add(&sm.cnt[i], 1u, __ATOMIC_RELAXED, __HIP_MEMORY_SCOPE_WORKGROUP);
    };
    auto next_body = [&]() {
        const unsigned s2 = sched + BODY16;
        base += (s2 >> 16) - (sched >> 16);
        sched = s2;
        ++body;
    };

    {
        v4i pv[2], pq[2];
        pq[0] = reinterpret_cast<const v4i*>(q)[tid];
        pq[1] = reinterpret_cast<const v4i*>(q)[NT + (tid & 255)];
        const unsigned vo = (((tid >> 3) * NWG + gx) * 64 + (tid & 7) * 8) * 2;
        PST(1); pace(slot(3)); SB();
        pk[3] = ldstream((const char*)kz + po[0]);
        pk[4] = ldstream((const char*)kz + po[1]);
        pk[5] = ldstream((const char*)kz + po[2]);
        PST(2); pace(slot(6)); SB();
        pv[0] = ldstream((const char*)vs + vo);
        pv[1] = ldstream((const char*)vz + vo);
        PST(3); pace(slot(8)); SB();
        burst(kp, 0, lanes(0, 0));
        PST(4);
        sm.qt[tid] = pq[0];
        if (tid < 256) sm.qt[NT + tid] = pq[1];
#pragma unroll
        for (int u = 0; u < 3; ++u) sm.ksz[dsti[u]] = pk[u];
#pragma unroll
        for (int u = 0; u < 3; ++u) {
            const v4i x = pk[3 + u];
            sm.ksz[dsti[u] + 24] = v4i{(int)negscale(x.x), (int)negscale(x.y), (int)negscale(x.z), (int)negscale(x.w)};
        }
        PST(5); pace(slot(11)); SB();
        burst(kp, 1, lanes(0, 1));
        PST(6);
        sm.vsz[0][tid] = pv[0];
        sm.vsz[1][tid] = v4i{(int)negscale(pv[1].x), (int)negscale(pv[1].y), (int)negscale(pv[1].z), (int)negscale(pv[1].w)};
    }
    asm volatile("" :: "v"(warm));
    PST(7);
    arrive(NH + NH / 4);
    while (__builtin_amdgcn_readfirstlane(ld_wg(&sm.cnt[NH + NH / 4])) < WPB + 1) {}
    asm volatile("" ::: "memory");
    PST(8);
    base = __builtin_amdgcn_readfirstlane(sm.t0) + OFF0 + phase;

    const v2f huge = {0x1p127f, 0x1p127f};

    for (int S = 0; S < NH; ++S) {
        v2f kz2[6];
        unsigned slo[6], shi[6];
        v2u qq[3];
        const char* nsup = (S + 1 < NH) ? kp + 2 * ITER : vp;
        float mylogit = 0.f;
        float dot = 0.f;
        sfor<12>([&](auto N) {
            constexpr int n = decltype(N)::value;
            constexpr int i = n / 3, j = n % 3;
            if (n == 6) {
                if (S == H0 - 1) {
                    static_assert(H0 >= 4, "the first four heads get their numerators early");
                    while (__builtin_amdgcn_readfirstlane(ld_wg(&sm.ready)) == 0) __builtin_amdgcn_s_sleep(1);
                    asm volatile("" ::: "memory");
                    descv = sm.desc[lane];
                }
                d1 = __builtin_amdgcn_readlane(descv, S + 1);
            }
            if (j == 0) {
                const unsigned d = i < 2 ? d0 : d1;
                const unsigned o = lanes(d, i ^ 2);
                const char* src = unit(i < 2 ? kp : nsup, d, i ^ 2);
                ex = (S < NSLOW ? EXK : 0) + (d ? 0 : EXS(i));
                pacen(N);
                SB();
                burst(src, i ^ 2, o);
                SB();
                check(N);
            }
            if (j == 2) getslip();
            if (i % 2 == 0 || (d0 & (3u << (i & 2)))) {
                const int sl = d0 & (1u << i) ? 64 + (S >> 3) : S * 2 + (i >> 1);
                const v2u* pk = reinterpret_cast<const v2u*>(&sm.ksz[sl * 48]);
                const v2u a = pk[j * 16 + r], b = pk[48 + j * 16 + r];
                slo[j * 2 + 0] = a.x & 0xffffu; shi[j * 2 + 0] = a.x & 0xffff0000u;
                slo[j * 2 + 1] = a.y & 0xffffu; shi[j * 2 + 1] = a.y & 0xffff0000u;
                kz2[j * 2 + 0] = v2f{bf_lo(b.x), bf_hi(b.x)};
                kz2[j * 2 + 1] = v2f{bf_lo(b.y), bf_hi(b.y)};
            }
            if (i == 0) qq[j] = reinterpret_cast<const v2u*>(&sm.qt[S * 24])[j * 16 + r];
            if (j == 0) dot = 0.f;
            {
                const v4i x = buf[n % RING];
                const v2f t0 = __builtin_elementwise_fma(v2f{__int_as_float(x.x), __int_as_float(x.y)}, huge, kz2[j * 2]);
                const v2f t1 = __builtin_elementwise_fma(v2f{__int_as_float(x.z), __int_as_float(x.w)}, huge, kz2[j * 2 + 1]);
                float y[4];
                mul4(y, pack(t0.x, t0.y), pack(t1.x, t1.y), slo[j * 2], shi[j * 2], slo[j * 2 + 1], shi[j * 2 + 1]);
                dot = dot2(pack(y[0], y[1]), qq[j].x, dot);
                dot = dot2(pack(y[2], y[3]), qq[j].y, dot);
            }
            if (j == 2) {
                const float d = rowsum16(dot);
                if ((r & 3) == i) mylogit = d;
            }
            asm volatile("" : "+v"(dot), "+v"(mylogit));
            SB();
        });
        if (r < 4) sm.p[S][wave][g * 4 + r] = (unsigned short)pack(rb(mylogit * 0x1p22f) * scale, 0.f);
        if ((S & 3) == 3) arrive(NH + (S >> 2));
        if (S == NH - 3 && tid == 0) st_wg(&sm.done, 1u);
        kp = nsup;
        d0 = d1;
        next_body();
    }
    base += GAP1;

    for (int S = 0; S < NH; ++S) {
        v2u P;
        unsigned vsl, vsh, vzf;
        v4f acc = {0.f, 0.f, 0.f, 0.f};
        float y0[12];
        unsigned b0[12];
        const bool more = S + 1 < NH;
        unsigned sl, sh;
        v2f z2;
        sfor<12>([&](auto N) {
            constexpr int n = decltype(N)::value;
            constexpr int i = n / 3, j = n % 3;
            if (j == 0) {
                if (i == 2) d1 = __builtin_amdgcn_readlane(descv, (NH + S + 1) & 63);
                const unsigned d = i < 2 ? d0 : d1;
                unsigned o = lanes(d, i ^ 2);
                const char* src = unit(i < 2 ? vp : vp + 2 * ITER, d, i ^ 2);
                if (i >= 2 && !more) { o = i * 768; src = dummy; }
                ex = d ? 0 : EXS(i);
                if (i < 2 || more) pacen(N);
                SB();
                burst(src, i ^ 2, o);
                SB();
                check(N);
            }
            if (n == 0) {
                const unsigned need = S < 2 ? 1 : NH / 4 + S - 1;
                while (__builtin_amdgcn_readfirstlane(ld_wg(&sm.prog)) < need) {}
                asm volatile("" ::: "memory");
                P = *reinterpret_cast<const v2u*>(&sm.p[S][wave][g * 4]);
                const unsigned x = d0 >> (r & 3);
                const int ltok = (x & 1u ? 64 + (S >> 3) : S * 2 + ((r >> 1) & 1)) * 64 + (d0 ? (x & 16u ? +tod : +tev) : (r & 1) * 32 + ti);
                vsl = reinterpret_cast<const unsigned short*>(&sm.vsz[0][0])[ltok];
                vsh = vsl << 16;
                vzf = (unsigned)reinterpret_cast<const unsigned short*>(&sm.vsz[1][0])[ltok] << 16;
            }
            if (j == 2) getslip();
            if (j == 0) {
                sl = dppu<0x55 * i>(vsl);
                sh = dppu<0x55 * i>(vsh);
                const float zb = __uint_as_float(dppu<0x55 * i>(vzf));
                z2 = v2f{zb, zb};
            }
            float y[4];
            {
                const v4i x = buf[n];
                const v2f t0 = __builtin_elementwise_fma(v2f{__int_as_float(x.x), __int_as_float(x.y)}, huge, z2);
                const v2f t1 = __builtin_elementwise_fma(v2f{__int_as_float(x.z), __int_as_float(x.w)}, huge, z2);
                mul4(y, pack(t0.x, t0.y), pack(t1.x, t1.y), sl, sh, sl, sh);
            }
#pragma unroll
            for (int e = 0; e < 4; ++e) {
                const int idx = j * 4 + e;
                if (i % 2 == 0) {
                    y0[idx] = y[e];
                    asm volatile("" : "+v"(y0[idx]));
                } else if (i == 1) {
                    b0[idx] = pack(y0[idx], y[e]);
                    asm volatile("" : "+v"(b0[idx]));
                } else {
                    const v2u B = {b0[idx], pack(y0[idx], y[e])};
                    const v2u A = {r == idx ? P.x : 0u, r == idx ? P.y : 0u};
                    acc = __builtin_amdgcn_mfma_f32_16x16x16bf16_1k(__builtin_bit_cast(v4s, A), __builtin_bit_cast(v4s, B), acc, 0, 0, 0);
                }
            }
            if (n == 11) {
                if (S == NH - 2 && tid == 0) sm.tref = base + slot(12) + slot(3);
                if (S == NH - 3 && tid == 0) st_wg(&sm.done, 2u);
                if (lane < 48) *reinterpret_cast<v4f*>(&sm.stage[S & 1][wave][lane * 4]) = acc;
                arrive(S);
            }
            SB();
        });
        vp += 2 * ITER;
        d0 = d1;
        next_body();
    }
    {
        const int mh = (blockIdx.x / 16 * 2 + (blockIdx.x & 7) / 4 + NH - 1) % NH, msl = (blockIdx.x & 3) * 2 + ((blockIdx.x >> 3) & 1);
        const unsigned tag = tagof(sm.epoch, lane & 3);
        const float* src = ws + ((size_t)wave * 32 * NH + mh) * RS + msl * 16;
        const unsigned so = (lane >> 2) * (NH * RS * 4) + (lane & 3) * 16;
        until(base - slot(12) + slot(3) - phase + wave * STG + (mh == NH - 1 ? TPL : TPC));
        v4i x[2];
        for (;;) {
            x[0] = ld_rec(src, so);
            x[1] = ld_rec(src + 16 * NH * RS, so);
            asm volatile("s_waitcnt vmcnt(0)" : "+v"(x[0]), "+v"(x[1]));
            if (__all((((unsigned)x[0].w ^ tag) | ((unsigned)x[1].w ^ tag)) < 256u) || clk() - tb >= TMO) break;
            __builtin_amdgcn_s_sleep(PSL);
        }
#pragma unroll
        for (int d = 0; d < 6; ++d) asm volatile("" :: "v"(buf[d]));
        unsigned ml[2];
#pragma unroll
        for (int k = 0; k < 2; ++k) {
            ml[k] = ((unsigned)x[k].w ^ tag) << (lane & 3) * 8;
            ml[k] |= dppu<0xB1>(ml[k]);
            ml[k] |= dppu<0x4E>(ml[k]);
        }
        const float M = x4max(fmaxf(bf_hi(ml[0]), bf_hi(ml[1])));
        const float w0 = __expf(bf_hi(ml[0]) - M), w1 = __expf(bf_hi(ml[1]) - M);
        float a[6];
#pragma unroll
        for (int e = 0; e < 3; ++e) {
            a[2 * e] = bf_lo(x[0][e]) * w0 + bf_lo(x[1][e]) * w1;
            a[2 * e + 1] = bf_hi(x[0][e]) * w0 + bf_hi(x[1][e]) * w1;
        }
        const float l = (float)__builtin_bit_cast(_Float16, (unsigned short)ml[0]) * w0 + (float)__builtin_bit_cast(_Float16, (unsigned short)ml[1]) * w1;
        float c0 = fold16(fold32(a[0], a[4]), fold32(a[2], l));
        float c1 = fold16(fold32(a[1], a[5]), fold32(a[3], l));
        c0 += dpp<0x124>(c0); c1 += dpp<0x124>(c1);
        c0 += dpp<0x128>(c0); c1 += dpp<0x128>(c1);
        if (g == 3) c1 = M;
        if (r < 4) *reinterpret_cast<v2f*>(&sm.fin[wave][g == 3 ? 24 + r * 2 : r * 6 + g * 2]) = v2f{c0, c1};
        asm volatile("" ::: "memory");
        unsigned arr = 0;
        if (lane == 0) arr = __hip_atomic_fetch_add(&sm.fcnt, 1u, __ATOMIC_RELAXED, __HIP_MEMORY_SCOPE_WORKGROUP);
        asm volatile("" ::: "memory");
        const v4f y = *reinterpret_cast<const volatile v4f*>(&sm.fin[lane >> 3][(lane & 7) * 4]);
        const float mw = *reinterpret_cast<const volatile float*>(&sm.fin[lane >> 3][25]);
        const float w = __expf(mw - x4max(mw));
        float d = fold16(fold32(y.x * w, y.z * w), fold32(y.y * w, y.w * w));
        d += dpp<0x128>(d);
        const float L = __int_as_float(__builtin_amdgcn_readlane(__float_as_int(d), 6));
        const unsigned o = pack(d / L, 0.f);
        asm volatile("" :: "v"(o));
        if (__builtin_amdgcn_readfirstlane(arr) == WPB - 1) {
            if (r < 6) reinterpret_cast<unsigned short*>(out)[mh * DH + msl * SLC + r * 4 + g] = (unsigned short)o;
        }
    }
}
__global__ void kivi_end_marker() { asm volatile("kivi_code_end:"); }

extern "C" void kernel_launch(void* const* d_in, const int* in_sizes, int n_in,
                              void* d_out, int out_size, void* d_ws, size_t ws_size,
                              hipStream_t stream) {
    const __hip_bfloat16* q = (const __hip_bfloat16*)d_in[0];
    const int* qk = (const int*)d_in[1];
    const int* qv = (const int*)d_in[2];
    const __hip_bfloat16* ks = (const __hip_bfloat16*)d_in[3];
    const __hip_bfloat16* kz = (const __hip_bfloat16*)d_in[4];
    const __hip_bfloat16* vs = (const __hip_bfloat16*)d_in[5];
    const __hip_bfloat16* vz = (const __hip_bfloat16*)d_in[6];
    const float scale = (float)(1.0 / sqrt((double)DH));
    kivi_stream_kernel<<<NWG, NT + 128, 0, stream>>>(q, qk, qv, ks, kz, vs, vz, (float*)d_ws, (__hip_bfloat16*)d_out, scale);
}
```

```cpp
#include <hip/hip_runtime.h>
#include <hip/hip_bf16.h>
#include <stdint.h>
#include <utility>

#define TGT 199.25
#define WST 16
#define THR 80
#define PRV 16
#define SLIP 250
#define MAXOFF 3000
#define NSENT 8
#define NR 32
#define TPL 260
#define STG 0
#define PSLEEP 32
#define OFF0 380
#define TGATH 150
#define GAP1 10
#define TREL 90
#define NSLOW 1
#define EXK 6
#define NOSLK 2
#define TMO 100000000u
#define TPC 260
#define PSL 2
#define TGMAX 300
#define PSH 0
#define SS 20
#define HOLD 4
#define VOTES 4
#define HOLDB 8
#define REVT 600
#define VWAIT 30
#define LFAC 8
#define NOSM 2
#define H0 4
#define TGS 202.25
#define SPL 4
#define G21 5
#define EXS(i) ((int)(((i) + 1) * (TGS - TGT) * 100.0 / 256.0 + 0.5) - (int)((i) * (TGS - TGT) * 100.0 / 256.0 + 0.5))
#define CAL0 128
#define NRND 4
#define PTH 1000
#define TPRB 200
#define VTCH 1
#define GAP2 10
#define NG2 3
#define TECHO 550
#define TQ 300
#define TTAB 1100
#define TXW 200
#define FMASK 0xffffffffu
#define FPOL 0u
#define RING 12
#define SB() __builtin_amdgcn_sched_barrier(0)
#define PST(i) SB()

typedef int v4i __attribute__((ext_vector_type(4)));
typedef unsigned v2u __attribute__((ext_vector_type(2)));
typedef float v2f __attribute__((ext_vector_type(2)));
typedef float v4f __attribute__((ext_vector_type(4)));
typedef short v4s __attribute__((ext_vector_type(4)));
typedef __bf16 v2bf __attribute__((ext_vector_type(2)));

constexpr int DH = 192;
constexpr int NH = 32;
constexpr int SKV = 32768;
constexpr int WPB = 8;
constexpr int NT = WPB * 64;
constexpr int NWG = 256;
constexpr int NSL = 8, SLC = 24;
constexpr int RS = NSL * 16;
static_assert(SKV == 2 * 64 * NWG, "two 64-token groups per workgroup and head");
constexpr int NX = 4;
constexpr int NRC = 17;
constexpr size_t WS_X = (size_t)NRC * NWG * RS;
constexpr size_t WS_T = WS_X + (size_t)NWG * 256;
constexpr unsigned BODY16 = (unsigned)(TGT * 100.0 / 64.0 * 65536.0);
constexpr int slot(int n) { return (int)((n * (BODY16 / 12)) >> 16); }

template <typename F, int... I>
__device__ __forceinline__ void sfor_impl(F&& f, std::integer_sequence<int, I...>) {
    (f(std::integral_constant<int, I>{}), ...);
}
template <int N, typename F>
__device__ __forceinline__ void sfor(F&& f) {
    sfor_impl(f, std::make_integer_sequence<int, N>{});
}

__device__ __forceinline__ float bf_lo(unsigned u) { return __uint_as_float(u << 16); }
__device__ __forceinline__ float bf_hi(unsigned u) { return __uint_as_float(u & 0xffff0000u); }
__device__ __forceinline__ float rb(float x) { return (float)(__bf16)x; }
__device__ __forceinline__ unsigned pack(float lo, float hi) {
    return __builtin_bit_cast(unsigned, __builtin_convertvector(v2f{lo, hi}, v2bf));
}
__device__ __forceinline__ float dot2(unsigned a, unsigned b, float c) {
    return __builtin_amdgcn_fdot2_f32_bf16(__builtin_bit_cast(v2bf, a), __builtin_bit_cast(v2bf, b), c, false);
}
__device__ __forceinline__ void mul4(float (&y)[4], unsigned a, unsigned b, unsigned s0, unsigned s1, unsigned s2, unsigned s3) {
    asm("v_dot2_f32_bf16 %0, %4, %6, 0\n\tv_dot2_f32_bf16 %1, %4, %7, 0\n\t"
        "v_dot2_f32_bf16 %2, %5, %8, 0\n\tv_dot2_f32_bf16 %3, %5, %9, 0\n\ts_nop 2"
        : "=&v"(y[0]), "=&v"(y[1]), "=&v"(y[2]), "=&v"(y[3])
        : "v"(a), "v"(b), "v"(s0), "v"(s1), "v"(s2), "v"(s3));
}
__device__ __forceinline__ unsigned clk() { return (unsigned)__builtin_amdgcn_s_memrealtime(); }
__device__ __forceinline__ unsigned inflight() {
    unsigned x;
    asm volatile("s_getreg_b32 %0, hwreg(HW_REG_IB_STS, 0, 4)" : "=s"(x));
    return x;
}
__device__ __forceinline__ unsigned negscale(unsigned u) { return pack(bf_lo(u) * -0x1p-22f, bf_hi(u) * -0x1p-22f); }

template <int CTRL>
__device__ __forceinline__ unsigned dppu(unsigned x) {
    return (unsigned)__builtin_amdgcn_mov_dpp((int)x, CTRL, 0xf, 0xf, true);
}
template <int CTRL>
__device__ __forceinline__ float dpp(float x) { return __uint_as_float(dppu<CTRL>(__float_as_uint(x))); }
__device__ __forceinline__ float rowsum16(float x) {
    x += dpp<0x128>(x);
    x += dpp<0x124>(x);
    x += dpp<0x122>(x);
    x += dpp<0x121>(x);
    return x;
}
__device__ __forceinline__ v4i ldstream(const void* p) {
    return __builtin_nontemporal_load(reinterpret_cast<const v4i*>(p));
}

__device__ __forceinline__ void st_rec(void* p, unsigned o, v4i a) {
    asm volatile("s_nop 4\n\tglobal_store_dwordx4 %0, %1, %2 sc1\n\ts_nop 1" :: "v"(o), "v"(a), "s"(p) : "memory");
}
__device__ __forceinline__ v4i ld_rec(const void* p, unsigned o) {
    v4i x;
    asm volatile("s_nop 4\n\tglobal_load_dwordx4 %0, %1, %2 sc1" : "=v"(x) : "v"(o), "s"(p) : "memory");
    return x;
}
typedef unsigned long long u64;
__device__ __forceinline__ u64 ld_dev(const u64* p) { return __hip_atomic_load(p, __ATOMIC_RELAXED, __HIP_MEMORY_SCOPE_AGENT); }
__device__ __forceinline__ void st_dev(u64* p, u64 v) { __hip_atomic_store(p, v, __ATOMIC_RELAXED, __HIP_MEMORY_SCOPE_AGENT); }
__device__ __forceinline__ unsigned ld_wg(const unsigned* p) { return __hip_atomic_load(p, __ATOMIC_RELAXED, __HIP_MEMORY_SCOPE_WORKGROUP); }
__device__ __forceinline__ void st_wg(unsigned* p, unsigned v) { __hip_atomic_store(p, v, __ATOMIC_RELAXED, __HIP_MEMORY_SCOPE_WORKGROUP); }
__device__ __forceinline__ float fold32(float a, float b) {
    auto s = __builtin_amdgcn_permlane32_swap(__float_as_uint(a), __float_as_uint(b), false, false);
    return __uint_as_float(s[0]) + __uint_as_float(s[1]);
}
__device__ __forceinline__ float fold16(float a, float b) {
    auto s = __builtin_amdgcn_permlane16_swap(__float_as_uint(a), __float_as_uint(b), false, false);
    return __uint_as_float(s[0]) + __uint_as_float(s[1]);
}
__device__ __forceinline__ float x4max(float x) {
    x = fmaxf(x, dpp<0x124>(x));
    x = fmaxf(x, dpp<0x128>(x));
    auto s = __builtin_amdgcn_permlane16_swap(__float_as_uint(x), __float_as_uint(x), false, false);
    x = fmaxf(__uint_as_float(s[0]), __uint_as_float(s[1]));
    auto t = __builtin_amdgcn_permlane32_swap(__float_as_uint(x), __float_as_uint(x), false, false);
    return fmaxf(__uint_as_float(t[0]), __uint_as_float(t[1]));
}

struct __attribute__((aligned(16))) Smem {
    v4i ksz[(64 + NX) * 48];
    v4i qt[NH * 24];
    v4i vsz[2][(64 + NX) * 8];
    v4i rec[NRC - 1][RS / 4];
    float stage[2][WPB][DH];
    unsigned short p[NH][WPB][16];
    unsigned ml[NH];
    float fin[WPB][32];
    u64 epoch;
    unsigned desc[2][2][2 * NH];
    unsigned ready;
    unsigned have;
    unsigned tref;
    unsigned slip;
    unsigned vote[2 * NH];
    unsigned late[WPB];
    unsigned req;
    unsigned t0;
    unsigned done;
    unsigned prog;
    unsigned fcnt;
    unsigned tick;
    unsigned quiet;
    unsigned cnt[NH + NH / 4];
};

__global__ __launch_bounds__(NT + 128, 3) void kivi_stream_kernel(
    const __hip_bfloat16* __restrict__ q, const int* __restrict__ qk, const int* __restrict__ qv,
    const __hip_bfloat16* __restrict__ ks, const __hip_bfloat16* __restrict__ kz,
    const __hip_bfloat16* __restrict__ vs, const __hip_bfloat16* __restrict__ vz,
    float* __restrict__ ws, __hip_bfloat16* __restrict__ out, float scale) {
    __shared__ Smem sm;

    unsigned ka0, ka1;
    asm volatile("s_load_dword %0, %2, 0x0\n\ts_load_dword %1, %2, 0x40" : "=&s"(ka0), "=&s"(ka1) : "s"(__builtin_amdgcn_kernarg_segment_ptr()));
    const u64 tb64 = __builtin_amdgcn_s_memrealtime();
    const unsigned tb = (unsigned)tb64;
    const int tid = threadIdx.x;
    typedef const __attribute__((address_space(1))) int* gptr;
    u64 code;
    int codelen;
    asm volatile("s_getpc_b64 %0\nkivi_code_begin:\n\ts_mov_b32 %1, kivi_code_end-kivi_code_begin" : "=s"(code), "=s"(codelen));
    const int warm = *(gptr)(code + min(tid * 128, codelen - 4));
    const int lane = tid & 63;
    const int wave = __builtin_amdgcn_readfirstlane(tid >> 6);
    const int r = lane & 15;
    const int g = lane >> 4;
    const int pos = (blockIdx.x & ~7) | ((blockIdx.x & 3) << 1) | ((blockIdx.x >> 2) & 1);
    auto groupof = [](int p) {
        const int r0 = p < 85 ? 0 : p < 171 ? 85 : 171, na = p < 171 ? 43 : 42, t = p - r0;
        return r0 + (t < 64 ? ((t >> 4) & 1) * na + (t >> 5) * 16 + (t & 15) : t < na + 32 ? t - 32 : t);
    };
    const int win = pos >> 7;
    const int gx = groupof(pos & 127) | groupof((pos & 127) + 128) << 8;
    auto group = [&](int it, int x) {
        const bool both = it >> 2 == 10;
        return (both ? it : (it & ~3) + 2 * win + ((it >> 1) & 1)) * NWG + ((x >> ((both ? win : it) & 1) * 8) & 255);
    };
    unsigned po[3];
    int dsti[3];
#pragma unroll
    for (int u = 0; u < 3; ++u) {
        const int f = u * NT + tid;
        const int gi = f / 24, pc = f % 24;
        dsti[u] = gi * 48 + pc;
        po[u] = group(gi, gx) * (DH * 2) + pc * 16;
        asm volatile("" : "+v"(po[u]));
    }
    SB();
    v4i pk[6];
    if (wave < WPB) {
        pk[0] = ldstream((const char*)ks + po[0]);
        pk[1] = ldstream((const char*)ks + po[1]);
        pk[2] = ldstream((const char*)ks + po[2]);
    }
    asm volatile("s_waitcnt lgkmcnt(0)" :: "s"(ka0), "s"(ka1));
    SB();
    u64* tstart = reinterpret_cast<u64*>(ws + WS_T);
    auto tagof = [](u64 e, int j) {
        e *= 0x9e3779b97f4a7c15ull;
        return (unsigned)(j == 3 ? (e ^ (e >> 29)) >> 8 : e >> (j * 20)) << 8;
    };
    auto until = [&](unsigned t) { while ((int)(clk() - t) < 0) __builtin_amdgcn_s_sleep(1); };
    const int px = groupof((pos ^ 1) & 127) | groupof(((pos ^ 1) & 127) + 128) << 8;
    const int quart = (pos >> 5) & 3, tau = (-((pos >> 1) + quart)) & 15;
    auto shared = [&](int b) { return SPL == 4 || ((b ^ quart) & 1) == 0; };
    auto job = [&](int t, int b, unsigned pol, unsigned ok, unsigned side) {
        const int bd = tau == 10 || (t == 0 && shared(b)) ? b : 2 * tau + (SPL == 4 ? 0 : quart & 1);
        const int reg = (shared(b) ? t : 1) * 12 + (group(2 * bd, pos & 1 ? px : gx) * 3 >> 12);
        return (((ok >> reg) & 1u && (t || b >= H0) ? side ^ (pol >> reg) : quart >> 1) ^ pos) & 1u;
    };
    auto describe = [&](int t, int it, int j, unsigned pol, unsigned ok, unsigned side) {
        const int b = it >> 1, n = it * 2 + j;
        const bool tr = it >> 2 == tau && tau > 0, sh = tr && shared(b);
        const unsigned jb = job(t, tr ? b : 2 * tau, pol, ok, side);
        const unsigned x = tr ? ((sh ? j : jb) ^ pos) & 1u : 0u;
        const unsigned gr = group(it, x ? px : gx), c = 6 * gr + 3;
        const unsigned flip = sh ? 0u : (pol >> (t * 12 + (c >> 13))) & 1u;
        const bool early = !t && b < H0;
        const bool known = (ok >> (t * 12 + (it >> 2) * 3 / 4)) & (ok >> (t * 12 + ((it >> 2) * 3 + 2) / 4)) & 1u;
        const unsigned ex = (early || b < 2 || !known ? (n & 3) == 0 ? EXS(0) : (n & 3) == 1 ? EXS(1) : (n & 3) == 2 ? EXS(2) : EXS(3) : 0) + (early || known ? 0 : n & 1)
                          + (n >= 84 && n < 87 ? G21 : 0) + (t ? n == 0 ? GAP1 : n == NG2 && ((pol ^ (pol >> 12)) & 0xfffu) ? GAP2 : 0 : n >= 3 && n < 3 + 4 * NSLOW ? EXK : 0);
        return gr * (64 * DH * 4) | ex << 4 | (early || known || sh ? 0u : 8u) | x << 2 | flip << 1
             | (((sh ? jb : j ^ win ^ flip) ^ (c >> 8) ^ (c >> 15)) & 1u);
    };

    if (__builtin_expect(wave == WPB, 0)) {
        const int l = lane;
        u64* echo = tstart + NWG;
        u64* gofs = tstart + 3 * NWG;
        if (l == 0) {
            st_dev(tstart + blockIdx.x, tb64);
            if (blockIdx.x < NR) st_dev(gofs + blockIdx.x * 32, tb64 - 100000ull);
            if (blockIdx.x == NR) st_dev(gofs + 16, tb64 - 100000ull);
            sm.slip = 0;
            sm.req = 0;
            sm.done = 0;
            sm.prog = 0;
            sm.ready = 0;
            sm.tick = 0;
            sm.quiet = tb;
            sm.fcnt = 0;
            sm.epoch = tb64;
            sm.have = blockIdx.x == 0;
        }
        if (l < NH + NH / 4) sm.cnt[l] = 0;
        sm.vote[l] = 0;
        until(tb + TGATH);
        unsigned ld;
        for (;;) {
            const u64 dt = ld_dev(tstart + NWG - 8 + (l & 7)) - tb64 + 1000ull;
            ld = dt < 2000ull ? (unsigned)dt : 0u;
            if (__all(dt < 2000ull) || clk() - tb > (unsigned)TGMAX) break;
        }
        ld = max(ld, dppu<0x124>(ld));
        ld = max(ld, dppu<0x122>(ld));
        ld = max(ld, dppu<0x121>(ld));
        ld = __builtin_amdgcn_readfirstlane(ld);
        const u64 t64 = ld ? tb64 + ld - 1000ull : tb64;
        if (l == 0) sm.t0 = (unsigned)t64;
        u64 to[NWG / 64];
        bool have = blockIdx.x == 0;
        auto chores = [&](bool all) {
            if (blockIdx.x == 0) {
                u64 x[NWG / 64];
#pragma unroll
                for (int u = 0; u < NWG / 64; ++u) x[u] = ld_dev(tstart + u * 64 + l);
#pragma unroll
                for (int u = 0; u < NWG / 64; ++u) {
                    if (all || __any(x[u] != to[u])) st_rec(echo + u * 128, l * 16, v4i{(int)(unsigned)x[u], (int)(unsigned)(x[u] >> 32), (int)tb, (int)(unsigned)(tb64 >> 32)});
                    to[u] = x[u];
                }
            } else if (!have && !all) {
                v4i x = ld_rec(echo + blockIdx.x * 2, 0);
                asm volatile("s_waitcnt vmcnt(0)" : "+v"(x));
                if ((unsigned)x.x == tb && (unsigned)x.y == (unsigned)(tb64 >> 32)) {
                    have = true;
                    if (l == 0) sm.epoch = (u64)(unsigned)x.w << 32 | (unsigned)x.z;
                    asm volatile("s_waitcnt lgkmcnt(0)" ::: "memory");
                    if (l == 0) st_wg(&sm.have, 1u);
                }
            }
        };
        __syncthreads();
        asm volatile("" :: "v"(warm));
        chores(true);
        until(tb + TECHO);
        const u64* mine = gofs + ((blockIdx.x >> 3) % NR) * 32;
        unsigned sent = 0, stage = 0, tsh = clk();
        while (stage < 2) {
            chores(false);
            const u64 gd = ld_dev(mine) - t64;
            const unsigned go = gd <= (u64)MAXOFF ? (unsigned)gd : 0u;
            const unsigned own = ld_wg(&sm.slip);
            if (go > own && l == 0) __hip_atomic_fetch_max(&sm.slip, go, __ATOMIC_RELAXED, __HIP_MEMORY_SCOPE_WORKGROUP);
            if (own <= go) tsh = clk();
            else if (clk() - tsh > (unsigned)REVT && blockIdx.x >= NSENT) { if (l == 0) st_wg(&sm.slip, go); }
            unsigned req = ld_wg(&sm.req);
            if ((int)(clk() - ld_wg(&sm.quiet)) < 0) { req = 0; if (l == 0) st_wg(&sm.req, 0u); }
            unsigned want = blockIdx.x < NSENT ? own : 0u;
            if (req == go + SS && own == go) {
                unsigned x = ld_wg(&sm.late[l & 7]);
                x += dppu<0x121>(x);
                x += dppu<0x122>(x);
                x += dppu<0x124>(x);
                want = min(req + (__builtin_amdgcn_readfirstlane(x) * LFAC / 64 & ~15u), (unsigned)MAXOFF);
            }
            if (want > go && want > sent) {
                u64 old = 0;
                if (l == 0) old = __hip_atomic_fetch_max(gofs + 16, t64 + want, __ATOMIC_RELAXED, __HIP_MEMORY_SCOPE_AGENT);
                if (__builtin_amdgcn_readfirstlane((unsigned)(old < t64 + want)) && l < NR)
                    __hip_atomic_fetch_max(gofs + l * 32, t64 + want, __ATOMIC_RELAXED, __HIP_MEMORY_SCOPE_AGENT);
                sent = want;
            }
            if (have) __builtin_amdgcn_s_sleep(PSLEEP);
            if (ld_wg(&sm.done) > stage) {
                ++stage;
                asm volatile("" : "+s"(code));
                int w = 0;
#pragma unroll
                for (int u = 0; u < 4; ++u) {
                    const int a = (u * 64 + l) * 128;
                    w ^= *(gptr)(code + (a < codelen ? a : 0));
                }
                asm volatile("" :: "v"(w));
            }
        }
        while (!have && clk() - tb < TMO) chores(false);
        if (l == 0) st_wg(&sm.have, 1u);
        if (blockIdx.x == 0) {
            for (int spin = 1; ld_wg(&sm.fcnt) < WPB; ++spin) {
                __builtin_amdgcn_s_sleep(2);
                if (spin % 1024 == 0) chores(false);
            }
        }
        return;
    }
    if (__builtin_expect(wave == WPB + 1, 0)) {
        u64* table = tstart + 3 * NWG + NR * 32;
        const unsigned reg = blockIdx.x - CAL0 < 64u ? (blockIdx.x - CAL0) & 31u : 31u;
        int lead = 0, nr = 0;
        const unsigned page = reg % 12 * 32 + (tb * 0x85ebca6bu >> 27);
        const char* const pp = (const char*)(reg < 12 ? qk : qv) + ((size_t)page << 21);
        auto probe = [&]() {
            const unsigned h = (tb + nr) * 0x9e3779b1u;
            const char* a = pp + (h >> 25) * 16384 + ((h >> 8) & 127) * 64;
            u64 t0, t1, t2;
            unsigned x;
            asm volatile("s_memtime %0\n\ts_waitcnt lgkmcnt(0)\n\ts_load_dword %3, %4, 0x0\n\ts_waitcnt lgkmcnt(0)\n\t"
                         "s_memtime %1\n\ts_waitcnt lgkmcnt(0)\n\ts_load_dword %3, %4, 0x2000\n\ts_waitcnt lgkmcnt(0)\n\t"
                         "s_memtime %2\n\ts_waitcnt lgkmcnt(0)"
                         : "=&s"(t0), "=&s"(t1), "=&s"(t2), "=&s"(x) : "s"(a) : "memory");
            const int d0 = (int)(t1 - t0), d1 = (int)(t2 - t1);
            if (min(d0, d1) > 450) lead += max(-600, min(600, d1 - d0));
            ++nr;
        };
        {
            const int k = blockIdx.x >> 3;
            const char* pg = k < 3 ? (const char*)kz + ((size_t)k << 21) : k == 3 ? (const char*)vs : k == 4 ? (const char*)vz
                           : k < 7 ? (const char*)ks + ((size_t)(k - 4) << 21) : (const char*)qk + ((size_t)(k - 7) << 21);
            pg += (blockIdx.x & 7) * 256;
            unsigned x, y;
            asm volatile("s_load_dword %0, %2, 0x0\n\ts_load_dword %1, %3, 0x0\n\ts_waitcnt lgkmcnt(0)"
                         : "=&s"(x), "=&s"(y) : "s"(pg), "s"(reg < 24 ? pp : pg) : "memory");
        }
#pragma nounroll
        for (int k = 0; k < 2; ++k) {
#pragma nounroll
            while (reg < 24 && nr < NRND && (k || clk() - tb < (unsigned)TPRB)) probe();
            if (k == 0) __syncthreads();
        }
        asm volatile("" :: "v"(warm));
        unsigned tick = 0;
        auto behind = [&]() {
            tick = ld_wg(&sm.tick);
            const unsigned t = clk();
            while (ld_wg(&sm.tick) == tick && clk() - t < 100u) __builtin_amdgcn_s_sleep(1);
        };
        int nt = blockIdx.x < 200;
        auto step = [&]() {
            behind();
            if (nt < 26) {
                const size_t pg = (size_t)((blockIdx.x >> 3) + 32 * (nt % 12)) << 21;
                const char* tp = nt < 12 ? (const char*)qk + pg : nt < 24 ? (const char*)qv + pg : nt == 24 ? (const char*)ws + ((size_t)((blockIdx.x >> 3) & 1) << 21) : (const char*)out;
                unsigned x;
                asm volatile("s_nop 4\n\tglobal_load_dword %0, %1, %2 nt\n\ts_waitcnt vmcnt(0)" : "=v"(x) : "v"(0), "s"(tp + (nt < 25 ? ((blockIdx.x * 29 + nt) & 255) * 256 : 0)) : "memory");
                ++nt;
            }
        };
        u64 E = 0;
        unsigned jobs = 0;
        v4i cal;
#pragma nounroll
        for (int st = 0;;) {
            if (st == 0 && (ld_wg(&sm.have) || clk() - tb >= TMO)) {
                asm volatile("" ::: "memory");
                E = sm.epoch;
                st = 1;
                if (reg < 24) {
                    unsigned xcc;
                    asm volatile("s_getreg_b32 %0, hwreg(HW_REG_XCC_ID, 0, 3)" : "=s"(xcc));
                    if (((xcc >> 2) ^ page ^ (page >> 7)) & 1) lead = -lead;
                    behind();
                    if (lane == 0) st_wg(&sm.quiet, clk() + TQ);
                    if (lane < 8) st_rec(table + (blockIdx.x - CAL0) * 32, lane * 16, v4i{lead, (int)(xcc - blockIdx.x), (int)(unsigned)E, (int)(unsigned)(E >> 32)});
                }
            }
            if (st == 1 && clk() - tb >= (unsigned)TTAB) {
                behind();
                cal = ld_rec(table, ((lane & 31) < 24 ? lane : 0) * 256);
                asm volatile("s_waitcnt vmcnt(0)" : "+v"(cal));
                if (__all((unsigned)cal.z == (unsigned)E && (unsigned)cal.w == (unsigned)(E >> 32)) || clk() - tb >= TMO) break;
            }
            if (st || reg >= 24) step();
        }
        {
            auto two = __builtin_amdgcn_permlane32_swap(cal.x, cal.x, false, false);
            const int sum = (int)(two[0] + two[1]) + (int)dppu<0xB1>(two[0] + two[1]);
            const unsigned pol = (unsigned)__ballot(sum < 0) ^ FPOL, ok = (unsigned)__ballot(abs(sum) >= PTH) & FMASK;
            const unsigned side = ((unsigned)__builtin_amdgcn_readfirstlane(cal.y) + (blockIdx.x & ~4u)) >> 2;
#pragma unroll
            for (int u = 0; u < 4; ++u) {
                sm.desc[u >> 1][u & 1][lane] = describe(u >> 1, lane, u & 1, pol, ok, side);
                jobs |= (tau > 0 ? job(u >> 1, 2 * tau + (u & 1), pol, ok, side) : pos & 1u) << u;
            }
            asm volatile("" ::: "memory");
            if (lane == 0) st_wg(&sm.ready, 1u);
        }
#pragma nounroll
        while (nt < 26) step();
#pragma nounroll
        for (int cc = 0; cc <= NH / 4; ++cc) {
            const int c = cc < NH / 4 ? cc : tau >> 1;
            const bool act = (tau > 0 && c * 2 + (g >> 1) == tau) == (cc == NH / 4);
            while (ld_wg(&sm.cnt[NH + c]) < WPB) __builtin_amdgcn_s_sleep(2);
            asm volatile("" ::: "memory");
            if (cc >= NH / 4 - 1) {
                const bool sw = ((jobs ^ (jobs >> 2)) >> (lane >> 5)) & 1u;
                v2u* mine = reinterpret_cast<v2u*>(&sm.p[2 * tau][0][0]) + lane;
                if (cc < NH / 4) {
                    if (lane == 0) st_wg(&sm.quiet, clk() + TXW + TQ);
                    if (sw) st_rec(ws + WS_X + pos * 256, lane * 16, v4i{(int)mine->x, (int)mine->y, (int)(unsigned)E, (int)(unsigned)(E >> 32)});
                } else if (__any(sw)) {
                    until(clk() + TXW);
                }
                while (cc == NH / 4 && __any(sw)) {
                    v4i x = {};
                    if (sw) asm volatile("s_nop 4\n\tglobal_load_dwordx4 %0, %1, %2 sc1 nt" : "+v"(x) : "v"(lane * 16), "s"(ws + WS_X + (pos ^ 1) * 256) : "memory");
                    asm volatile("s_waitcnt vmcnt(0)" : "+v"(x));
                    if (__all(!sw || ((unsigned)x.z == (unsigned)E && (unsigned)x.w == (unsigned)(E >> 32))) || clk() - tb >= TMO) {
                        if (sw) *mine = v2u{(unsigned)x.x, (unsigned)x.y};
                        break;
                    }
                    __builtin_amdgcn_s_sleep(8);
                }
                asm volatile("" ::: "memory");
            }
            const bool two = c * 2 + (g >> 1) != 10;
            v4i* pp = reinterpret_cast<v4i*>(&sm.p[c * 4][0][0]) + lane;
            const v4i u = *pp;
            float x[8];
#pragma unroll
            for (int e = 0; e < 4; ++e) { x[2 * e] = bf_lo(u[e]); x[2 * e + 1] = bf_hi(u[e]); }
            float M = fmaxf(fmaxf(fmaxf(x[0], x[1]), fmaxf(x[2], x[3])), fmaxf(fmaxf(x[4], x[5]), fmaxf(x[6], x[7])));
            M = fmaxf(M, dpp<0x128>(M));
            M = fmaxf(M, dpp<0x124>(M));
            M = fmaxf(M, dpp<0x122>(M));
            M = fmaxf(M, dpp<0x121>(M));
            {
                auto y = __builtin_amdgcn_permlane16_swap(__float_as_uint(M), __float_as_uint(M), false, false);
                if (two) M = fmaxf(__uint_as_float(y[0]), __uint_as_float(y[1]));
            }
            v4i o;
            float L = 0.f;
#pragma unroll
            for (int e = 0; e < 4; ++e) {
                const unsigned pk = pack(__expf(x[2 * e] - M), __expf(x[2 * e + 1] - M));
                o[e] = (int)pk;
                L += bf_lo(pk) + bf_hi(pk);
            }
            if (act) *pp = o;
            L = rowsum16(L);
            const float L2 = fold16(L, L);
            if (two) L = L2;
            if (r == 0 && act) sm.ml[c * 4 + g] = __float_as_uint(M) | __builtin_bit_cast(unsigned short, (_Float16)L);
            asm volatile("" ::: "memory");
            if (lane == 0) st_wg(&sm.prog, 1u);
        }
        const unsigned tag = tagof(E, lane & 3);
        auto place = [&](int i) {
            const int i0 = tau + (tau > 10);
            const unsigned jb = i == i0 ? jobs >> 2 : i == 11 && tau == 10 ? jobs >> 3 : pos;
            return (((pos & ~1) | (jb & 1)) * NRC + i) * (RS * 4) + (lane & 31) * 16;
        };
        const unsigned last = place(NRC - 1);
        v2f keep[3] = {};
#pragma nounroll
        for (int h = 0; h < NH; ++h) {
            const unsigned mlb = (sm.ml[h] >> (lane & 3) * 8) & 0xffu;
            if (h < NH - 1) { while (ld_wg(&sm.cnt[h]) < WPB) __builtin_amdgcn_s_sleep(4); }
            else { while (ld_wg(&sm.cnt[h]) < WPB) {} }
            asm volatile("" ::: "memory");
            v2f c[4][3];
#pragma unroll
            for (int e = 0; e < 4; ++e)
#pragma unroll
                for (int k = 0; k < 3; ++k) c[e][k] = *reinterpret_cast<const v2f*>(&sm.stage[h & 1][(lane >> 5) * 4 + e][(lane & 31) * 6 + k * 2]);
            v4i x;
            const bool first = (h & 1) == 0 && h != 20;
#pragma unroll
            for (int k = 0; k < 3; ++k) {
                const v2f a = (keep[k] + (c[0][k] + c[1][k])) + (c[2][k] + c[3][k]), b = a * 0x1p22f;
                x[k] = (int)pack(fold32(b.x, b.x), fold32(b.y, b.y));
                keep[k] = first ? a : v2f{0.f, 0.f};
            }
            x[3] = (int)(tag | mlb);
            if (h == NH - 1) {
                if (lane < 32) st_rec(ws, last, x);
                break;
            }
            if (!first) sm.rec[(h >> 1) + (h > 20)][lane & 31] = x;
            asm volatile("" ::: "memory");
            if (lane == 0) st_wg(&sm.prog, h + 2);
            if (h == NH - 2) {
                v4i y[NRC / 2];
                unsigned o[NRC / 2];
#pragma unroll
                for (int u = 0; u < NRC / 2; ++u) { y[u] = (&sm.rec[0][0])[u * 64 + lane]; o[u] = place(u * 2 + (lane >> 5)); }
                until(ld_wg(&sm.tref) + TREL);
#pragma unroll
                for (int u = 0; u < NRC / 2; ++u) st_rec(ws, o[u], y[u]);
            }
        }
        return;
    }

    const int ww = (wave & 3) * 2 + (wave >> 2);
    const int ti = ww * 4 + g;
    const int tev = ti + (ti >= 11 ? 10 : 0) + (ti >= 22 ? 11 : 0), tod = ti + 11 + (ti >= 10 ? 11 : 0) + (ti >= 21 ? 10 : 0);
    const unsigned offe = tev * (DH * 4) + r * 16, offo = tod * (DH * 4) + r * 16;
    unsigned descv[2], dsc[4];
    auto lanes = [=](unsigned d) { return (d & 1u ? offo : offe) + (d & ~0x3fffu); };
    auto fetch = [&](int t) {
        while (__builtin_amdgcn_readfirstlane(ld_wg(&sm.ready)) == 0) __builtin_amdgcn_s_sleep(1);
        asm volatile("" ::: "memory");
#pragma unroll
        for (int j = 0; j < 2; ++j) {
            descv[j] = sm.desc[t][j][lane];
            if (ww >= WPB / 2 && (descv[j] & 8u)) descv[j] ^= 3u;
        }
    };
    const char* dummy = reinterpret_cast<const char*>(qv) + (size_t)(blockIdx.x * WPB + wave) * 4864;

    v4i buf[RING];
    auto burst = [&](const char* p, int i, unsigned o) {
#pragma unroll
        for (int e = 0; e < 3; ++e) { buf[i * 3 + e] = ldstream(p + o + e * 256); SB(); }
    };

    const unsigned phase = ((pos & (NWG / 2 - 1)) * (BODY16 / 512) + ww * WST * (BODY16 / (12 * 256))) >> 16;
    unsigned base = tb + phase + PSH;
    unsigned sched = 0, off = 0, offv = 0;
    int prev = 0, now = 0, hold = 0, big = 0, body = 0;
    unsigned vc = 0;
    auto pace = [&](int due) {
        int now;
        while ((now = (int)(clk() - base)) < due) __builtin_amdgcn_s_sleep(1);
        return now;
    };
    auto follow = [&](unsigned o2) {
        hold = o2 - off > SLIP / 2 ? HOLDB : o2 != off ? HOLD : max(hold - 1, 0);
        big = o2 - off > SLIP / 2 ? HOLDB : max(big - 1, 0);
        base += o2 - off;
        off = o2;
    };
    auto pacen = [&](auto N) {
        constexpr int n = decltype(N)::value;
        follow(__builtin_amdgcn_readfirstlane(offv));
        now = pace(slot(n));
        vc = inflight();
        if (__builtin_expect(vc > 3, 0)) {
            asm volatile("s_waitcnt vmcnt(3)");
            now = (int)(clk() - base);
            if (now > slot(n) + THR && prev < PRV && big == 0 && off + SLIP <= MAXOFF && body >= NOSLK) {
                if (lane == 0 && __hip_atomic_fetch_add(&sm.vote[body], 1u, __ATOMIC_RELAXED, __HIP_MEMORY_SCOPE_WORKGROUP) + 1 >= VOTES)
                    __hip_atomic_fetch_max(&sm.slip, off + SLIP, __ATOMIC_RELAXED, __HIP_MEMORY_SCOPE_WORKGROUP);
                unsigned o2;
                while ((o2 = __builtin_amdgcn_readfirstlane(ld_wg(&sm.slip))) == off && (int)(clk() - base) < now + VWAIT) {}
                if (o2 != off) { follow(o2); now = pace(slot(n)); }
            }
            if (hold == 0 && off + SS <= MAXOFF && body >= NOSM && lane == 0) st_wg(&sm.req, off + SS);
        }
    };
    auto check = [&](auto N) {
        constexpr int n = decltype(N)::value;
        prev = now - slot(n);
        if (lane == 0) st_wg(&sm.late[wave], (unsigned)prev);
    };
    auto getslip = [&]() { offv = __hip_atomic_load(&sm.slip, __ATOMIC_RELAXED, __HIP_MEMORY_SCOPE_WORKGROUP); };
    auto arrive = [&](int i) {
        asm volatile("" ::: "memory");
        if (lane == 0) __hip_atomic_fetch_add(&sm.cnt[i], 1u, __ATOMIC_RELAXED, __HIP_MEMORY_SCOPE_WORKGROUP);
    };
    auto next_body = [&]() {
        const unsigned s2 = sched + BODY16;
        base += (s2 >> 16) - (sched >> 16);
        sched = s2;
        ++body;
    };

    {
        v4i pv[2], pq[2];
        pq[0] = reinterpret_cast<const v4i*>(q)[tid];
        pq[1] = reinterpret_cast<const v4i*>(q)[NT + (tid & 255)];
        const unsigned vo = (group(tid >> 3, gx) * 64 + (tid & 7) * 8) * 2;
        PST(1); pace(slot(3)); SB();
        pk[3] = ldstream((const char*)kz + po[0]);
        pk[4] = ldstream((const char*)kz + po[1]);
        pk[5] = ldstream((const char*)kz + po[2]);
        const bool isk = lane < 48, isz = isk ? lane >= 24 : lane >= 56;
        const char* par = (const char*)(isk ? isz ? kz : ks : isz ? vz : vs) + (isk ? lane % 24 : lane & 7) * 16
                        + group(tau * 4 + (wave & 3), px) * (isk ? DH * 2 : 128);
        asm volatile("" : "+v"(par));
        v4i pp;
        PST(2); pace(slot(6)); SB();
        pv[0] = ldstream((const char*)vs + vo);
        pv[1] = ldstream((const char*)vz + vo);
        if (wave >= WPB - NX) pp = __builtin_nontemporal_load((const __attribute__((address_space(1))) v4i*)par);
        descv[0] = describe(0, lane, 0, 0u, 0u, 0u);
        descv[1] = describe(0, lane, 1, 0u, 0u, 0u);
        dsc[0] = __builtin_amdgcn_readfirstlane(descv[0]);
        dsc[1] = __builtin_amdgcn_readfirstlane(descv[1]);
        PST(3); pace(slot(8)); SB();
        burst((const char*)qk, 0, lanes(dsc[0]));
        PST(4);
        sm.qt[tid] = pq[0];
        if (tid < 256) sm.qt[NT + tid] = pq[1];
#pragma unroll
        for (int u = 0; u < 3; ++u) sm.ksz[dsti[u]] = pk[u];
#pragma unroll
        for (int u = 0; u < 3; ++u) {
            const v4i x = pk[3 + u];
            sm.ksz[dsti[u] + 24] = v4i{(int)negscale(x.x), (int)negscale(x.y), (int)negscale(x.z), (int)negscale(x.w)};
        }
        PST(5); pace(slot(11)); SB();
        burst((const char*)qk, 1, lanes(dsc[1]));
        PST(6);
        sm.vsz[0][tid] = pv[0];
        sm.vsz[1][tid] = v4i{(int)negscale(pv[1].x), (int)negscale(pv[1].y), (int)negscale(pv[1].z), (int)negscale(pv[1].w)};
        if (wave >= WPB - NX) {
            if (isz) pp = v4i{(int)negscale(pp.x), (int)negscale(pp.y), (int)negscale(pp.z), (int)negscale(pp.w)};
            *(isk ? &sm.ksz[(64 + (wave & 3)) * 48 + lane] : &sm.vsz[isz][(64 + (wave & 3)) * 8 + (lane & 7)]) = pp;
        }
    }
    asm volatile("" :: "v"(warm));
    PST(7);
    __syncthreads();
    PST(8);
    base = __builtin_amdgcn_readfirstlane(sm.t0) + OFF0 + phase;

    const v2f huge = {0x1p127f, 0x1p127f};

    for (int S = 0; S < NH; ++S) {
        v2f kz2[6];
        unsigned slo[6], shi[6];
        v2u qq[3];
        const char* nsup = (const char*)(S + 1 < NH ? qk : qv);
        const int head = S >> 1 == 10 ? S : (S & ~1) + win;
        float mylogit = 0.f;
        float dot = 0.f;
        sfor<12>([&](auto N) {
            constexpr int n = decltype(N)::value;
            constexpr int i = n / 3, j = n % 3;
            if (n == 6 && (S == H0 - 1 || S == NH - 1)) fetch(S == NH - 1);
            if (j == 0) {
                const unsigned d = dsc[i ^ 2] = __builtin_amdgcn_readlane(descv[i & 1], (2 * S + 1 + (i >> 1)) & 63);
                const unsigned o = lanes(d);
                base += (d >> 4) & 31u;
                pacen(N);
                SB();
                burst(i < 2 ? (const char*)qk : nsup, i ^ 2, o);
                SB();
                if (tid == NT - 1) st_wg(&sm.tick, S * 4 + i + 1);
                check(N);
            }
            if (j == 2) getslip();
            if (i % 2 == 0 || ((dsc[i] ^ dsc[i ^ 1]) & 4u)) {
                const int it = S * 2 + (i >> 1), sl = dsc[i] & 4u ? 64 + (it & 3) : it;
                const v2u* pk = reinterpret_cast<const v2u*>(&sm.ksz[sl * 48]);
                const v2u a = pk[j * 16 + r], b = pk[48 + j * 16 + r];
                slo[j * 2 + 0] = a.x & 0xffffu; shi[j * 2 + 0] = a.x & 0xffff0000u;
                slo[j * 2 + 1] = a.y & 0xffffu; shi[j * 2 + 1] = a.y & 0xffff0000u;
                kz2[j * 2 + 0] = v2f{bf_lo(b.x), bf_hi(b.x)};
                kz2[j * 2 + 1] = v2f{bf_lo(b.y), bf_hi(b.y)};
            }
            if (i == 0) qq[j] = reinterpret_cast<const v2u*>(&sm.qt[head * 24])[j * 16 + r];
            if (j == 0) dot = 0.f;
            {
                const v4i x = buf[n % RING];
                const v2f t0 = __builtin_elementwise_fma(v2f{__int_as_float(x.x), __int_as_float(x.y)}, huge, kz2[j * 2]);
                const v2f t1 = __builtin_elementwise_fma(v2f{__int_as_float(x.z), __int_as_float(x.w)}, huge, kz2[j * 2 + 1]);
                float y[4];
                mul4(y, pack(t0.x, t0.y), pack(t1.x, t1.y), slo[j * 2], shi[j * 2], slo[j * 2 + 1], shi[j * 2 + 1]);
                dot = dot2(pack(y[0], y[1]), qq[j].x, dot);
                dot = dot2(pack(y[2], y[3]), qq[j].y, dot);
            }
            if (j == 2) {
                const float d = rowsum16(dot);
                if ((r & 3) == (i ^ ((dsc[i] >> 1) & 1))) mylogit = d;
            }
            asm volatile("" : "+v"(dot), "+v"(mylogit));
            SB();
        });
        if (r < 4) sm.p[S][wave][g * 4 + r] = (unsigned short)pack(rb(mylogit * 0x1p22f) * scale, 0.f);
        if ((S & 3) == 3) arrive(NH + (S >> 2));
        if (S == NH - 3 && tid == 0) st_wg(&sm.done, 1u);
        next_body();
    }

    for (int S = 0; S < NH; ++S) {
        v2u P;
        unsigned vsl, vsh, vzf;
        v4f acc = {0.f, 0.f, 0.f, 0.f};
        float y0[12];
        unsigned b0[12];
        const bool more = S + 1 < NH;
        unsigned sl, sh;
        v2f z2;
        sfor<12>([&](auto N) {
            constexpr int n = decltype(N)::value;
            constexpr int i = n / 3, j = n % 3;
            if (j == 0) {
                const unsigned d = dsc[i ^ 2] = __builtin_amdgcn_readlane(descv[i & 1], (2 * S + 1 + (i >> 1)) & 63);
                unsigned o = lanes(d);
                const char* src = (const char*)qv;
                if (i >= 2 && !more) { o = i * 768; src = dummy; }
                if (i < 2 || more) base += (d >> 4) & 31u;
                if (i < 2 || more) pacen(N);
                SB();
                burst(src, i ^ 2, o);
                SB();
                check(N);
            }
            if (n == 0) {
                const unsigned need = S < 2 ? 1 : S;
                while (__builtin_amdgcn_readfirstlane(ld_wg(&sm.prog)) < need) {}
                asm volatile("" ::: "memory");
                P = *reinterpret_cast<const v2u*>(&sm.p[S][wave][g * 4]);
                P.x = __builtin_amdgcn_alignbit(P.x, P.x, (dsc[0] & 2u) << 3);
                P.y = __builtin_amdgcn_alignbit(P.y, P.y, (dsc[2] & 2u) << 3);
                const unsigned d3 = __builtin_amdgcn_readlane(descv[1], 2 * S + 1);
                const unsigned x = (((dsc[0] & 7u) | (dsc[1] & 7u) << 8) | ((dsc[2] & 7u) << 16 | (d3 & 7u) << 24)) >> (r & 3) * 8;
                const int it = S * 2 + ((r >> 1) & 1);
                const int ltok = (x & 4u ? 64 + (it & 3) : it) * 64 + (x & 1u ? tod : tev);
                vsl = reinterpret_cast<const unsigned short*>(&sm.vsz[0][0])[ltok];
                vsh = vsl << 16;
                vzf = (unsigned)reinterpret_cast<const unsigned short*>(&sm.vsz[1][0])[ltok] << 16;
            }
            if (j == 2) getslip();
            if (j == 0) {
                sl = dppu<0x55 * i>(vsl);
                sh = dppu<0x55 * i>(vsh);
                const float zb = __uint_as_float(dppu<0x55 * i>(vzf));
                z2 = v2f{zb, zb};
            }
            float y[4];
            {
                const v4i x = buf[n];
                const v2f t0 = __builtin_elementwise_fma(v2f{__int_as_float(x.x), __int_as_float(x.y)}, huge, z2);
                const v2f t1 = __builtin_elementwise_fma(v2f{__int_as_float(x.z), __int_as_float(x.w)}, huge, z2);
                mul4(y, pack(t0.x, t0.y), pack(t1.x, t1.y), sl, sh, sl, sh);
            }
#pragma unroll
            for (int e = 0; e < 4; ++e) {
                const int idx = j * 4 + e;
                if (i % 2 == 0) {
                    y0[idx] = y[e];
                    asm volatile("" : "+v"(y0[idx]));
                } else if (i == 1) {
                    b0[idx] = pack(y0[idx], y[e]);
                    asm volatile("" : "+v"(b0[idx]));
                } else {
                    const v2u B = {b0[idx], pack(y0[idx], y[e])};
                    const v2u A = {r == idx ? P.x : 0u, r == idx ? P.y : 0u};
                    acc = __builtin_amdgcn_mfma_f32_16x16x16bf16_1k(__builtin_bit_cast(v4s, A), __builtin_bit_cast(v4s, B), acc, 0, 0, 0);
                }
            }
            if (n == 11) {
                if (S == NH - 2 && tid == 0) sm.tref = base + slot(12) + slot(3);
                if (S == NH - 3 && tid == 0) st_wg(&sm.done, 2u);
                if (lane < 48) *reinterpret_cast<v4f*>(&sm.stage[S & 1][wave][lane * 4]) = acc;
                arrive(S);
            }
            SB();
        });
        next_body();
    }
    {
        const int mh = (blockIdx.x / 16 * 2 + (blockIdx.x & 7) / 4 + 20) % NH, msl = (blockIdx.x & 3) * 2 + ((blockIdx.x >> 3) & 1);
        const bool both = mh >> 1 == 10;
        const unsigned tag = tagof(sm.epoch, lane & 3);
        const float* src = ws + ((size_t)((both ? 0 : mh & 1) * (NWG / 2) + wave * 16) * NRC + (mh >> 1) + (mh > 20)) * RS + msl * 16;
        const unsigned so = (lane >> 2) * (NRC * RS * 4) + (lane & 3) * 16;
        until(base - slot(12) + slot(3) - phase + wave * STG + (mh >= NH - 2 ? TPL : TPC));
        v4i x[2];
        for (;;) {
            x[0] = ld_rec(src, so);
            x[1] = v4i{0, 0, 0, (int)tag};
            if (both) x[1] = ld_rec(src + NWG / 2 * NRC * RS, so);
            asm volatile("s_waitcnt vmcnt(0)" : "+v"(x[0]), "+v"(x[1]));
            if (__all((((unsigned)x[0].w ^ tag) | ((unsigned)x[1].w ^ tag)) < 256u) || clk() - tb >= TMO) break;
            __builtin_amdgcn_s_sleep(PSL);
        }
#pragma unroll
        for (int d = 0; d < 6; ++d) asm volatile("" :: "v"(buf[d]));
        unsigned ml[2];
#pragma unroll
        for (int k = 0; k < 2; ++k) {
            ml[k] = ((unsigned)x[k].w ^ tag) << (lane & 3) * 8;
            ml[k] |= dppu<0xB1>(ml[k]);
            ml[k] |= dppu<0x4E>(ml[k]);
        }
        if (!both) ml[1] = ml[0] & 0xffff0000u;
        const float M = x4max(fmaxf(bf_hi(ml[0]), bf_hi(ml[1])));
        const float w0 = __expf(bf_hi(ml[0]) - M), w1 = __expf(bf_hi(ml[1]) - M);
        float a[6];
#pragma unroll
        for (int e = 0; e < 3; ++e) {
            a[2 * e] = bf_lo(x[0][e]) * w0 + bf_lo(x[1][e]) * w1;
            a[2 * e + 1] = bf_hi(x[0][e]) * w0 + bf_hi(x[1][e]) * w1;
        }
        const float l = (float)__builtin_bit_cast(_Float16, (unsigned short)ml[0]) * w0 + (float)__builtin_bit_cast(_Float16, (unsigned short)ml[1]) * w1;
        float c0 = fold16(fold32(a[0], a[4]), fold32(a[2], l));
        float c1 = fold16(fold32(a[1], a[5]), fold32(a[3], l));
        c0 += dpp<0x124>(c0); c1 += dpp<0x124>(c1);
        c0 += dpp<0x128>(c0); c1 += dpp<0x128>(c1);
        if (g == 3) c1 = M;
        if (r < 4) *reinterpret_cast<v2f*>(&sm.fin[wave][g == 3 ? 24 + r * 2 : r * 6 + g * 2]) = v2f{c0, c1};
        asm volatile("" ::: "memory");
        unsigned arr = 0;
        if (lane == 0) arr = __hip_atomic_fetch_add(&sm.fcnt, 1u, __ATOMIC_RELAXED, __HIP_MEMORY_SCOPE_WORKGROUP);
        asm volatile("" ::: "memory");
        const v4f y = *reinterpret_cast<const v4f*>(&sm.fin[lane >> 3][(lane & 7) * 4]);
        const float mw = sm.fin[lane >> 3][25];
        const float w = __expf(mw - x4max(mw));
        float d = fold16(fold32(y.x * w, y.z * w), fold32(y.y * w, y.w * w));
        d += dpp<0x128>(d);
        const float L = __int_as_float(__builtin_amdgcn_readlane(__float_as_int(d), 6));
        const unsigned o = pack(d / L, 0.f);
        asm volatile("" :: "v"(o));
        if (__builtin_amdgcn_readfirstlane(arr) == WPB - 1) {
            if (r < 6) reinterpret_cast<unsigned short*>(out)[mh * DH + msl * SLC + r * 4 + g] = (unsigned short)o;
        }
    }
}
__global__ void kivi_end_marker() { asm volatile("kivi_code_end:"); }

extern "C" void kernel_launch(void* const* d_in, const int* in_sizes, int n_in,
                              void* d_out, int out_size, void* d_ws, size_t ws_size,
                              hipStream_t stream) {
    const __hip_bfloat16* q = (const __hip_bfloat16*)d_in[0];
    const int* qk = (const int*)d_in[1];
    const int* qv = (const int*)d_in[2];
    const __hip_bfloat16* ks = (const __hip_bfloat16*)d_in[3];
    const __hip_bfloat16* kz = (const __hip_bfloat16*)d_in[4];
    const __hip_bfloat16* vs = (const __hip_bfloat16*)d_in[5];
    const __hip_bfloat16* vz = (const __hip_bfloat16*)d_in[6];
    const float scale = (float)(1.0 / sqrt((double)DH));
    kivi_stream_kernel<<<NWG, NT + 128, 0, stream>>>(q, qk, qv, ks, kz, vs, vz, (float*)d_ws, (__hip_bfloat16*)d_out, scale);
}
```

```cpp
#define DIAG 6
#include <hip/hip_runtime.h>
#include <hip/hip_bf16.h>
#include <stdint.h>
#include <utility>

#define TGT 199.25
#define WST 16
#define THR 80
#define PRV 16
#define SLIP 250
#define MAXOFF 3000
#define NSENT 8
#define NR 32
#define TPL 260
#define STG 0
#define PSLEEP 32
#define OFF0 380
#define TGATH 150
#define GAP1 10
#define TREL 90
#define NSLOW 1
#define EXK 6
#define NOSLK 2
#define TMO 100000000u
#define TPC 260
#define PSL 2
#define DST 22
#define BA 0
#define BB 16
#define TGMAX 300
#define PSH 0
#define SS 20
#define HOLD 4
#define VOTES 4
#define HOLDB 8
#define REVT 600
#define VWAIT 30
#define LFAC 8
#define NOSM 2
#define H0 4
#define TGS 202.25
#define SPL 2
#define G21 5
#define EXS(i) ((int)(((i) + 1) * (TGS - TGT) * 100.0 / 256.0 + 0.5) - (int)((i) * (TGS - TGT) * 100.0 / 256.0 + 0.5))
#define CAL0 128
#define NRND 4
#define PTH 1000
#define TPRB 200
#define VTCH 1
#define GAP2 10
#define NG2 3
#define TECHO 550
#define TQ 300
#define TTAB 1100
#define TXW 200
#define FMASK 0xffffffffu
#define FPOL 0u
#define RING 12
#define SB() __builtin_amdgcn_sched_barrier(0)
#define PST(i) SB()
#undef PST
#define PST(i) do { SB(); if (DST == i + 1) d_x = clk() - base; SB(); } while (0)

typedef int v4i __attribute__((ext_vector_type(4)));
typedef unsigned v2u __attribute__((ext_vector_type(2)));
typedef float v2f __attribute__((ext_vector_type(2)));
typedef float v4f __attribute__((ext_vector_type(4)));
typedef short v4s __attribute__((ext_vector_type(4)));
typedef __bf16 v2bf __attribute__((ext_vector_type(2)));

constexpr int DH = 192;
constexpr int NH = 32;
constexpr int SKV = 32768;
constexpr int WPB = 8;
constexpr int NT = WPB * 64;
constexpr int NWG = 256;
constexpr int NSL = 8, SLC = 24;
constexpr int RS = NSL * 16;
static_assert(SKV == 2 * 64 * NWG, "two 64-token groups per workgroup and head");
constexpr int NX = 4;
constexpr int NRC = 17;
constexpr size_t WS_X = (size_t)NRC * NWG * RS;
constexpr size_t WS_T = WS_X + (size_t)NWG * 256;
constexpr unsigned BODY16 = (unsigned)(TGT * 100.0 / 64.0 * 65536.0);
constexpr int slot(int n) { return (int)((n * (BODY16 / 12)) >> 16); }

template <typename F, int... I>
__device__ __forceinline__ void sfor_impl(F&& f, std::integer_sequence<int, I...>) {
    (f(std::integral_constant<int, I>{}), ...);
}
template <int N, typename F>
__device__ __forceinline__ void sfor(F&& f) {
    sfor_impl(f, std::make_integer_sequence<int, N>{});
}

__device__ __forceinline__ float bf_lo(unsigned u) { return __uint_as_float(u << 16); }
__device__ __forceinline__ float bf_hi(unsigned u) { return __uint_as_float(u & 0xffff0000u); }
__device__ __forceinline__ float rb(float x) { return (float)(__bf16)x; }
__device__ __forceinline__ unsigned pack(float lo, float hi) {
    return __builtin_bit_cast(unsigned, __builtin_convertvector(v2f{lo, hi}, v2bf));
}
__device__ __forceinline__ float dot2(unsigned a, unsigned b, float c) {
    return __builtin_amdgcn_fdot2_f32_bf16(__builtin_bit_cast(v2bf, a), __builtin_bit_cast(v2bf, b), c, false);
}
__device__ __forceinline__ void mul4(float (&y)[4], unsigned a, unsigned b, unsigned s0, unsigned s1, unsigned s2, unsigned s3) {
    asm("v_dot2_f32_bf16 %0, %4, %6, 0\n\tv_dot2_f32_bf16 %1, %4, %7, 0\n\t"
        "v_dot2_f32_bf16 %2, %5, %8, 0\n\tv_dot2_f32_bf16 %3, %5, %9, 0\n\ts_nop 2"
        : "=&v"(y[0]), "=&v"(y[1]), "=&v"(y[2]), "=&v"(y[3])
        : "v"(a), "v"(b), "v"(s0), "v"(s1), "v"(s2), "v"(s3));
}
__device__ __forceinline__ unsigned clk() { return (unsigned)__builtin_amdgcn_s_memrealtime(); }
__device__ __forceinline__ unsigned inflight() {
    unsigned x;
    asm volatile("s_getreg_b32 %0, hwreg(HW_REG_IB_STS, 0, 4)" : "=s"(x));
    return x;
}
__device__ __forceinline__ unsigned negscale(unsigned u) { return pack(bf_lo(u) * -0x1p-22f, bf_hi(u) * -0x1p-22f); }

template <int CTRL>
__device__ __forceinline__ unsigned dppu(unsigned x) {
    return (unsigned)__builtin_amdgcn_mov_dpp((int)x, CTRL, 0xf, 0xf, true);
}
template <int CTRL>
__device__ __forceinline__ float dpp(float x) { return __uint_as_float(dppu<CTRL>(__float_as_uint(x))); }
__device__ __forceinline__ float rowsum16(float x) {
    x += dpp<0x128>(x);
    x += dpp<0x124>(x);
    x += dpp<0x122>(x);
    x += dpp<0x121>(x);
    return x;
}
__device__ __forceinline__ v4i ldstream(const void* p) {
    return __builtin_nontemporal_load(reinterpret_cast<const v4i*>(p));
}

__device__ __forceinline__ void st_rec(void* p, unsigned o, v4i a) {
    asm volatile("s_nop 4\n\tglobal_store_dwordx4 %0, %1, %2 sc1\n\ts_nop 1" :: "v"(o), "v"(a), "s"(p) : "memory");
}
__device__ __forceinline__ v4i ld_rec(const void* p, unsigned o) {
    v4i x;
    asm volatile("s_nop 4\n\tglobal_load_dwordx4 %0, %1, %2 sc1" : "=v"(x) : "v"(o), "s"(p) : "memory");
    return x;
}
typedef unsigned long long u64;
__device__ __forceinline__ u64 ld_dev(const u64* p) { return __hip_atomic_load(p, __ATOMIC_RELAXED, __HIP_MEMORY_SCOPE_AGENT); }
__device__ __forceinline__ void st_dev(u64* p, u64 v) { __hip_atomic_store(p, v, __ATOMIC_RELAXED, __HIP_MEMORY_SCOPE_AGENT); }
__device__ __forceinline__ unsigned ld_wg(const unsigned* p) { return __hip_atomic_load(p, __ATOMIC_RELAXED, __HIP_MEMORY_SCOPE_WORKGROUP); }
__device__ __forceinline__ void st_wg(unsigned* p, unsigned v) { __hip_atomic_store(p, v, __ATOMIC_RELAXED, __HIP_MEMORY_SCOPE_WORKGROUP); }
__device__ __forceinline__ float fold32(float a, float b) {
    auto s = __builtin_amdgcn_permlane32_swap(__float_as_uint(a), __float_as_uint(b), false, false);
    return __uint_as_float(s[0]) + __uint_as_float(s[1]);
}
__device__ __forceinline__ float fold16(float a, float b) {
    auto s = __builtin_amdgcn_permlane16_swap(__float_as_uint(a), __float_as_uint(b), false, false);
    return __uint_as_float(s[0]) + __uint_as_float(s[1]);
}
__device__ __forceinline__ float x4max(float x) {
    x = fmaxf(x, dpp<0x124>(x));
    x = fmaxf(x, dpp<0x128>(x));
    auto s = __builtin_amdgcn_permlane16_swap(__float_as_uint(x), __float_as_uint(x), false, false);
    x = fmaxf(__uint_as_float(s[0]), __uint_as_float(s[1]));
    auto t = __builtin_amdgcn_permlane32_swap(__float_as_uint(x), __float_as_uint(x), false, false);
    return fmaxf(__uint_as_float(t[0]), __uint_as_float(t[1]));
}

struct __attribute__((aligned(16))) Smem {
    v4i ksz[(64 + NX) * 48];
    v4i qt[NH * 24];
    v4i vsz[2][(64 + NX) * 8];
    v4i rec[NRC - 1][RS / 4];
    float stage[2][WPB][DH];
    unsigned short p[NH][WPB][16];
    unsigned ml[NH];
    float fin[WPB][32];
    u64 epoch;
    unsigned desc[2][2][2 * NH];
    unsigned ready;
    unsigned have;
    unsigned tref;
    unsigned slip;
    unsigned vote[2 * NH];
    unsigned late[WPB];
    unsigned req;
    unsigned t0;
    unsigned done;
    unsigned prog;
    unsigned fcnt;
    unsigned dsv;
    unsigned tick;
    unsigned quiet;
    unsigned cnt[NH + NH / 4];
};

__global__ __launch_bounds__(NT + 128, 3) void kivi_stream_kernel(
    const __hip_bfloat16* __restrict__ q, const int* __restrict__ qk, const int* __restrict__ qv,
    const __hip_bfloat16* __restrict__ ks, const __hip_bfloat16* __restrict__ kz,
    const __hip_bfloat16* __restrict__ vs, const __hip_bfloat16* __restrict__ vz,
    float* __restrict__ ws, __hip_bfloat16* __restrict__ out, float scale) {
    __shared__ Smem sm;

    unsigned ka0, ka1;
    asm volatile("s_load_dword %0, %2, 0x0\n\ts_load_dword %1, %2, 0x40" : "=&s"(ka0), "=&s"(ka1) : "s"(__builtin_amdgcn_kernarg_segment_ptr()));
    const u64 tb64 = __builtin_amdgcn_s_memrealtime();
    const unsigned tb = (unsigned)tb64;
    const int tid = threadIdx.x;
    typedef const __attribute__((address_space(1))) int* gptr;
    u64 code;
    int codelen;
    asm volatile("s_getpc_b64 %0\nkivi_code_begin:\n\ts_mov_b32 %1, kivi_code_end-kivi_code_begin" : "=s"(code), "=s"(codelen));
    const int warm = *(gptr)(code + min(tid * 128, codelen - 4));
    const int lane = tid & 63;
    const int wave = __builtin_amdgcn_readfirstlane(tid >> 6);
    const int r = lane & 15;
    const int g = lane >> 4;
    const int pos = (blockIdx.x & ~7) | ((blockIdx.x & 3) << 1) | ((blockIdx.x >> 2) & 1);
    auto groupof = [](int p) {
        const int r0 = p < 85 ? 0 : p < 171 ? 85 : 171, na = p < 171 ? 43 : 42, t = p - r0;
        return r0 + (t < 64 ? ((t >> 4) & 1) * na + (t >> 5) * 16 + (t & 15) : t < na + 32 ? t - 32 : t);
    };
    const int win = pos >> 7;
    const int gx = groupof(pos & 127) | groupof((pos & 127) + 128) << 8;
    auto group = [&](int it, int x) {
        const bool both = it >> 2 == 10;
        return (both ? it : (it & ~3) + 2 * win + ((it >> 1) & 1)) * NWG + ((x >> ((both ? win : it) & 1) * 8) & 255);
    };
    unsigned po[3];
    int dsti[3];
#pragma unroll
    for (int u = 0; u < 3; ++u) {
        const int f = u * NT + tid;
        const int gi = f / 24, pc = f % 24;
        dsti[u] = gi * 48 + pc;
        po[u] = group(gi, gx) * (DH * 2) + pc * 16;
        asm volatile("" : "+v"(po[u]));
    }
    SB();
    v4i pk[6];
    if (wave < WPB) {
        pk[0] = ldstream((const char*)ks + po[0]);
        pk[1] = ldstream((const char*)ks + po[1]);
        pk[2] = ldstream((const char*)ks + po[2]);
    }
    asm volatile("s_waitcnt lgkmcnt(0)" :: "s"(ka0), "s"(ka1));
    SB();
    u64* tstart = reinterpret_cast<u64*>(ws + WS_T);
    auto tagof = [](u64 e, int j) {
        e *= 0x9e3779b97f4a7c15ull;
        return (unsigned)(j == 3 ? (e ^ (e >> 29)) >> 8 : e >> (j * 20)) << 8;
    };
    auto until = [&](unsigned t) { while ((int)(clk() - t) < 0) __builtin_amdgcn_s_sleep(1); };
    const int px = groupof((pos ^ 1) & 127) | groupof(((pos ^ 1) & 127) + 128) << 8;
    const int quart = (pos >> 5) & 3, tau = (-((pos >> 1) + quart)) & 15;
    auto shared = [&](int b) { return SPL == 4 || ((b ^ quart) & 1) == 0; };
    auto job = [&](int t, int b, unsigned pol, unsigned ok, unsigned side) {
        const int bd = tau == 10 || (t == 0 && shared(b)) ? b : 2 * tau + (SPL == 4 ? 0 : quart & 1);
        const int reg = (shared(b) ? t : 1) * 12 + (group(2 * bd, pos & 1 ? px : gx) * 3 >> 12);
        return (((ok >> reg) & 1u && (t || b >= H0) ? side ^ (pol >> reg) : quart >> 1) ^ pos) & 1u;
    };
    auto describe = [&](int t, int it, int j, unsigned pol, unsigned ok, unsigned side) {
        const int b = it >> 1, n = it * 2 + j;
        const bool tr = it >> 2 == tau && tau > 0, sh = tr && shared(b);
        const unsigned jb = job(t, tr ? b : 2 * tau, pol, ok, side);
        const unsigned x = tr ? ((sh ? j : jb) ^ pos) & 1u : 0u;
        const unsigned gr = group(it, x ? px : gx), c = 6 * gr + 3;
        const unsigned flip = sh ? 0u : (pol >> (t * 12 + (c >> 13))) & 1u;
        const bool early = !t && b < H0;
        const bool known = (ok >> (t * 12 + (it >> 2) * 3 / 4)) & (ok >> (t * 12 + ((it >> 2) * 3 + 2) / 4)) & 1u;
        const unsigned ex = (early || b < 2 || !known ? (n & 3) == 0 ? EXS(0) : (n & 3) == 1 ? EXS(1) : (n & 3) == 2 ? EXS(2) : EXS(3) : 0) + (early || known ? 0 : n & 1)
                          + (n >= 84 && n < 87 ? G21 : 0) + (t ? n == 0 ? GAP1 : n == NG2 && ((pol ^ (pol >> 12)) & 0xfffu) ? GAP2 : 0 : n >= 3 && n < 3 + 4 * NSLOW ? EXK : 0);
        return gr * (64 * DH * 4) | ex << 4 | (early || known || sh ? 0u : 8u) | x << 2 | flip << 1
             | (((sh ? jb : j ^ win ^ flip) ^ (c >> 8) ^ (c >> 15)) & 1u);
    };

    if (__builtin_expect(wave == WPB, 0)) {
        const int l = lane;
        u64* echo = tstart + NWG;
        u64* gofs = tstart + 3 * NWG;
        if (l == 0) {
            st_dev(tstart + blockIdx.x, tb64);
            if (blockIdx.x < NR) st_dev(gofs + blockIdx.x * 32, tb64 - 100000ull);
            if (blockIdx.x == NR) st_dev(gofs + 16, tb64 - 100000ull);
            sm.slip = 0;
            sm.req = 0;
            sm.done = 0;
            sm.prog = 0;
            sm.ready = 0;
            sm.tick = 0;
            sm.quiet = tb;
            sm.fcnt = 0;
            sm.epoch = tb64;
            sm.have = blockIdx.x == 0;
        }
        if (l < NH + NH / 4) sm.cnt[l] = 0;
        sm.vote[l] = 0;
        until(tb + TGATH);
        unsigned ld;
        for (;;) {
            const u64 dt = ld_dev(tstart + NWG - 8 + (l & 7)) - tb64 + 1000ull;
            ld = dt < 2000ull ? (unsigned)dt : 0u;
            if (__all(dt < 2000ull) || clk() - tb > (unsigned)TGMAX) break;
        }
        ld = max(ld, dppu<0x124>(ld));
        ld = max(ld, dppu<0x122>(ld));
        ld = max(ld, dppu<0x121>(ld));
        ld = __builtin_amdgcn_readfirstlane(ld);
        const u64 t64 = ld ? tb64 + ld - 1000ull : tb64;
        if (l == 0) sm.t0 = (unsigned)t64;
        if (l == 0) sm.dsv = clk() - tb;
        u64 to[NWG / 64];
        bool have = blockIdx.x == 0;
        auto chores = [&](bool all) {
            if (blockIdx.x == 0) {
                u64 x[NWG / 64];
#pragma unroll
                for (int u = 0; u < NWG / 64; ++u) x[u] = ld_dev(tstart + u * 64 + l);
#pragma unroll
                for (int u = 0; u < NWG / 64; ++u) {
                    if (all || __any(x[u] != to[u])) st_rec(echo + u * 128, l * 16, v4i{(int)(unsigned)x[u], (int)(unsigned)(x[u] >> 32), (int)tb, (int)(unsigned)(tb64 >> 32)});
                    to[u] = x[u];
                }
            } else if (!have && !all) {
                v4i x = ld_rec(echo + blockIdx.x * 2, 0);
                asm volatile("s_waitcnt vmcnt(0)" : "+v"(x));
                if ((unsigned)x.x == tb && (unsigned)x.y == (unsigned)(tb64 >> 32)) {
                    have = true;
                    if (l == 0) sm.epoch = (u64)(unsigned)x.w << 32 | (unsigned)x.z;
                    asm volatile("s_waitcnt lgkmcnt(0)" ::: "memory");
                    if (l == 0) st_wg(&sm.have, 1u);
                }
            }
        };
        __syncthreads();
        asm volatile("" :: "v"(warm));
        chores(true);
        until(tb + TECHO);
        const u64* mine = gofs + ((blockIdx.x >> 3) % NR) * 32;
        unsigned sent = 0, stage = 0, tsh = clk();
        while (stage < 2) {
            chores(false);
            const u64 gd = ld_dev(mine) - t64;
            const unsigned go = gd <= (u64)MAXOFF ? (unsigned)gd : 0u;
            const unsigned own = ld_wg(&sm.slip);
            if (go > own && l == 0) __hip_atomic_fetch_max(&sm.slip, go, __ATOMIC_RELAXED, __HIP_MEMORY_SCOPE_WORKGROUP);
            if (own <= go) tsh = clk();
            else if (clk() - tsh > (unsigned)REVT && blockIdx.x >= NSENT) { if (l == 0) st_wg(&sm.slip, go); }
            unsigned req = ld_wg(&sm.req);
            if ((int)(clk() - ld_wg(&sm.quiet)) < 0) { req = 0; if (l == 0) st_wg(&sm.req, 0u); }
            unsigned want = blockIdx.x < NSENT ? own : 0u;
            if (req == go + SS && own == go) {
                unsigned x = ld_wg(&sm.late[l & 7]);
                x += dppu<0x121>(x);
                x += dppu<0x122>(x);
                x += dppu<0x124>(x);
                want = min(req + (__builtin_amdgcn_readfirstlane(x) * LFAC / 64 & ~15u), (unsigned)MAXOFF);
            }
            if (want > go && want > sent) {
                u64 old = 0;
                if (l == 0) old = __hip_atomic_fetch_max(gofs + 16, t64 + want, __ATOMIC_RELAXED, __HIP_MEMORY_SCOPE_AGENT);
                if (__builtin_amdgcn_readfirstlane((unsigned)(old < t64 + want)) && l < NR)
                    __hip_atomic_fetch_max(gofs + l * 32, t64 + want, __ATOMIC_RELAXED, __HIP_MEMORY_SCOPE_AGENT);
                sent = want;
            }
            if (have) __builtin_amdgcn_s_sleep(PSLEEP);
            if (ld_wg(&sm.done) > stage) {
                ++stage;
                asm volatile("" : "+s"(code));
                int w = 0;
#pragma unroll
                for (int u = 0; u < 4; ++u) {
                    const int a = (u * 64 + l) * 128;
                    w ^= *(gptr)(code + (a < codelen ? a : 0));
                }
                asm volatile("" :: "v"(w));
            }
        }
        while (!have && clk() - tb < TMO) chores(false);
        if (l == 0) st_wg(&sm.have, 1u);
        if (blockIdx.x == 0) {
            for (int spin = 1; ld_wg(&sm.fcnt) < WPB; ++spin) {
                __builtin_amdgcn_s_sleep(2);
                if (spin % 1024 == 0) chores(false);
            }
        }
        return;
    }
    if (__builtin_expect(wave == WPB + 1, 0)) {
        u64* table = tstart + 3 * NWG + NR * 32;
        const unsigned reg = blockIdx.x - CAL0 < 64u ? (blockIdx.x - CAL0) & 31u : 31u;
        int lead = 0, nr = 0;
        const unsigned page = reg % 12 * 32 + (tb * 0x85ebca6bu >> 27);
        const char* const pp = (const char*)(reg < 12 ? qk : qv) + ((size_t)page << 21);
        auto probe = [&]() {
            const unsigned h = (tb + nr) * 0x9e3779b1u;
            const char* a = pp + (h >> 25) * 16384 + ((h >> 8) & 127) * 64;
            u64 t0, t1, t2;
            unsigned x;
            asm volatile("s_memtime %0\n\ts_waitcnt lgkmcnt(0)\n\ts_load_dword %3, %4, 0x0\n\ts_waitcnt lgkmcnt(0)\n\t"
                         "s_memtime %1\n\ts_waitcnt lgkmcnt(0)\n\ts_load_dword %3, %4, 0x2000\n\ts_waitcnt lgkmcnt(0)\n\t"
                         "s_memtime %2\n\ts_waitcnt lgkmcnt(0)"
                         : "=&s"(t0), "=&s"(t1), "=&s"(t2), "=&s"(x) : "s"(a) : "memory");
            const int d0 = (int)(t1 - t0), d1 = (int)(t2 - t1);
            if (min(d0, d1) > 450) lead += max(-600, min(600, d1 - d0));
            ++nr;
        };
        {
            const int k = blockIdx.x >> 3;
            const char* pg = k < 3 ? (const char*)kz + ((size_t)k << 21) : k == 3 ? (const char*)vs : k == 4 ? (const char*)vz
                           : k < 7 ? (const char*)ks + ((size_t)(k - 4) << 21) : (const char*)qk + ((size_t)(k - 7) << 21);
            pg += (blockIdx.x & 7) * 256;
            unsigned x, y;
            asm volatile("s_load_dword %0, %2, 0x0\n\ts_load_dword %1, %3, 0x0\n\ts_waitcnt lgkmcnt(0)"
                         : "=&s"(x), "=&s"(y) : "s"(pg), "s"(reg < 24 ? pp : pg) : "memory");
        }
#pragma nounroll
        for (int k = 0; k < 2; ++k) {
#pragma nounroll
            while (reg < 24 && nr < NRND && (k || clk() - tb < (unsigned)TPRB)) probe();
            if (k == 0) __syncthreads();
        }
        asm volatile("" :: "v"(warm));
        unsigned tick = 0;
        auto behind = [&]() {
            tick = ld_wg(&sm.tick);
            const unsigned t = clk();
            while (ld_wg(&sm.tick) == tick && clk() - t < 100u) __builtin_amdgcn_s_sleep(1);
        };
        int nt = blockIdx.x < 200;
        auto step = [&]() {
            behind();
            if (nt < 26) {
                const size_t pg = (size_t)((blockIdx.x >> 3) + 32 * (nt % 12)) << 21;
                const char* tp = nt < 12 ? (const char*)qk + pg : nt < 24 ? (const char*)qv + pg : nt == 24 ? (const char*)ws + ((size_t)((blockIdx.x >> 3) & 1) << 21) : (const char*)out;
                unsigned x;
                asm volatile("s_nop 4\n\tglobal_load_dword %0, %1, %2 nt\n\ts_waitcnt vmcnt(0)" : "=v"(x) : "v"(0), "s"(tp + (nt < 25 ? ((blockIdx.x * 29 + nt) & 255) * 256 : 0)) : "memory");
                ++nt;
            }
        };
        u64 E = 0;
        unsigned jobs = 0;
        v4i cal;
#pragma nounroll
        for (int st = 0;;) {
            if (st == 0 && (ld_wg(&sm.have) || clk() - tb >= TMO)) {
                asm volatile("" ::: "memory");
                E = sm.epoch;
                st = 1;
                if (reg < 24) {
                    unsigned xcc;
                    asm volatile("s_getreg_b32 %0, hwreg(HW_REG_XCC_ID, 0, 3)" : "=s"(xcc));
                    if (((xcc >> 2) ^ page ^ (page >> 7)) & 1) lead = -lead;
                    behind();
                    if (lane == 0) st_wg(&sm.quiet, clk() + TQ);
                    if (lane < 8) st_rec(table + (blockIdx.x - CAL0) * 32, lane * 16, v4i{lead, (int)(xcc - blockIdx.x), (int)(unsigned)E, (int)(unsigned)(E >> 32)});
                }
            }
            if (st == 1 && clk() - tb >= (unsigned)TTAB) {
                behind();
                cal = ld_rec(table, ((lane & 31) < 24 ? lane : 0) * 256);
                asm volatile("s_waitcnt vmcnt(0)" : "+v"(cal));
                if (__all((unsigned)cal.z == (unsigned)E && (unsigned)cal.w == (unsigned)(E >> 32)) || clk() - tb >= TMO) break;
            }
            if (st || reg >= 24) step();
        }
        {
            auto two = __builtin_amdgcn_permlane32_swap(cal.x, cal.x, false, false);
            const int sum = (int)(two[0] + two[1]) + (int)dppu<0xB1>(two[0] + two[1]);
            const unsigned pol = (unsigned)__ballot(sum < 0) ^ FPOL, ok = (unsigned)__ballot(abs(sum) >= PTH) & FMASK;
            const unsigned side = ((unsigned)__builtin_amdgcn_readfirstlane(cal.y) + (blockIdx.x & ~4u)) >> 2;
#pragma unroll
            for (int u = 0; u < 4; ++u) {
                sm.desc[u >> 1][u & 1][lane] = describe(u >> 1, lane, u & 1, pol, ok, side);
                jobs |= (tau > 0 ? job(u >> 1, 2 * tau + (u & 1), pol, ok, side) : pos & 1u) << u;
            }
            asm volatile("" ::: "memory");
            if (lane == 0 && DST == 21) sm.dsv = clk() - tb;
            if (lane == 0 && DST == 22) sm.dsv = (24 - __popc(ok & 0xffffffu)) * 10;
            if (lane == 0 && DST == 23) sm.dsv = ((pol & 1) + ((pol >> 8) & 1) * 2 + ((pol >> 12) & 1) * 4 + ((pol >> 20) & 1) * 8) * 100;
            if (lane == 0) st_wg(&sm.ready, 1u);
        }
#pragma nounroll
        while (nt < 26) step();
#pragma nounroll
        for (int cc = 0; cc <= NH / 4; ++cc) {
            const int c = cc < NH / 4 ? cc : tau >> 1;
            const bool act = (tau > 0 && c * 2 + (g >> 1) == tau) == (cc == NH / 4);
            while (ld_wg(&sm.cnt[NH + c]) < WPB) __builtin_amdgcn_s_sleep(2);
            asm volatile("" ::: "memory");
            if (cc >= NH / 4 - 1) {
                const bool sw = ((jobs ^ (jobs >> 2)) >> (lane >> 5)) & 1u;
                v2u* mine = reinterpret_cast<v2u*>(&sm.p[2 * tau][0][0]) + lane;
                if (cc < NH / 4) {
                    if (lane == 0) st_wg(&sm.quiet, clk() + TXW + TQ);
                    if (sw) st_rec(ws + WS_X + pos * 256, lane * 16, v4i{(int)mine->x, (int)mine->y, (int)(unsigned)E, (int)(unsigned)(E >> 32)});
                } else if (__any(sw)) {
                    until(clk() + TXW);
                }
                while (cc == NH / 4 && __any(sw)) {
                    v4i x = {};
                    if (sw) asm volatile("s_nop 4\n\tglobal_load_dwordx4 %0, %1, %2 sc1 nt" : "+v"(x) : "v"(lane * 16), "s"(ws + WS_X + (pos ^ 1) * 256) : "memory");
                    asm volatile("s_waitcnt vmcnt(0)" : "+v"(x));
                    if (__all(!sw || ((unsigned)x.z == (unsigned)E && (unsigned)x.w == (unsigned)(E >> 32))) || clk() - tb >= TMO) {
                        if (sw) *mine = v2u{(unsigned)x.x, (unsigned)x.y};
                        break;
                    }
                    __builtin_amdgcn_s_sleep(8);
                }
                asm volatile("" ::: "memory");
            }
            const bool two = c * 2 + (g >> 1) != 10;
            v4i* pp = reinterpret_cast<v4i*>(&sm.p[c * 4][0][0]) + lane;
            const v4i u = *pp;
            float x[8];
#pragma unroll
            for (int e = 0; e < 4; ++e) { x[2 * e] = bf_lo(u[e]); x[2 * e + 1] = bf_hi(u[e]); }
            float M = fmaxf(fmaxf(fmaxf(x[0], x[1]), fmaxf(x[2], x[3])), fmaxf(fmaxf(x[4], x[5]), fmaxf(x[6], x[7])));
            M = fmaxf(M, dpp<0x128>(M));
            M = fmaxf(M, dpp<0x124>(M));
            M = fmaxf(M, dpp<0x122>(M));
            M = fmaxf(M, dpp<0x121>(M));
            {
                auto y = __builtin_amdgcn_permlane16_swap(__float_as_uint(M), __float_as_uint(M), false, false);
                if (two) M = fmaxf(__uint_as_float(y[0]), __uint_as_float(y[1]));
            }
            v4i o;
            float L = 0.f;
#pragma unroll
            for (int e = 0; e < 4; ++e) {
                const unsigned pk = pack(__expf(x[2 * e] - M), __expf(x[2 * e + 1] - M));
                o[e] = (int)pk;
                L += bf_lo(pk) + bf_hi(pk);
            }
            if (act) *pp = o;
            L = rowsum16(L);
            const float L2 = fold16(L, L);
            if (two) L = L2;
            if (r == 0 && act) sm.ml[c * 4 + g] = __float_as_uint(M) | __builtin_bit_cast(unsigned short, (_Float16)L);
            asm volatile("" ::: "memory");
            if (lane == 0) st_wg(&sm.prog, 1u);
        }
        const unsigned tag = tagof(E, lane & 3);
        auto place = [&](int i) {
            const int i0 = tau + (tau > 10);
            const unsigned jb = i == i0 ? jobs >> 2 : i == 11 && tau == 10 ? jobs >> 3 : pos;
            return (((pos & ~1) | (jb & 1)) * NRC + i) * (RS * 4) + (lane & 31) * 16;
        };
        const unsigned last = place(NRC - 1);
        v2f keep[3] = {};
#pragma nounroll
        for (int h = 0; h < NH; ++h) {
            const unsigned mlb = (sm.ml[h] >> (lane & 3) * 8) & 0xffu;
            if (h < NH - 1) { while (ld_wg(&sm.cnt[h]) < WPB) __builtin_amdgcn_s_sleep(4); }
            else { while (ld_wg(&sm.cnt[h]) < WPB) {} }
            asm volatile("" ::: "memory");
            v2f c[4][3];
#pragma unroll
            for (int e = 0; e < 4; ++e)
#pragma unroll
                for (int k = 0; k < 3; ++k) c[e][k] = *reinterpret_cast<const v2f*>(&sm.stage[h & 1][(lane >> 5) * 4 + e][(lane & 31) * 6 + k * 2]);
            v4i x;
            const bool first = (h & 1) == 0 && h != 20;
#pragma unroll
            for (int k = 0; k < 3; ++k) {
                const v2f a = (keep[k] + (c[0][k] + c[1][k])) + (c[2][k] + c[3][k]), b = a * 0x1p22f;
                x[k] = (int)pack(fold32(b.x, b.x), fold32(b.y, b.y));
                keep[k] = first ? a : v2f{0.f, 0.f};
            }
            x[3] = (int)(tag | mlb);
            if (h == NH - 1) {
                if (lane < 32) st_rec(ws, last, x);
                break;
            }
            if (!first) sm.rec[(h >> 1) + (h > 20)][lane & 31] = x;
            asm volatile("" ::: "memory");
            if (lane == 0) st_wg(&sm.prog, h + 2);
            if (h == NH - 2) {
                v4i y[NRC / 2];
                unsigned o[NRC / 2];
#pragma unroll
                for (int u = 0; u < NRC / 2; ++u) { y[u] = (&sm.rec[0][0])[u * 64 + lane]; o[u] = place(u * 2 + (lane >> 5)); }
                until(ld_wg(&sm.tref) + TREL);
#pragma unroll
                for (int u = 0; u < NRC / 2; ++u) st_rec(ws, o[u], y[u]);
            }
        }
        return;
    }

    const int ww = (wave & 3) * 2 + (wave >> 2);
    const int ti = ww * 4 + g;
    const int tev = ti + (ti >= 11 ? 10 : 0) + (ti >= 22 ? 11 : 0), tod = ti + 11 + (ti >= 10 ? 11 : 0) + (ti >= 21 ? 10 : 0);
    const unsigned offe = tev * (DH * 4) + r * 16, offo = tod * (DH * 4) + r * 16;
    unsigned descv[2], dsc[4];
    auto lanes = [=](unsigned d) { return (d & 1u ? offo : offe) + (d & ~0x3fffu); };
    auto fetch = [&](int t) {
        while (__builtin_amdgcn_readfirstlane(ld_wg(&sm.ready)) == 0) __builtin_amdgcn_s_sleep(1);
        asm volatile("" ::: "memory");
#pragma unroll
        for (int j = 0; j < 2; ++j) {
            descv[j] = sm.desc[t][j][lane];
            if (ww >= WPB / 2 && (descv[j] & 8u)) descv[j] ^= 3u;
        }
    };
    const char* dummy = reinterpret_cast<const char*>(qv) + (size_t)(blockIdx.x * WPB + wave) * 4864;

    v4i buf[RING];
    auto burst = [&](const char* p, int i, unsigned o) {
#pragma unroll
        for (int e = 0; e < 3; ++e) { buf[i * 3 + e] = ldstream(p + o + e * 256); SB(); }
    };

    const unsigned phase = ((pos & (NWG / 2 - 1)) * (BODY16 / 512) + ww * WST * (BODY16 / (12 * 256))) >> 16;
    unsigned base = tb + phase + PSH;
    unsigned sched = 0, off = 0, offv = 0;
    int prev = 0, now = 0, hold = 0, big = 0, body = 0;
    unsigned vc = 0;
    int d_l = 0, d_n = 0, d_f = 0, d_s = 0, d_nb = 0;
    unsigned d_bar = 0, d_x = 0;
    auto pace = [&](int due) {
        int now;
        while ((now = (int)(clk() - base)) < due) __builtin_amdgcn_s_sleep(1);
        return now;
    };
    auto follow = [&](unsigned o2) {
        hold = o2 - off > SLIP / 2 ? HOLDB : o2 != off ? HOLD : max(hold - 1, 0);
        big = o2 - off > SLIP / 2 ? HOLDB : max(big - 1, 0);
        if ((int)(o2 - off) >= SLIP) ++d_nb;
        base += o2 - off;
        off = o2;
    };
    auto pacen = [&](auto N) {
        constexpr int n = decltype(N)::value;
        follow(__builtin_amdgcn_readfirstlane(offv));
        now = pace(slot(n));
        vc = inflight();
        if (__builtin_expect(vc > 3, 0)) {
            asm volatile("s_waitcnt vmcnt(3)");
            now = (int)(clk() - base);
            if (now > slot(n) + THR && prev < PRV && big == 0 && off + SLIP <= MAXOFF && body >= NOSLK) {
                if (lane == 0 && __hip_atomic_fetch_add(&sm.vote[body], 1u, __ATOMIC_RELAXED, __HIP_MEMORY_SCOPE_WORKGROUP) + 1 >= VOTES)
                    __hip_atomic_fetch_max(&sm.slip, off + SLIP, __ATOMIC_RELAXED, __HIP_MEMORY_SCOPE_WORKGROUP);
                unsigned o2;
                while ((o2 = __builtin_amdgcn_readfirstlane(ld_wg(&sm.slip))) == off && (int)(clk() - base) < now + VWAIT) {}
                if (o2 != off) { follow(o2); now = pace(slot(n)); }
            }
            if (hold == 0 && off + SS <= MAXOFF && body >= NOSM && lane == 0) st_wg(&sm.req, off + SS);
        }
        if (d_n >= BA && d_n < BB) d_l = max(d_l, now - slot(n));
        if (now - slot(n) > 30 && d_f == 0) d_f = d_n + 1;
        if (off > 0 && d_s == 0) d_s = d_n + 1;
        ++d_n;
    };
    auto check = [&](auto N) {
        constexpr int n = decltype(N)::value;
        prev = now - slot(n);
        if (lane == 0) st_wg(&sm.late[wave], (unsigned)prev);
    };
    auto getslip = [&]() { offv = __hip_atomic_load(&sm.slip, __ATOMIC_RELAXED, __HIP_MEMORY_SCOPE_WORKGROUP); };
    auto arrive = [&](int i) {
        asm volatile("" ::: "memory");
        if (lane == 0) __hip_atomic_fetch_add(&sm.cnt[i], 1u, __ATOMIC_RELAXED, __HIP_MEMORY_SCOPE_WORKGROUP);
    };
    auto next_body = [&]() {
        const unsigned s2 = sched + BODY16;
        base += (s2 >> 16) - (sched >> 16);
        sched = s2;
        ++body;
        if (DIAG == 14 && body == 10) d_bar = base - (sched >> 16) - d_nb * SLIP;
    };

    {
        v4i pv[2], pq[2];
        pq[0] = reinterpret_cast<const v4i*>(q)[tid];
        pq[1] = reinterpret_cast<const v4i*>(q)[NT + (tid & 255)];
        const unsigned vo = (group(tid >> 3, gx) * 64 + (tid & 7) * 8) * 2;
        PST(1); pace(slot(3)); SB();
        pk[3] = ldstream((const char*)kz + po[0]);
        pk[4] = ldstream((const char*)kz + po[1]);
        pk[5] = ldstream((const char*)kz + po[2]);
        const bool isk = lane < 48, isz = isk ? lane >= 24 : lane >= 56;
        const char* par = (const char*)(isk ? isz ? kz : ks : isz ? vz : vs) + (isk ? lane % 24 : lane & 7) * 16
                        + group(tau * 4 + (wave & 3), px) * (isk ? DH * 2 : 128);
        asm volatile("" : "+v"(par));
        v4i pp;
        PST(2); pace(slot(6)); SB();
        pv[0] = ldstream((const char*)vs + vo);
        pv[1] = ldstream((const char*)vz + vo);
        if (wave >= WPB - NX) pp = __builtin_nontemporal_load((const __attribute__((address_space(1))) v4i*)par);
        descv[0] = describe(0, lane, 0, 0u, 0u, 0u);
        descv[1] = describe(0, lane, 1, 0u, 0u, 0u);
        dsc[0] = __builtin_amdgcn_readfirstlane(descv[0]);
        dsc[1] = __builtin_amdgcn_readfirstlane(descv[1]);
        PST(3); pace(slot(8)); SB();
        burst((const char*)qk, 0, lanes(dsc[0]));
        PST(4);
        sm.qt[tid] = pq[0];
        if (tid < 256) sm.qt[NT + tid] = pq[1];
#pragma unroll
        for (int u = 0; u < 3; ++u) sm.ksz[dsti[u]] = pk[u];
#pragma unroll
        for (int u = 0; u < 3; ++u) {
            const v4i x = pk[3 + u];
            sm.ksz[dsti[u] + 24] = v4i{(int)negscale(x.x), (int)negscale(x.y), (int)negscale(x.z), (int)negscale(x.w)};
        }
        PST(5); pace(slot(11)); SB();
        burst((const char*)qk, 1, lanes(dsc[1]));
        PST(6);
        sm.vsz[0][tid] = pv[0];
        sm.vsz[1][tid] = v4i{(int)negscale(pv[1].x), (int)negscale(pv[1].y), (int)negscale(pv[1].z), (int)negscale(pv[1].w)};
        if (wave >= WPB - NX) {
            if (isz) pp = v4i{(int)negscale(pp.x), (int)negscale(pp.y), (int)negscale(pp.z), (int)negscale(pp.w)};
            *(isk ? &sm.ksz[(64 + (wave & 3)) * 48 + lane] : &sm.vsz[isz][(64 + (wave & 3)) * 8 + (lane & 7)]) = pp;
        }
    }
    asm volatile("" :: "v"(warm));
    PST(7);
    __syncthreads();
    PST(8);
    if (DST == 20) d_x = sm.dsv;
    d_bar = clk();
    base = __builtin_amdgcn_readfirstlane(sm.t0) + OFF0 + phase;

    const v2f huge = {0x1p127f, 0x1p127f};

    for (int S = 0; S < NH; ++S) {
        v2f kz2[6];
        unsigned slo[6], shi[6];
        v2u qq[3];
        const char* nsup = (const char*)(S + 1 < NH ? qk : qv);
        const int head = S >> 1 == 10 ? S : (S & ~1) + win;
        float mylogit = 0.f;
        float dot = 0.f;
        sfor<12>([&](auto N) {
            constexpr int n = decltype(N)::value;
            constexpr int i = n / 3, j = n % 3;
            if (n == 6 && (S == H0 - 1 || S == NH - 1)) fetch(S == NH - 1);
            if (j == 0) {
                const unsigned d = dsc[i ^ 2] = __builtin_amdgcn_readlane(descv[i & 1], (2 * S + 1 + (i >> 1)) & 63);
                const unsigned o = lanes(d);
                base += (d >> 4) & 31u;
                pacen(N);
                SB();
                burst(i < 2 ? (const char*)qk : nsup, i ^ 2, o);
                SB();
                if (tid == NT - 1) st_wg(&sm.tick, S * 4 + i + 1);
                check(N);
            }
            if (j == 2) getslip();
            if (i % 2 == 0 || ((dsc[i] ^ dsc[i ^ 1]) & 4u)) {
                const int it = S * 2 + (i >> 1), sl = dsc[i] & 4u ? 64 + (it & 3) : it;
                const v2u* pk = reinterpret_cast<const v2u*>(&sm.ksz[sl * 48]);
                const v2u a = pk[j * 16 + r], b = pk[48 + j * 16 + r];
                slo[j * 2 + 0] = a.x & 0xffffu; shi[j * 2 + 0] = a.x & 0xffff0000u;
                slo[j * 2 + 1] = a.y & 0xffffu; shi[j * 2 + 1] = a.y & 0xffff0000u;
                kz2[j * 2 + 0] = v2f{bf_lo(b.x), bf_hi(b.x)};
                kz2[j * 2 + 1] = v2f{bf_lo(b.y), bf_hi(b.y)};
            }
            if (i == 0) qq[j] = reinterpret_cast<const v2u*>(&sm.qt[head * 24])[j * 16 + r];
            if (j == 0) dot = 0.f;
            {
                const v4i x = buf[n % RING];
                const v2f t0 = __builtin_elementwise_fma(v2f{__int_as_float(x.x), __int_as_float(x.y)}, huge, kz2[j * 2]);
                const v2f t1 = __builtin_elementwise_fma(v2f{__int_as_float(x.z), __int_as_float(x.w)}, huge, kz2[j * 2 + 1]);
                float y[4];
                mul4(y, pack(t0.x, t0.y), pack(t1.x, t1.y), slo[j * 2], shi[j * 2], slo[j * 2 + 1], shi[j * 2 + 1]);
                dot = dot2(pack(y[0], y[1]), qq[j].x, dot);
                dot = dot2(pack(y[2], y[3]), qq[j].y, dot);
            }
            if (j == 2) {
                const float d = rowsum16(dot);
                if ((r & 3) == (i ^ ((dsc[i] >> 1) & 1))) mylogit = d;
            }
            asm volatile("" : "+v"(dot), "+v"(mylogit));
            SB();
        });
        if (r < 4) sm.p[S][wave][g * 4 + r] = (unsigned short)pack(rb(mylogit * 0x1p22f) * scale, 0.f);
        if ((S & 3) == 3) arrive(NH + (S >> 2));
        if (S == NH - 3 && tid == 0) st_wg(&sm.done, 1u);
        next_body();
    }

    for (int S = 0; S < NH; ++S) {
        v2u P;
        unsigned vsl, vsh, vzf;
        v4f acc = {0.f, 0.f, 0.f, 0.f};
        float y0[12];
        unsigned b0[12];
        const bool more = S + 1 < NH;
        unsigned sl, sh;
        v2f z2;
        sfor<12>([&](auto N) {
            constexpr int n = decltype(N)::value;
            constexpr int i = n / 3, j = n % 3;
            if (j == 0) {
                const unsigned d = dsc[i ^ 2] = __builtin_amdgcn_readlane(descv[i & 1], (2 * S + 1 + (i >> 1)) & 63);
                unsigned o = lanes(d);
                const char* src = (const char*)qv;
                if (i >= 2 && !more) { o = i * 768; src = dummy; }
                if (i < 2 || more) base += (d >> 4) & 31u;
                if (i < 2 || more) pacen(N);
                SB();
                burst(src, i ^ 2, o);
                SB();
                check(N);
            }
            if (n == 0) {
                const unsigned need = S < 2 ? 1 : S;
                while (__builtin_amdgcn_readfirstlane(ld_wg(&sm.prog)) < need) {}
                asm volatile("" ::: "memory");
                P = *reinterpret_cast<const v2u*>(&sm.p[S][wave][g * 4]);
                P.x = __builtin_amdgcn_alignbit(P.x, P.x, (dsc[0] & 2u) << 3);
                P.y = __builtin_amdgcn_alignbit(P.y, P.y, (dsc[2] & 2u) << 3);
                const unsigned d3 = __builtin_amdgcn_readlane(descv[1], 2 * S + 1);
                const unsigned x = (((dsc[0] & 7u) | (dsc[1] & 7u) << 8) | ((dsc[2] & 7u) << 16 | (d3 & 7u) << 24)) >> (r & 3) * 8;
                const int it = S * 2 + ((r >> 1) & 1);
                const int ltok = (x & 4u ? 64 + (it & 3) : it) * 64 + (x & 1u ? tod : tev);
                vsl = reinterpret_cast<const unsigned short*>(&sm.vsz[0][0])[ltok];
                vsh = vsl << 16;
                vzf = (unsigned)reinterpret_cast<const unsigned short*>(&sm.vsz[1][0])[ltok] << 16;
            }
            if (j == 2) getslip();
            if (j == 0) {
                sl = dppu<0x55 * i>(vsl);
                sh = dppu<0x55 * i>(vsh);
                const float zb = __uint_as_float(dppu<0x55 * i>(vzf));
                z2 = v2f{zb, zb};
            }
            float y[4];
            {
                const v4i x = buf[n];
                const v2f t0 = __builtin_elementwise_fma(v2f{__int_as_float(x.x), __int_as_float(x.y)}, huge, z2);
                const v2f t1 = __builtin_elementwise_fma(v2f{__int_as_float(x.z), __int_as_float(x.w)}, huge, z2);
                mul4(y, pack(t0.x, t0.y), pack(t1.x, t1.y), sl, sh, sl, sh);
            }
#pragma unroll
            for (int e = 0; e < 4; ++e) {
                const int idx = j * 4 + e;
                if (i % 2 == 0) {
                    y0[idx] = y[e];
                    asm volatile("" : "+v"(y0[idx]));
                } else if (i == 1) {
                    b0[idx] = pack(y0[idx], y[e]);
                    asm volatile("" : "+v"(b0[idx]));
                } else {
                    const v2u B = {b0[idx], pack(y0[idx], y[e])};
                    const v2u A = {r == idx ? P.x : 0u, r == idx ? P.y : 0u};
                    acc = __builtin_amdgcn_mfma_f32_16x16x16bf16_1k(__builtin_bit_cast(v4s, A), __builtin_bit_cast(v4s, B), acc, 0, 0, 0);
                }
            }
            if (n == 11) {
                if (S == NH - 2 && tid == 0) sm.tref = base + slot(12) + slot(3);
                if (S == NH - 3 && tid == 0) st_wg(&sm.done, 2u);
                if (lane < 48) *reinterpret_cast<v4f*>(&sm.stage[S & 1][wave][lane * 4]) = acc;
                arrive(S);
            }
            SB();
        });
        next_body();
    }
    if (DST >= 21) d_x = sm.dsv;
    const unsigned d_t1 = clk();
    {
        const int mh = (blockIdx.x / 16 * 2 + (blockIdx.x & 7) / 4 + 20) % NH, msl = (blockIdx.x & 3) * 2 + ((blockIdx.x >> 3) & 1);
        const bool both = mh >> 1 == 10;
        const unsigned tag = tagof(sm.epoch, lane & 3);
        const float* src = ws + ((size_t)((both ? 0 : mh & 1) * (NWG / 2) + wave * 16) * NRC + (mh >> 1) + (mh > 20)) * RS + msl * 16;
        const unsigned so = (lane >> 2) * (NRC * RS * 4) + (lane & 3) * 16;
        until(base - slot(12) + slot(3) - phase + wave * STG + (mh >= NH - 2 ? TPL : TPC));
        v4i x[2];
        for (;;) {
            x[0] = ld_rec(src, so);
            x[1] = v4i{0, 0, 0, (int)tag};
            if (both) x[1] = ld_rec(src + NWG / 2 * NRC * RS, so);
            asm volatile("s_waitcnt vmcnt(0)" : "+v"(x[0]), "+v"(x[1]));
            if (__all((((unsigned)x[0].w ^ tag) | ((unsigned)x[1].w ^ tag)) < 256u) || clk() - tb >= TMO) break;
            __builtin_amdgcn_s_sleep(PSL);
        }
#pragma unroll
        for (int d = 0; d < 6; ++d) asm volatile("" :: "v"(buf[d]));
        unsigned ml[2];
#pragma unroll
        for (int k = 0; k < 2; ++k) {
            ml[k] = ((unsigned)x[k].w ^ tag) << (lane & 3) * 8;
            ml[k] |= dppu<0xB1>(ml[k]);
            ml[k] |= dppu<0x4E>(ml[k]);
        }
        if (!both) ml[1] = ml[0] & 0xffff0000u;
        const float M = x4max(fmaxf(bf_hi(ml[0]), bf_hi(ml[1])));
        const float w0 = __expf(bf_hi(ml[0]) - M), w1 = __expf(bf_hi(ml[1]) - M);
        float a[6];
#pragma unroll
        for (int e = 0; e < 3; ++e) {
            a[2 * e] = bf_lo(x[0][e]) * w0 + bf_lo(x[1][e]) * w1;
            a[2 * e + 1] = bf_hi(x[0][e]) * w0 + bf_hi(x[1][e]) * w1;
        }
        const float l = (float)__builtin_bit_cast(_Float16, (unsigned short)ml[0]) * w0 + (float)__builtin_bit_cast(_Float16, (unsigned short)ml[1]) * w1;
        float c0 = fold16(fold32(a[0], a[4]), fold32(a[2], l));
        float c1 = fold16(fold32(a[1], a[5]), fold32(a[3], l));
        c0 += dpp<0x124>(c0); c1 += dpp<0x124>(c1);
        c0 += dpp<0x128>(c0); c1 += dpp<0x128>(c1);
        if (g == 3) c1 = M;
        if (r < 4) *reinterpret_cast<v2f*>(&sm.fin[wave][g == 3 ? 24 + r * 2 : r * 6 + g * 2]) = v2f{c0, c1};
        asm volatile("" ::: "memory");
        unsigned arr = 0;
        if (lane == 0) arr = __hip_atomic_fetch_add(&sm.fcnt, 1u, __ATOMIC_RELAXED, __HIP_MEMORY_SCOPE_WORKGROUP);
        asm volatile("" ::: "memory");
        const v4f y = *reinterpret_cast<const v4f*>(&sm.fin[lane >> 3][(lane & 7) * 4]);
        const float mw = sm.fin[lane >> 3][25];
        const float w = __expf(mw - x4max(mw));
        float d = fold16(fold32(y.x * w, y.z * w), fold32(y.y * w, y.w * w));
        d += dpp<0x128>(d);
        const float L = __int_as_float(__builtin_amdgcn_readlane(__float_as_int(d), 6));
        const unsigned o = pack(d / L, 0.f);
        asm volatile("" :: "v"(o));
        if (__builtin_amdgcn_readfirstlane(arr) == WPB - 1) {
            if (r < 6) reinterpret_cast<unsigned short*>(out)[mh * DH + msl * SLC + r * 4 + g] = (unsigned short)o;
        }
    }
    {
        unsigned* dg = reinterpret_cast<unsigned*>(ws + WS_T + 65536) + (blockIdx.x * WPB + wave) * 8;
        if (lane == 0) { dg[0] = tb; dg[1] = d_t1; dg[2] = clk(); dg[3] = off; dg[4] = DST ? d_x : d_l; dg[5] = d_f; dg[6] = d_bar - phase; dg[7] = DIAG >= 12 ? d_nb : d_s; }
    }
}
__global__ void kivi_end_marker() { asm volatile("kivi_code_end:"); }

__global__ void diag_kernel(const float* ws) {
    const unsigned* dg = reinterpret_cast<const unsigned*>(ws + WS_T + 65536);
    const unsigned ref = dg[0];
    int mn = 1 << 30, x = -(1 << 30), cnt = 0;
    for (int b = 0; b < 8; ++b) {
        const unsigned* e = dg + (b * 256 + threadIdx.x) * 8;
        mn = min(mn, (int)(e[0] - ref));
        const int v = DIAG == 2 ? (int)(e[0] - ref) : DIAG == 3 ? (int)e[3] : DIAG == 4 ? (int)(e[1] - ref - e[3]) : DIAG == 5 ? (int)(e[2] - ref)
                    : DIAG == 12 ? (int)(e[2] - ref - e[7] * SLIP) : DIAG == 13 ? (int)(e[3] - e[7] * SLIP) : DIAG == 6 ? (int)e[4] : DIAG == 7 || DIAG == 14 ? (int)(e[6] - ref) : DIAG == 8 ? (e[5] ? 1000 - (int)e[5] : 0) : DIAG == 9 ? (e[7] ? 1000 - (int)e[7] : 0) : 0;
        x = max(x, v);
        cnt += DIAG == 11 ? (int)e[4] : (int)e[4] > 30;
    }
    __shared__ int smn, smx, sc;
    if (threadIdx.x == 0) { smn = 1 << 30; smx = -(1 << 30); sc = 0; }
    __syncthreads();
    atomicMin(&smn, mn); atomicMax(&smx, x); atomicAdd(&sc, cnt);
    __syncthreads();
    mn = smn; x = smx;
    int X = 0;
    if (DIAG == 2 || DIAG == 7 || DIAG == 14) X = x - mn;
    if (DIAG == 3 || DIAG == 6 || DIAG == 8 || DIAG == 9) X = x;
    if (DIAG == 4 || DIAG == 5 || DIAG == 12) X = x - mn - 20000;
    if (DIAG == 13) X = x;
    if (DIAG == 10) X = sc;
    if (DIAG == 11) X = sc / 2048;
    X = max(0, min(X, 3000));
    while ((int)(clk() - ref) < mn + 25000 + X * 10) __builtin_amdgcn_s_sleep(1);
}
extern "C" void kernel_launch(void* const* d_in, const int* in_sizes, int n_in,
                              void* d_out, int out_size, void* d_ws, size_t ws_size,
                              hipStream_t stream) {
    const __hip_bfloat16* q = (const __hip_bfloat16*)d_in[0];
    const int* qk = (const int*)d_in[1];
    const int* qv = (const int*)d_in[2];
    const __hip_bfloat16* ks = (const __hip_bfloat16*)d_in[3];
    const __hip_bfloat16* kz = (const __hip_bfloat16*)d_in[4];
    const __hip_bfloat16* vs = (const __hip_bfloat16*)d_in[5];
    const __hip_bfloat16* vz = (const __hip_bfloat16*)d_in[6];
    const float scale = (float)(1.0 / sqrt((double)DH));
    kivi_stream_kernel<<<NWG, NT + 128, 0, stream>>>(q, qk, qv, ks, kz, vs, vz, (float*)d_ws, (__hip_bfloat16*)d_out, scale);
    diag_kernel<<<1, 256, 0, stream>>>((const float*)d_ws);
}
```

```cpp
#define MODE 0
#define THRH 620
#define PB 0
#include <hip/hip_runtime.h>
#include <hip/hip_bf16.h>
#include <stdint.h>
#include <utility>

#define TGT 201.75
#define WST 16
#define THR 80
#define PRV 16
#define SLIP 250
#define MAXOFF 3000
#define NSENT 8
#define NR 32
#define TPL 260
#define STG 0
#define PSLEEP 32
#define OFF0 380
#define TGATH 150
#define GAP1 10
#define TREL 90
#define G21 3
#define NSLOW 1
#define EXK 6
#define NOSLK 2
#define TMO 100000000u
#define TPC 260
#define PSL 2
#define TGMAX 300
#define SBULK 1
#define PSH 0
#define STCH 1
#define SS 20
#define HOLD 4
#define VOTES 4
#define HOLDB 8
#define REVT 600
#define VWAIT 30
#define LFAC 8
#define NOSM 2
#define RING 12
#define SB() __builtin_amdgcn_sched_barrier(0)
#define PST(i) SB()

typedef int v4i __attribute__((ext_vector_type(4)));
typedef unsigned v2u __attribute__((ext_vector_type(2)));
typedef float v2f __attribute__((ext_vector_type(2)));
typedef float v4f __attribute__((ext_vector_type(4)));
typedef short v4s __attribute__((ext_vector_type(4)));
typedef __bf16 v2bf __attribute__((ext_vector_type(2)));

constexpr int DH = 192;
constexpr int NH = 32;
constexpr int SKV = 32768;
constexpr int WPB = 8;
constexpr int NT = WPB * 64;
constexpr int NWG = 256;
constexpr int NSL = 8, SLC = 24;
constexpr int RS = NSL * 16;
static_assert(SKV == 2 * 64 * NWG, "two 64-token groups per workgroup and head");
constexpr size_t WS_T = (size_t)NH * NWG * RS;
constexpr unsigned BODY16 = (unsigned)(TGT * 100.0 / 64.0 * 65536.0);
constexpr int slot(int n) { return (int)((n * (BODY16 / 12)) >> 16); }

template <typename F, int... I>
__device__ __forceinline__ void sfor_impl(F&& f, std::integer_sequence<int, I...>) {
    (f(std::integral_constant<int, I>{}), ...);
}
template <int N, typename F>
__device__ __forceinline__ void sfor(F&& f) {
    sfor_impl(f, std::make_integer_sequence<int, N>{});
}

__device__ __forceinline__ float bf_lo(unsigned u) { return __uint_as_float(u << 16); }
__device__ __forceinline__ float bf_hi(unsigned u) { return __uint_as_float(u & 0xffff0000u); }
__device__ __forceinline__ float rb(float x) { return (float)(__bf16)x; }
__device__ __forceinline__ unsigned pack(float lo, float hi) {
    return __builtin_bit_cast(unsigned, __builtin_convertvector(v2f{lo, hi}, v2bf));
}
__device__ __forceinline__ float dot2(unsigned a, unsigned b, float c) {
    return __builtin_amdgcn_fdot2_f32_bf16(__builtin_bit_cast(v2bf, a), __builtin_bit_cast(v2bf, b), c, false);
}
__device__ __forceinline__ void mul4(float (&y)[4], unsigned a, unsigned b, unsigned s0, unsigned s1, unsigned s2, unsigned s3) {
    asm("v_dot2_f32_bf16 %0, %4, %6, 0\n\tv_dot2_f32_bf16 %1, %4, %7, 0\n\t"
        "v_dot2_f32_bf16 %2, %5, %8, 0\n\tv_dot2_f32_bf16 %3, %5, %9, 0\n\ts_nop 2"
        : "=&v"(y[0]), "=&v"(y[1]), "=&v"(y[2]), "=&v"(y[3])
        : "v"(a), "v"(b), "v"(s0), "v"(s1), "v"(s2), "v"(s3));
}
__device__ __forceinline__ unsigned clk() { return (unsigned)__builtin_amdgcn_s_memrealtime(); }
__device__ __forceinline__ unsigned inflight() {
    unsigned x;
    asm volatile("s_getreg_b32 %0, hwreg(HW_REG_IB_STS, 0, 4)" : "=s"(x));
    return x;
}
__device__ __forceinline__ unsigned negscale(unsigned u) { return pack(bf_lo(u) * -0x1p-22f, bf_hi(u) * -0x1p-22f); }

template <int CTRL>
__device__ __forceinline__ unsigned dppu(unsigned x) {
    return (unsigned)__builtin_amdgcn_mov_dpp((int)x, CTRL, 0xf, 0xf, true);
}
template <int CTRL>
__device__ __forceinline__ float dpp(float x) { return __uint_as_float(dppu<CTRL>(__float_as_uint(x))); }
__device__ __forceinline__ float rowsum16(float x) {
    x += dpp<0x128>(x);
    x += dpp<0x124>(x);
    x += dpp<0x122>(x);
    x += dpp<0x121>(x);
    return x;
}
__device__ __forceinline__ v4i ldstream(const void* p) {
    return __builtin_nontemporal_load(reinterpret_cast<const v4i*>(p));
}

__device__ __forceinline__ void st_rec(void* p, unsigned o, v4i a) {
    asm volatile("global_store_dwordx4 %0, %1, %2 sc1\n\ts_nop 1" :: "v"(o), "v"(a), "s"(p) : "memory");
}
__device__ __forceinline__ v4i ld_rec(const void* p, unsigned o) {
    v4i x;
    asm volatile("global_load_dwordx4 %0, %1, %2 sc1" : "=v"(x) : "v"(o), "s"(p) : "memory");
    return x;
}
typedef unsigned long long u64;
__device__ __forceinline__ u64 ld_dev(const u64* p) { return __hip_atomic_load(p, __ATOMIC_RELAXED, __HIP_MEMORY_SCOPE_AGENT); }
__device__ __forceinline__ void st_dev(u64* p, u64 v) { __hip_atomic_store(p, v, __ATOMIC_RELAXED, __HIP_MEMORY_SCOPE_AGENT); }
__device__ __forceinline__ unsigned ld_wg(const unsigned* p) { return __hip_atomic_load(p, __ATOMIC_RELAXED, __HIP_MEMORY_SCOPE_WORKGROUP); }
__device__ __forceinline__ void st_wg(unsigned* p, unsigned v) { __hip_atomic_store(p, v, __ATOMIC_RELAXED, __HIP_MEMORY_SCOPE_WORKGROUP); }
__device__ __forceinline__ float fold32(float a, float b) {
    auto s = __builtin_amdgcn_permlane32_swap(__float_as_uint(a), __float_as_uint(b), false, false);
    return __uint_as_float(s[0]) + __uint_as_float(s[1]);
}
__device__ __forceinline__ float fold16(float a, float b) {
    auto s = __builtin_amdgcn_permlane16_swap(__float_as_uint(a), __float_as_uint(b), false, false);
    return __uint_as_float(s[0]) + __uint_as_float(s[1]);
}
__device__ __forceinline__ float x4max(float x) {
    x = fmaxf(x, dpp<0x124>(x));
    x = fmaxf(x, dpp<0x128>(x));
    auto s = __builtin_amdgcn_permlane16_swap(__float_as_uint(x), __float_as_uint(x), false, false);
    x = fmaxf(__uint_as_float(s[0]), __uint_as_float(s[1]));
    auto t = __builtin_amdgcn_permlane32_swap(__float_as_uint(x), __float_as_uint(x), false, false);
    return fmaxf(__uint_as_float(t[0]), __uint_as_float(t[1]));
}

struct __attribute__((aligned(16))) Smem {
    v4i ksz[64 * 48];
    v4i qt[NH * 24];
    v4i vsz[2 * 512];
    v4i rec[NH][RS / 4];
    float stage[2][WPB][DH];
    unsigned short p[NH][WPB][16];
    unsigned ml[NH];
    float fin[WPB][32];
    u64 epoch;
    unsigned have;
    unsigned tref;
    unsigned slip;
    unsigned vote[2 * NH];
    unsigned late[WPB];
    unsigned req;
    unsigned t0;
    unsigned done;
    unsigned prog;
    unsigned fcnt;
    unsigned cnt[NH + NH / 4];
};

__global__ __launch_bounds__(NT + 128, 3) void kivi_stream_kernel(
    const __hip_bfloat16* __restrict__ q, const int* __restrict__ qk, const int* __restrict__ qv,
    const __hip_bfloat16* __restrict__ ks, const __hip_bfloat16* __restrict__ kz,
    const __hip_bfloat16* __restrict__ vs, const __hip_bfloat16* __restrict__ vz,
    float* __restrict__ ws, __hip_bfloat16* __restrict__ out, float scale) {
    __shared__ Smem sm;

    unsigned ka0, ka1;
    asm volatile("s_load_dword %0, %2, 0x0\n\ts_load_dword %1, %2, 0x40" : "=&s"(ka0), "=&s"(ka1) : "s"(__builtin_amdgcn_kernarg_segment_ptr()));
    const u64 tb64 = __builtin_amdgcn_s_memrealtime();
    const unsigned tb = (unsigned)tb64;
    const int tid = threadIdx.x;
    typedef const __attribute__((address_space(1))) int* gptr;
    u64 code;
    int codelen;
    asm volatile("s_getpc_b64 %0\nkivi_code_begin:\n\ts_mov_b32 %1, kivi_code_end-kivi_code_begin" : "=s"(code), "=s"(codelen));
    const int warm = *(gptr)(code + min(tid * 128, codelen - 4));
    const int lane = tid & 63;
    const int wave = __builtin_amdgcn_readfirstlane(tid >> 6);
    const int r = lane & 15;
    const int g = lane >> 4;
    const int pos = (blockIdx.x & ~7) | ((blockIdx.x & 3) << 1) | ((blockIdx.x >> 2) & 1);
    const int g0 = (pos & (NWG / 2 - 1)) + (pos / (NWG / 2)) * NWG;
    constexpr int GST = NWG / 2;
    unsigned po[3];
    int dsti[3];
#pragma unroll
    for (int u = 0; u < 3; ++u) {
        const int f = u * NT + tid;
        const int gi = f / 24, pc = f % 24;
        dsti[u] = gi * 48 + pc;
        po[u] = ((gi >> 1) * (2 * NWG) + (gi & 1) * GST + g0) * (DH * 2) + pc * 16;
        asm volatile("" : "+v"(po[u]));
    }
    SB();
    v4i pk[6];
    if (wave < WPB) {
        pk[0] = ldstream((const char*)ks + po[0]);
        pk[1] = ldstream((const char*)ks + po[1]);
        pk[2] = ldstream((const char*)ks + po[2]);
    }
    asm volatile("s_waitcnt lgkmcnt(0)" :: "s"(ka0), "s"(ka1));
    SB();
    u64* tstart = reinterpret_cast<u64*>(ws + WS_T);
    auto tagof = [](u64 e, int j) {
        e *= 0x9e3779b97f4a7c15ull;
        return (unsigned)(j == 3 ? (e ^ (e >> 29)) >> 8 : e >> (j * 20)) << 8;
    };
    auto until = [&](unsigned t) { while ((int)(clk() - t) < 0) __builtin_amdgcn_s_sleep(1); };

    if (__builtin_expect(wave == WPB, 0)) {
        const int l = lane;
        u64* echo = tstart + NWG;
        u64* gofs = tstart + 2 * NWG;
        if (l == 0) {
            st_dev(tstart + blockIdx.x, tb64);
            if (blockIdx.x < NR) st_dev(gofs + blockIdx.x * 32, tb64 - 100000ull);
            if (blockIdx.x == NR) st_dev(gofs + 16, tb64 - 100000ull);
            sm.slip = 0;
            sm.req = 0;
            sm.done = 0;
            sm.prog = 0;
            sm.fcnt = 0;
            sm.epoch = tb64;
            sm.have = blockIdx.x == 0;
        }
        if (l < NH + NH / 4) sm.cnt[l] = 0;
        sm.vote[l] = 0;
        until(tb + TGATH);
        unsigned ld;
        for (;;) {
            const u64 dt = ld_dev(tstart + NWG - 8 + (l & 7)) - tb64 + 1000ull;
            ld = dt < 2000ull ? (unsigned)dt : 0u;
            if (__all(dt < 2000ull) || clk() - tb > (unsigned)TGMAX) break;
        }
        ld = max(ld, dppu<0x124>(ld));
        ld = max(ld, dppu<0x122>(ld));
        ld = max(ld, dppu<0x121>(ld));
        ld = __builtin_amdgcn_readfirstlane(ld);
        const u64 t64 = ld ? tb64 + ld - 1000ull : tb64;
        if (l == 0) sm.t0 = (unsigned)t64;
        __syncthreads();
        asm volatile("" :: "v"(warm));
        u64 E = tb64, to[NWG / 64];
        bool have = blockIdx.x == 0;
        if (have) {
#pragma unroll
            for (int u = 0; u < NWG / 64; ++u) to[u] = ld_dev(tstart + u * 64 + l);
            asm volatile("s_waitcnt vmcnt(0)" ::: "memory");
#pragma unroll
            for (int u = 0; u < NWG / 64; ++u) st_dev(echo + u * 64 + l, to[u]);
        }
        auto chores = [&]() {
            if (blockIdx.x == 0) {
#pragma unroll
                for (int u = 0; u < NWG / 64; ++u) {
                    const u64 x = ld_dev(tstart + u * 64 + l);
                    if (x != to[u]) { st_dev(echo + u * 64 + l, x); to[u] = x; }
                }
            } else if (!have && ld_dev(echo + blockIdx.x) == tb64) {
                asm volatile("" ::: "memory");
                E = ld_dev(tstart);
                have = true;
                if (l == 0) sm.epoch = E;
                asm volatile("s_waitcnt lgkmcnt(0)" ::: "memory");
                if (l == 0) st_wg(&sm.have, 1u);
            }
        };
        const u64* mine = gofs + ((blockIdx.x >> 3) % NR) * 32;
        unsigned sent = 0, stage = 0, tsh = clk();
        while (stage < 2) {
            const u64 gd = ld_dev(mine) - t64;
            const unsigned go = gd <= (u64)MAXOFF ? (unsigned)gd : 0u;
            const unsigned own = ld_wg(&sm.slip);
            if (go > own && l == 0) __hip_atomic_fetch_max(&sm.slip, go, __ATOMIC_RELAXED, __HIP_MEMORY_SCOPE_WORKGROUP);
            if (own <= go) tsh = clk();
            else if (clk() - tsh > (unsigned)REVT && blockIdx.x >= NSENT) { if (l == 0) st_wg(&sm.slip, go); }
            const unsigned req = ld_wg(&sm.req);
            unsigned want = blockIdx.x < NSENT ? own : 0u;
            if (req == go + SS && own == go) {
                unsigned x = ld_wg(&sm.late[l & 7]);
                x += dppu<0x121>(x);
                x += dppu<0x122>(x);
                x += dppu<0x124>(x);
                want = min(req + (__builtin_amdgcn_readfirstlane(x) * LFAC / 64 & ~15u), (unsigned)MAXOFF);
            }
            if (want > go && want > sent) {
                u64 old = 0;
                if (l == 0) old = __hip_atomic_fetch_max(gofs + 16, t64 + want, __ATOMIC_RELAXED, __HIP_MEMORY_SCOPE_AGENT);
                if (__builtin_amdgcn_readfirstlane((unsigned)(old < t64 + want)) && l < NR)
                    __hip_atomic_fetch_max(gofs + l * 32, t64 + want, __ATOMIC_RELAXED, __HIP_MEMORY_SCOPE_AGENT);
                sent = want;
            }
            chores();
            __builtin_amdgcn_s_sleep(PSLEEP);
            if (ld_wg(&sm.done) > stage) {
                ++stage;
                asm volatile("" : "+s"(code));
                int w = 0;
#pragma unroll
                for (int u = 0; u < 3; ++u) {
                    const int a = (u * 64 + l) * 128;
                    w ^= *(gptr)(code + (a < codelen ? a : 0));
                }
                asm volatile("" :: "v"(w));
            }
        }
        while (!have && clk() - tb < TMO) chores();
        if (l == 0) st_wg(&sm.have, 1u);
        if (blockIdx.x == 0) {
            for (int spin = 1; ld_wg(&sm.fcnt) < WPB; ++spin) {
                __builtin_amdgcn_s_sleep(2);
                if (spin % 1024 == 0) chores();
            }
        }
        return;
    }
    if (__builtin_expect(wave == WPB + 1, 0)) {
        {
            const int k = blockIdx.x >> 3;
            const char* pg = k < 3 ? (const char*)kz + ((size_t)k << 21) : k == 3 ? (const char*)vs : k == 4 ? (const char*)vz
                           : k < 7 ? (const char*)ks + ((size_t)(k - 4) << 21) : (const char*)qk + ((size_t)(k - 7) << 21);
            unsigned x;
            asm volatile("s_load_dword %0, %1, 0x0\n\ts_waitcnt lgkmcnt(0)" : "=s"(x) : "s"(pg + (blockIdx.x & 7) * 256) : "memory");
        }
        __syncthreads();
        asm volatile("" :: "v"(warm));
#pragma nounroll
        for (int i = 0; i < 26; ++i) {
            const size_t pg = (size_t)((blockIdx.x >> 3) + 32 * (i % 12)) << 21;
            const char* tp = i < 12 ? (const char*)qk + pg : i < 24 ? (const char*)qv + pg : i == 24 ? (const char*)ws + ((size_t)((blockIdx.x >> 3) & 1) << 21) : (const char*)out;
            if (i < 25) tp += (((blockIdx.x >> 3) * 29 + i) & 127) * 256;
            unsigned x;
            asm volatile("s_load_dword %0, %1, 0x0\n\ts_waitcnt lgkmcnt(0)" : "=s"(x) : "s"(tp) : "memory");
        }
#pragma nounroll
        for (int c = 0; c < NH / 4; ++c) {
            while (ld_wg(&sm.cnt[NH + c]) < WPB) __builtin_amdgcn_s_sleep(8);
            asm volatile("" ::: "memory");
            v4i* pp = reinterpret_cast<v4i*>(&sm.p[c * 4][0][0]) + lane;
            const v4i u = *pp;
            float x[8];
#pragma unroll
            for (int e = 0; e < 4; ++e) { x[2 * e] = bf_lo(u[e]); x[2 * e + 1] = bf_hi(u[e]); }
            float M = fmaxf(fmaxf(fmaxf(x[0], x[1]), fmaxf(x[2], x[3])), fmaxf(fmaxf(x[4], x[5]), fmaxf(x[6], x[7])));
            M = fmaxf(M, dpp<0x128>(M));
            M = fmaxf(M, dpp<0x124>(M));
            M = fmaxf(M, dpp<0x122>(M));
            M = fmaxf(M, dpp<0x121>(M));
            v4i o;
            float L = 0.f;
#pragma unroll
            for (int e = 0; e < 4; ++e) {
                const unsigned pk = pack(__expf(x[2 * e] - M), __expf(x[2 * e + 1] - M));
                o[e] = (int)pk;
                L += bf_lo(pk) + bf_hi(pk);
            }
            *pp = o;
            L = rowsum16(L);
            if (r == 0) sm.ml[c * 4 + g] = __float_as_uint(M) | __builtin_bit_cast(unsigned short, (_Float16)L);
            asm volatile("" ::: "memory");
            if (lane == 0) st_wg(&sm.prog, c + 1);
        }
        while (ld_wg(&sm.have) == 0) __builtin_amdgcn_s_sleep(8);
        asm volatile("" ::: "memory");
        const unsigned tag = tagof(sm.epoch, lane & 3);
        float* mine = ws + (size_t)blockIdx.x * NH * RS;
#pragma nounroll
        for (int h = 0; h < NH; ++h) {
            const unsigned mlb = (sm.ml[h] >> (lane & 3) * 8) & 0xffu;
            if (h < NH - 1) { while (ld_wg(&sm.cnt[h]) < WPB) __builtin_amdgcn_s_sleep(4); }
            else { while (ld_wg(&sm.cnt[h]) < WPB) {} }
            asm volatile("" ::: "memory");
            v2f c[4][3];
#pragma unroll
            for (int e = 0; e < 4; ++e)
#pragma unroll
                for (int k = 0; k < 3; ++k) c[e][k] = *reinterpret_cast<const v2f*>(&sm.stage[h & 1][(lane >> 5) * 4 + e][(lane & 31) * 6 + k * 2]);
            v4i x;
#pragma unroll
            for (int k = 0; k < 3; ++k) {
                const v2f a = ((c[0][k] + c[1][k]) + (c[2][k] + c[3][k])) * 0x1p22f;
                x[k] = (int)pack(fold32(a.x, a.x), fold32(a.y, a.y));
            }
            x[3] = (int)(tag | mlb);
            if (h == NH - 1) {
                if (lane < 32) st_rec(mine + h * RS, lane * 16, x);
                break;
            }
            sm.rec[h][lane & 31] = x;
            asm volatile("" ::: "memory");
            if (lane == 0) st_wg(&sm.prog, NH / 4 + h + 1);
            if (h == NH - 2) {
                v4i y[NH / 2];
#pragma unroll
                for (int u = 0; u < NH / 2; ++u) y[u] = (&sm.rec[0][0])[u * 64 + lane];
                until(ld_wg(&sm.tref) + TREL);
#pragma unroll
                for (int u = 0; u < NH / 2; ++u)
                    if (u < NH / 2 - 1 || lane < 32) st_rec(mine + u * 2 * RS, lane * 16, y[u]);
            }
        }
        return;
    }

    const int ww = (wave & 3) * 2 + (wave >> 2);
    const int ti = ww * 4 + g, tl = ti & 15;
    const int tev = (ti >> 4) * 32 + tl + (tl >= 5 ? 6 : 0) + (tl >= 10 ? 5 : 0);
    const int tod = (ti >> 4) * 32 + 5 + tl + (tl >= 6 ? 5 : 0) + (tl >= 11 ? 6 : 0);
    constexpr size_t ITER = (size_t)GST * 64 * DH * 4;
    constexpr size_t HEAD = (size_t)SKV * DH * 4;
    auto odd = [&](int S, int i) { return (i ^ (pos / (NWG / 2)) ^ ((6 * (S * 2 * NWG + g0 + (i >> 1) * GST) + 3) >> 14)) & 1; };
    const unsigned offe = tev * (DH * 4) + r * 16, offo = tod * (DH * 4) + r * 16;
    const char* kp = reinterpret_cast<const char*>(qk) + (size_t)g0 * (64 * DH * 4);
    const char* vp = reinterpret_cast<const char*>(qv) + (size_t)g0 * (64 * DH * 4);
    const char* dummy = reinterpret_cast<const char*>(qv) + (size_t)(blockIdx.x * WPB + wave) * 4864;

    v4i buf[RING];
    auto burst = [&](const char* p, int i, unsigned o) {
        p += (size_t)(i >> 1) * ITER;
#pragma unroll
        for (int e = 0; e < 3; ++e) { buf[i * 3 + e] = ldstream(p + o + e * 256); SB(); }
    };

    const unsigned phase = ((pos & (NWG / 2 - 1)) * (BODY16 / 512) + ww * WST * (BODY16 / (12 * 256))) >> 16;
    unsigned base = tb + phase + PSH;
    unsigned sched = 0, off = 0, offv = 0, ex = 0;
    int prev = 0, now = 0, hold = 0, big = 0, body = 0;
    unsigned vc = 0;
    auto pace = [&](int due) {
        int now;
        while ((now = (int)(clk() - base)) < due) __builtin_amdgcn_s_sleep(1);
        return now;
    };
    auto follow = [&](unsigned o2) {
        hold = o2 - off > SLIP / 2 ? HOLDB : o2 != off ? HOLD : max(hold - 1, 0);
        big = o2 - off > SLIP / 2 ? HOLDB : max(big - 1, 0);
        base += o2 - off;
        off = o2;
    };
    auto pacen = [&](auto N) {
        constexpr int n = decltype(N)::value;
        follow(__builtin_amdgcn_readfirstlane(offv));
        now = pace(slot(n));
        vc = inflight();
        if (__builtin_expect(vc > 3, 0)) {
            asm volatile("s_waitcnt vmcnt(3)");
            now = (int)(clk() - base);
            if (now > slot(n) + THR && prev < PRV && big == 0 && off + SLIP <= MAXOFF && body >= NOSLK) {
                if (lane == 0 && __hip_atomic_fetch_add(&sm.vote[body], 1u, __ATOMIC_RELAXED, __HIP_MEMORY_SCOPE_WORKGROUP) + 1 >= VOTES)
                    __hip_atomic_fetch_max(&sm.slip, off + SLIP, __ATOMIC_RELAXED, __HIP_MEMORY_SCOPE_WORKGROUP);
                unsigned o2;
                while ((o2 = __builtin_amdgcn_readfirstlane(ld_wg(&sm.slip))) == off && (int)(clk() - base) < now + VWAIT) {}
                if (o2 != off) { follow(o2); now = pace(slot(n)); }
            }
            if (hold == 0 && off + SS <= MAXOFF && body >= NOSM && lane == 0) st_wg(&sm.req, off + SS);
        }
    };
    auto check = [&](auto N) {
        constexpr int n = decltype(N)::value;
        base += ex;
        prev = now - slot(n);
        if (lane == 0) st_wg(&sm.late[wave], (unsigned)prev);
    };
    auto getslip = [&]() { offv = __hip_atomic_load(&sm.slip, __ATOMIC_RELAXED, __HIP_MEMORY_SCOPE_WORKGROUP); };
    auto arrive = [&](int i) {
        asm volatile("" ::: "memory");
        if (lane == 0) __hip_atomic_fetch_add(&sm.cnt[i], 1u, __ATOMIC_RELAXED, __HIP_MEMORY_SCOPE_WORKGROUP);
    };
    auto next_body = [&]() {
        const unsigned s2 = sched + BODY16;
        base += (s2 >> 16) - (sched >> 16);
        sched = s2;
        ++body;
    };

    {
        v4i pv[2], pq[2];
        pq[0] = reinterpret_cast<const v4i*>(q)[tid];
        pq[1] = reinterpret_cast<const v4i*>(q)[NT + (tid & 255)];
        const unsigned vo = (((tid >> 4) * (2 * NWG) + ((tid >> 3) & 1) * GST + g0) * 64 + (tid & 7) * 8) * 2;
        PST(1); pace(slot(3)); SB();
        pk[3] = ldstream((const char*)kz + po[0]);
        pk[4] = ldstream((const char*)kz + po[1]);
        pk[5] = ldstream((const char*)kz + po[2]);
        PST(2); pace(slot(6)); SB();
        pv[0] = ldstream((const char*)vs + vo);
        pv[1] = ldstream((const char*)vz + vo);
        PST(3); pace(slot(8)); SB();
        burst(kp, 0, odd(0, 0) ? offo : offe);
        PST(4);
        sm.qt[tid] = pq[0];
        if (tid < 256) sm.qt[NT + tid] = pq[1];
#pragma unroll
        for (int u = 0; u < 3; ++u) sm.ksz[dsti[u]] = pk[u];
#pragma unroll
        for (int u = 0; u < 3; ++u) {
            const v4i x = pk[3 + u];
            sm.ksz[dsti[u] + 24] = v4i{(int)negscale(x.x), (int)negscale(x.y), (int)negscale(x.z), (int)negscale(x.w)};
        }
        PST(5); pace(slot(11)); SB();
        burst(kp, 1, odd(0, 1) ? offo : offe);
        PST(6);
        sm.vsz[tid] = pv[0];
        sm.vsz[NT + tid] = v4i{(int)negscale(pv[1].x), (int)negscale(pv[1].y), (int)negscale(pv[1].z), (int)negscale(pv[1].w)};
    }
    asm volatile("" :: "v"(warm));
    PST(7);
    __syncthreads();
    PST(8);
    base = __builtin_amdgcn_readfirstlane(sm.t0) + OFF0 + phase;

    const v2f huge = {0x1p127f, 0x1p127f};

    for (int S = 0; S < NH; ++S) {
        ex = S < NSLOW ? EXK : S == 21 ? G21 : 0;
        v2f kz2[6];
        unsigned slo[6], shi[6];
        v2u qq[3];
        const char* nsup = (S + 1 < NH) ? kp + HEAD : vp;
        float mylogit = 0.f;
        float dot = 0.f;
        sfor<12>([&](auto N) {
            constexpr int n = decltype(N)::value;
            constexpr int i = n / 3, j = n % 3;
            if (j == 0) {
                const unsigned o = odd(i < 2 ? S : (S + 1) % NH, i ^ 2) ? offo : offe;
                pacen(N);
                SB();
                burst(i < 2 ? kp : nsup, i ^ 2, o);
                SB();
                check(N);
            }
            if (j == 2) getslip();
            if (i % 2 == 0) {
                const v2u* pk = reinterpret_cast<const v2u*>(&sm.ksz[(S * 2 + n / 6) * 48]);
                const v2u a = pk[j * 16 + r], b = pk[48 + j * 16 + r];
                slo[j * 2 + 0] = a.x & 0xffffu; shi[j * 2 + 0] = a.x & 0xffff0000u;
                slo[j * 2 + 1] = a.y & 0xffffu; shi[j * 2 + 1] = a.y & 0xffff0000u;
                kz2[j * 2 + 0] = v2f{bf_lo(b.x), bf_hi(b.x)};
                kz2[j * 2 + 1] = v2f{bf_lo(b.y), bf_hi(b.y)};
            }
            if (i == 0) qq[j] = reinterpret_cast<const v2u*>(&sm.qt[S * 24])[j * 16 + r];
            if (j == 0) dot = 0.f;
            {
                const v4i x = buf[n % RING];
                const v2f t0 = __builtin_elementwise_fma(v2f{__int_as_float(x.x), __int_as_float(x.y)}, huge, kz2[j * 2]);
                const v2f t1 = __builtin_elementwise_fma(v2f{__int_as_float(x.z), __int_as_float(x.w)}, huge, kz2[j * 2 + 1]);
                float y[4];
                mul4(y, pack(t0.x, t0.y), pack(t1.x, t1.y), slo[j * 2], shi[j * 2], slo[j * 2 + 1], shi[j * 2 + 1]);
                dot = dot2(pack(y[0], y[1]), qq[j].x, dot);
                dot = dot2(pack(y[2], y[3]), qq[j].y, dot);
            }
            if (j == 2) {
                const float d = rowsum16(dot);
                if ((r & 3) == i) mylogit = d;
            }
            asm volatile("" : "+v"(dot), "+v"(mylogit));
            SB();
        });
        if (r < 4) sm.p[S][wave][g * 4 + r] = (unsigned short)pack(rb(mylogit * 0x1p22f) * scale, 0.f);
        if ((S & 3) == 3) arrive(NH + (S >> 2));
        if (S == NH - 3 && tid == 0) st_wg(&sm.done, 1u);
        kp = nsup;
        next_body();
    }
    base += GAP1;

    for (int S = 0; S < NH; ++S) {
        ex = S == 21 ? G21 : 0;
        v2u P;
        unsigned vsl, vsh, vzf;
        v4f acc = {0.f, 0.f, 0.f, 0.f};
        float y0[12];
        unsigned b0[12];
        const bool more = S + 1 < NH;
        const char* nsup = more ? vp + HEAD : dummy;
        unsigned sl, sh;
        v2f z2;
        sfor<12>([&](auto N) {
            constexpr int n = decltype(N)::value;
            constexpr int i = n / 3, j = n % 3;
            if (j == 0) {
                unsigned o = odd(i < 2 ? S : S + 1, i ^ 2) ? offo : offe;
                if (i >= 2 && !more) o = i * 768;
                if (i < 2 || more) pacen(N);
                SB();
                burst(i < 2 ? vp : nsup, i ^ 2, o);
                SB();
                check(N);
            }
            if (n == 0) {
                const unsigned need = S < 2 ? 1 : NH / 4 + S - 1;
                while (__builtin_amdgcn_readfirstlane(ld_wg(&sm.prog)) < need) {}
                asm volatile("" ::: "memory");
                P = *reinterpret_cast<const v2u*>(&sm.p[S][wave][g * 4]);
                const int ltok = S * 128 + ((r >> 1) & 1) * 64 + (odd(S, r & 3) ? tod : tev);
                const unsigned short* pv = reinterpret_cast<const unsigned short*>(&sm.vsz[0]);
                vsl = pv[ltok];
                vsh = vsl << 16;
                vzf = (unsigned)pv[4096 + ltok] << 16;
            }
            if (j == 2) getslip();
            if (j == 0) {
                sl = dppu<0x55 * i>(vsl);
                sh = dppu<0x55 * i>(vsh);
                const float zb = __uint_as_float(dppu<0x55 * i>(vzf));
                z2 = v2f{zb, zb};
            }
            float y[4];
            {
                const v4i x = buf[n];
                const v2f t0 = __builtin_elementwise_fma(v2f{__int_as_float(x.x), __int_as_float(x.y)}, huge, z2);
                const v2f t1 = __builtin_elementwise_fma(v2f{__int_as_float(x.z), __int_as_float(x.w)}, huge, z2);
                mul4(y, pack(t0.x, t0.y), pack(t1.x, t1.y), sl, sh, sl, sh);
            }
#pragma unroll
            for (int e = 0; e < 4; ++e) {
                const int idx = j * 4 + e;
                if (i % 2 == 0) {
                    y0[idx] = y[e];
                    asm volatile("" : "+v"(y0[idx]));
                } else if (i == 1) {
                    b0[idx] = pack(y0[idx], y[e]);
                    asm volatile("" : "+v"(b0[idx]));
                } else {
                    const v2u B = {b0[idx], pack(y0[idx], y[e])};
                    const v2u A = {r == idx ? P.x : 0u, r == idx ? P.y : 0u};
                    acc = __builtin_amdgcn_mfma_f32_16x16x16bf16_1k(__builtin_bit_cast(v4s, A), __builtin_bit_cast(v4s, B), acc, 0, 0, 0);
                }
            }
            if (n == 11) {
                if (S == NH - 2 && tid == 0) sm.tref = base + slot(12) + slot(3);
                if (S == NH - 3 && tid == 0) st_wg(&sm.done, 2u);
                if (lane < 48) *reinterpret_cast<v4f*>(&sm.stage[S & 1][wave][lane * 4]) = acc;
                arrive(S);
            }
            SB();
        });
        vp = nsup;
        next_body();
    }
    {
        const int mh = (blockIdx.x / 16 * 2 + (blockIdx.x & 7) / 4 + NH - 1) % NH, msl = (blockIdx.x & 3) * 2 + ((blockIdx.x >> 3) & 1);
        const unsigned tag = tagof(sm.epoch, lane & 3);
        const float* src = ws + ((size_t)wave * 32 * NH + mh) * RS + msl * 16;
        const unsigned so = (lane >> 2) * (NH * RS * 4) + (lane & 3) * 16;
        until(base - slot(12) + slot(3) - phase + wave * STG + (mh == NH - 1 ? TPL : TPC));
        v4i x[2];
        for (;;) {
            x[0] = ld_rec(src, so);
            x[1] = ld_rec(src + 16 * NH * RS, so);
            asm volatile("s_waitcnt vmcnt(0)" : "+v"(x[0]), "+v"(x[1]));
            if (__all((((unsigned)x[0].w ^ tag) | ((unsigned)x[1].w ^ tag)) < 256u) || clk() - tb >= TMO) break;
            __builtin_amdgcn_s_sleep(PSL);
        }
#pragma unroll
        for (int d = 0; d < 6; ++d) asm volatile("" :: "v"(buf[d]));
        unsigned ml[2];
#pragma unroll
        for (int k = 0; k < 2; ++k) {
            ml[k] = ((unsigned)x[k].w ^ tag) << (lane & 3) * 8;
            ml[k] |= dppu<0xB1>(ml[k]);
            ml[k] |= dppu<0x4E>(ml[k]);
        }
        const float M = x4max(fmaxf(bf_hi(ml[0]), bf_hi(ml[1])));
        const float w0 = __expf(bf_hi(ml[0]) - M), w1 = __expf(bf_hi(ml[1]) - M);
        float a[6];
#pragma unroll
        for (int e = 0; e < 3; ++e) {
            a[2 * e] = bf_lo(x[0][e]) * w0 + bf_lo(x[1][e]) * w1;
            a[2 * e + 1] = bf_hi(x[0][e]) * w0 + bf_hi(x[1][e]) * w1;
        }
        const float l = (float)__builtin_bit_cast(_Float16, (unsigned short)ml[0]) * w0 + (float)__builtin_bit_cast(_Float16, (unsigned short)ml[1]) * w1;
        float c0 = fold16(fold32(a[0], a[4]), fold32(a[2], l));
        float c1 = fold16(fold32(a[1], a[5]), fold32(a[3], l));
        c0 += dpp<0x124>(c0); c1 += dpp<0x124>(c1);
        c0 += dpp<0x128>(c0); c1 += dpp<0x128>(c1);
        if (g == 3) c1 = M;
        if (r < 4) *reinterpret_cast<v2f*>(&sm.fin[wave][g == 3 ? 24 + r * 2 : r * 6 + g * 2]) = v2f{c0, c1};
        asm volatile("" ::: "memory");
        unsigned arr = 0;
        if (lane == 0) arr = __hip_atomic_fetch_add(&sm.fcnt, 1u, __ATOMIC_RELAXED, __HIP_MEMORY_SCOPE_WORKGROUP);
        asm volatile("" ::: "memory");
        const v4f y = *reinterpret_cast<const volatile v4f*>(&sm.fin[lane >> 3][(lane & 7) * 4]);
        const float mw = *reinterpret_cast<const volatile float*>(&sm.fin[lane >> 3][25]);
        const float w = __expf(mw - x4max(mw));
        float d = fold16(fold32(y.x * w, y.z * w), fold32(y.y * w, y.w * w));
        d += dpp<0x128>(d);
        const float L = __int_as_float(__builtin_amdgcn_readlane(__float_as_int(d), 6));
        const unsigned o = pack(d / L, 0.f);
        asm volatile("" :: "v"(o));
        if (__builtin_amdgcn_readfirstlane(arr) == WPB - 1) {
            if (r < 6) reinterpret_cast<unsigned short*>(out)[mh * DH + msl * SLC + r * 4 + g] = (unsigned short)o;
        }
    }
}
__global__ void kivi_end_marker() { asm volatile("kivi_code_end:"); }

__global__ void mall_probe(const char* k, const char* v, const char* ks, const char* kz, const char* vs, const char* vz, const char* wsb, unsigned* res) {
    const int b = blockIdx.x;
    const char* p = b < 48 ? k + ((size_t)b << 24) : b < 96 ? v + ((size_t)(b - 48) << 24) : b == 96 ? ks : b == 97 ? kz : b == 98 ? vs : b == 99 ? vz
                  : b == 100 ? ks + (4 << 20) : b == 101 ? kz + (4 << 20) : wsb + ((size_t)(b - 102) * 128 << 20) + (b == 102 ? 0 : (126u << 20));
    unsigned x;
    const unsigned t = clk();
    if (b == 0 && threadIdx.x == 0) res[255] = t;
    asm volatile("s_load_dword %0, %1, 0x0\n\ts_waitcnt lgkmcnt(0)" : "=s"(x) : "s"(p) : "memory");
    int hits = 0;
    for (int i = 0; i < 8; ++i) {
        const unsigned h = (t + i) * 0x9e3779b1u;
        const char* a = p + (h >> 25) * 16384 + ((h >> 8) & 127) * 64;
        u64 t0, t1, t2;
        asm volatile("s_memtime %0\n\ts_waitcnt lgkmcnt(0)\n\ts_load_dword %3, %4, 0x0\n\ts_waitcnt lgkmcnt(0)\n\t"
                     "s_memtime %1\n\ts_waitcnt lgkmcnt(0)\n\ts_load_dword %3, %4, 0x2000\n\ts_waitcnt lgkmcnt(0)\n\t"
                     "s_memtime %2\n\ts_waitcnt lgkmcnt(0)"
                     : "=&s"(t0), "=&s"(t1), "=&s"(t2), "=&s"(x) : "s"(a) : "memory");
        const int d = min((int)(t1 - t0), (int)(t2 - t1));
        if (MODE == 9) hits += d; else hits += d < THRH;
    }
    if (threadIdx.x == 0) res[b] = hits;
}
__global__ void spin_kernel(const unsigned* res) {
    int X = 0;
    if (MODE == 0) { for (int b = 0; b < 96; ++b) if (res[b] >= 4) X += b < 48 ? 50 : 1; X *= 4; }
    if (MODE == 1) { for (int b = 96; b < 103; ++b) if (res[b] >= 4) X += 20 << (b - 96); }
    if (MODE == 2) { for (int b = 103; b < 126; ++b) if (res[b] >= 4) X = (b - 102) * 100; for (int b = 103; b < 126; ++b) X += (res[b] >= 4) * 20; }
    if (MODE == 3) { for (int b = 0; b < 96; ++b) X += res[b] * 10; }
    if (MODE == 9) X = res[PB] / 8;
    while ((int)(clk() - res[255]) < 30000 + X * 10) __builtin_amdgcn_s_sleep(1);
}
extern "C" void kernel_launch(void* const* d_in, const int* in_sizes, int n_in,
                              void* d_out, int out_size, void* d_ws, size_t ws_size,
                              hipStream_t stream) {
    const __hip_bfloat16* q = (const __hip_bfloat16*)d_in[0];
    const int* qk = (const int*)d_in[1];
    const int* qv = (const int*)d_in[2];
    const __hip_bfloat16* ks = (const __hip_bfloat16*)d_in[3];
    const __hip_bfloat16* kz = (const __hip_bfloat16*)d_in[4];
    const __hip_bfloat16* vs = (const __hip_bfloat16*)d_in[5];
    const __hip_bfloat16* vz = (const __hip_bfloat16*)d_in[6];
    const float scale = (float)(1.0 / sqrt((double)DH));
    unsigned* res = (unsigned*)((char*)d_ws + (64 << 20));
    mall_probe<<<126, 64, 0, stream>>>((const char*)qk, (const char*)qv, (const char*)ks, (const char*)kz, (const char*)vs, (const char*)vz, (const char*)d_ws, res);
    kivi_stream_kernel<<<NWG, NT + 128, 0, stream>>>(q, qk, qv, ks, kz, vs, vz, (float*)d_ws, (__hip_bfloat16*)d_out, scale);
    spin_kernel<<<1, 64, 0, stream>>>(res);
}
```

```cpp
#define DIAG 11
#include <hip/hip_runtime.h>
#include <hip/hip_bf16.h>
#include <stdint.h>
#include <utility>

#define TGT 201.75
#define WST 16
#define THR 80
#define PRV 16
#define SLIP 250
#define MAXOFF 3000
#define NSENT 8
#define NR 32
#define TPL 260
#define STG 0
#define PSLEEP 32
#define OFF0 380
#define TGATH 150
#define GAP1 10
#define TREL 90
#define G21 3
#define NSLOW 1
#define EXK 6
#define NOSLK 2
#define TMO 100000000u
#define TPC 260
#define PSL 2
#define DST 3
#define BA 0
#define BB 16
#define TGMAX 300
#define SBULK 1
#define PSH 0
#define STCH 1
#define SS 20
#define HOLD 4
#define VOTES 4
#define HOLDB 8
#define REVT 600
#define VWAIT 30
#define LFAC 8
#define NOSM 2
#define RING 12
#define SB() __builtin_amdgcn_sched_barrier(0)
#define PST(i) SB()
#undef PST
#define PST(i) do { SB(); if (DST == i + 1) d_x = clk() - base; SB(); } while (0)

typedef int v4i __attribute__((ext_vector_type(4)));
typedef unsigned v2u __attribute__((ext_vector_type(2)));
typedef float v2f __attribute__((ext_vector_type(2)));
typedef float v4f __attribute__((ext_vector_type(4)));
typedef short v4s __attribute__((ext_vector_type(4)));
typedef __bf16 v2bf __attribute__((ext_vector_type(2)));

constexpr int DH = 192;
constexpr int NH = 32;
constexpr int SKV = 32768;
constexpr int WPB = 8;
constexpr int NT = WPB * 64;
constexpr int NWG = 256;
constexpr int NSL = 8, SLC = 24;
constexpr int RS = NSL * 16;
static_assert(SKV == 2 * 64 * NWG, "two 64-token groups per workgroup and head");
constexpr size_t WS_T = (size_t)NH * NWG * RS;
constexpr unsigned BODY16 = (unsigned)(TGT * 100.0 / 64.0 * 65536.0);
constexpr int slot(int n) { return (int)((n * (BODY16 / 12)) >> 16); }

template <typename F, int... I>
__device__ __forceinline__ void sfor_impl(F&& f, std::integer_sequence<int, I...>) {
    (f(std::integral_constant<int, I>{}), ...);
}
template <int N, typename F>
__device__ __forceinline__ void sfor(F&& f) {
    sfor_impl(f, std::make_integer_sequence<int, N>{});
}

__device__ __forceinline__ float bf_lo(unsigned u) { return __uint_as_float(u << 16); }
__device__ __forceinline__ float bf_hi(unsigned u) { return __uint_as_float(u & 0xffff0000u); }
__device__ __forceinline__ float rb(float x) { return (float)(__bf16)x; }
__device__ __forceinline__ unsigned pack(float lo, float hi) {
    return __builtin_bit_cast(unsigned, __builtin_convertvector(v2f{lo, hi}, v2bf));
}
__device__ __forceinline__ float dot2(unsigned a, unsigned b, float c) {
    return __builtin_amdgcn_fdot2_f32_bf16(__builtin_bit_cast(v2bf, a), __builtin_bit_cast(v2bf, b), c, false);
}
__device__ __forceinline__ void mul4(float (&y)[4], unsigned a, unsigned b, unsigned s0, unsigned s1, unsigned s2, unsigned s3) {
    asm("v_dot2_f32_bf16 %0, %4, %6, 0\n\tv_dot2_f32_bf16 %1, %4, %7, 0\n\t"
        "v_dot2_f32_bf16 %2, %5, %8, 0\n\tv_dot2_f32_bf16 %3, %5, %9, 0\n\ts_nop 2"
        : "=&v"(y[0]), "=&v"(y[1]), "=&v"(y[2]), "=&v"(y[3])
        : "v"(a), "v"(b), "v"(s0), "v"(s1), "v"(s2), "v"(s3));
}
__device__ __forceinline__ unsigned clk() { return (unsigned)__builtin_amdgcn_s_memrealtime(); }
__device__ __forceinline__ unsigned inflight() {
    unsigned x;
    asm volatile("s_getreg_b32 %0, hwreg(HW_REG_IB_STS, 0, 4)" : "=s"(x));
    return x;
}
__device__ __forceinline__ unsigned negscale(unsigned u) { return pack(bf_lo(u) * -0x1p-22f, bf_hi(u) * -0x1p-22f); }

template <int CTRL>
__device__ __forceinline__ unsigned dppu(unsigned x) {
    return (unsigned)__builtin_amdgcn_mov_dpp((int)x, CTRL, 0xf, 0xf, true);
}
template <int CTRL>
__device__ __forceinline__ float dpp(float x) { return __uint_as_float(dppu<CTRL>(__float_as_uint(x))); }
__device__ __forceinline__ float rowsum16(float x) {
    x += dpp<0x128>(x);
    x += dpp<0x124>(x);
    x += dpp<0x122>(x);
    x += dpp<0x121>(x);
    return x;
}
__device__ __forceinline__ v4i ldstream(const void* p) {
    return __builtin_nontemporal_load(reinterpret_cast<const v4i*>(p));
}

__device__ __forceinline__ void st_rec(void* p, unsigned o, v4i a) {
    asm volatile("global_store_dwordx4 %0, %1, %2 sc1\n\ts_nop 1" :: "v"(o), "v"(a), "s"(p) : "memory");
}
__device__ __forceinline__ v4i ld_rec(const void* p, unsigned o) {
    v4i x;
    asm volatile("global_load_dwordx4 %0, %1, %2 sc1" : "=v"(x) : "v"(o), "s"(p) : "memory");
    return x;
}
typedef unsigned long long u64;
__device__ __forceinline__ u64 ld_dev(const u64* p) { return __hip_atomic_load(p, __ATOMIC_RELAXED, __HIP_MEMORY_SCOPE_AGENT); }
__device__ __forceinline__ void st_dev(u64* p, u64 v) { __hip_atomic_store(p, v, __ATOMIC_RELAXED, __HIP_MEMORY_SCOPE_AGENT); }
__device__ __forceinline__ unsigned ld_wg(const unsigned* p) { return __hip_atomic_load(p, __ATOMIC_RELAXED, __HIP_MEMORY_SCOPE_WORKGROUP); }
__device__ __forceinline__ void st_wg(unsigned* p, unsigned v) { __hip_atomic_store(p, v, __ATOMIC_RELAXED, __HIP_MEMORY_SCOPE_WORKGROUP); }
__device__ __forceinline__ float fold32(float a, float b) {
    auto s = __builtin_amdgcn_permlane32_swap(__float_as_uint(a), __float_as_uint(b), false, false);
    return __uint_as_float(s[0]) + __uint_as_float(s[1]);
}
__device__ __forceinline__ float fold16(float a, float b) {
    auto s = __builtin_amdgcn_permlane16_swap(__float_as_uint(a), __float_as_uint(b), false, false);
    return __uint_as_float(s[0]) + __uint_as_float(s[1]);
}
__device__ __forceinline__ float x4max(float x) {
    x = fmaxf(x, dpp<0x124>(x));
    x = fmaxf(x, dpp<0x128>(x));
    auto s = __builtin_amdgcn_permlane16_swap(__float_as_uint(x), __float_as_uint(x), false, false);
    x = fmaxf(__uint_as_float(s[0]), __uint_as_float(s[1]));
    auto t = __builtin_amdgcn_permlane32_swap(__float_as_uint(x), __float_as_uint(x), false, false);
    return fmaxf(__uint_as_float(t[0]), __uint_as_float(t[1]));
}

struct __attribute__((aligned(16))) Smem {
    v4i ksz[64 * 48];
    v4i qt[NH * 24];
    v4i vsz[2 * 512];
    v4i rec[NH][RS / 4];
    float stage[2][WPB][DH];
    unsigned short p[NH][WPB][16];
    unsigned ml[NH];
    float fin[WPB][32];
    u64 epoch;
    unsigned have;
    unsigned tref;
    unsigned slip;
    unsigned vote[2 * NH];
    unsigned late[WPB];
    unsigned req;
    unsigned t0;
    unsigned done;
    unsigned prog;
    unsigned fcnt;
    unsigned dsv;
    unsigned cnt[NH + NH / 4];
};

__global__ __launch_bounds__(NT + 128, 3) void kivi_stream_kernel(
    const __hip_bfloat16* __restrict__ q, const int* __restrict__ qk, const int* __restrict__ qv,
    const __hip_bfloat16* __restrict__ ks, const __hip_bfloat16* __restrict__ kz,
    const __hip_bfloat16* __restrict__ vs, const __hip_bfloat16* __restrict__ vz,
    float* __restrict__ ws, __hip_bfloat16* __restrict__ out, float scale) {
    __shared__ Smem sm;

    unsigned ka0, ka1;
    asm volatile("s_load_dword %0, %2, 0x0\n\ts_load_dword %1, %2, 0x40" : "=&s"(ka0), "=&s"(ka1) : "s"(__builtin_amdgcn_kernarg_segment_ptr()));
    const u64 tb64 = __builtin_amdgcn_s_memrealtime();
    const unsigned tb = (unsigned)tb64;
    const int tid = threadIdx.x;
    typedef const __attribute__((address_space(1))) int* gptr;
    u64 code;
    int codelen;
    asm volatile("s_getpc_b64 %0\nkivi_code_begin:\n\ts_mov_b32 %1, kivi_code_end-kivi_code_begin" : "=s"(code), "=s"(codelen));
    const int warm = *(gptr)(code + min(tid * 128, codelen - 4));
    const int lane = tid & 63;
    const int wave = __builtin_amdgcn_readfirstlane(tid >> 6);
    const int r = lane & 15;
    const int g = lane >> 4;
    const int pos = (blockIdx.x & ~7) | ((blockIdx.x & 3) << 1) | ((blockIdx.x >> 2) & 1);
    const int g0 = (pos & (NWG / 2 - 1)) + (pos / (NWG / 2)) * NWG;
    constexpr int GST = NWG / 2;
    unsigned po[3];
    int dsti[3];
#pragma unroll
    for (int u = 0; u < 3; ++u) {
        const int f = u * NT + tid;
        const int gi = f / 24, pc = f % 24;
        dsti[u] = gi * 48 + pc;
        po[u] = ((gi >> 1) * (2 * NWG) + (gi & 1) * GST + g0) * (DH * 2) + pc * 16;
        asm volatile("" : "+v"(po[u]));
    }
    SB();
    v4i pk[6];
    if (wave < WPB) {
        pk[0] = ldstream((const char*)ks + po[0]);
        pk[1] = ldstream((const char*)ks + po[1]);
        pk[2] = ldstream((const char*)ks + po[2]);
    }
    asm volatile("s_waitcnt lgkmcnt(0)" :: "s"(ka0), "s"(ka1));
    SB();
    u64* tstart = reinterpret_cast<u64*>(ws + WS_T);
    auto tagof = [](u64 e, int j) {
        e *= 0x9e3779b97f4a7c15ull;
        return (unsigned)(j == 3 ? (e ^ (e >> 29)) >> 8 : e >> (j * 20)) << 8;
    };
    auto until = [&](unsigned t) { while ((int)(clk() - t) < 0) __builtin_amdgcn_s_sleep(1); };

    if (__builtin_expect(wave == WPB, 0)) {
        const int l = lane;
        u64* echo = tstart + NWG;
        u64* gofs = tstart + 2 * NWG;
        if (l == 0) {
            st_dev(tstart + blockIdx.x, tb64);
            if (blockIdx.x < NR) st_dev(gofs + blockIdx.x * 32, tb64 - 100000ull);
            if (blockIdx.x == NR) st_dev(gofs + 16, tb64 - 100000ull);
            sm.slip = 0;
            sm.req = 0;
            sm.done = 0;
            sm.prog = 0;
            sm.fcnt = 0;
            sm.epoch = tb64;
            sm.have = blockIdx.x == 0;
        }
        if (l < NH + NH / 4) sm.cnt[l] = 0;
        sm.vote[l] = 0;
        until(tb + TGATH);
        unsigned ld;
        for (;;) {
            const u64 dt = ld_dev(tstart + NWG - 8 + (l & 7)) - tb64 + 1000ull;
            ld = dt < 2000ull ? (unsigned)dt : 0u;
            if (__all(dt < 2000ull) || clk() - tb > (unsigned)TGMAX) break;
        }
        ld = max(ld, dppu<0x124>(ld));
        ld = max(ld, dppu<0x122>(ld));
        ld = max(ld, dppu<0x121>(ld));
        ld = __builtin_amdgcn_readfirstlane(ld);
        const u64 t64 = ld ? tb64 + ld - 1000ull : tb64;
        if (l == 0) sm.t0 = (unsigned)t64;
        if (l == 0) sm.dsv = clk() - tb;
        __syncthreads();
        asm volatile("" :: "v"(warm));
        u64 E = tb64, to[NWG / 64];
        bool have = blockIdx.x == 0;
        if (have) {
#pragma unroll
            for (int u = 0; u < NWG / 64; ++u) to[u] = ld_dev(tstart + u * 64 + l);
            asm volatile("s_waitcnt vmcnt(0)" ::: "memory");
#pragma unroll
            for (int u = 0; u < NWG / 64; ++u) st_dev(echo + u * 64 + l, to[u]);
        }
        auto chores = [&]() {
            if (blockIdx.x == 0) {
#pragma unroll
                for (int u = 0; u < NWG / 64; ++u) {
                    const u64 x = ld_dev(tstart + u * 64 + l);
                    if (x != to[u]) { st_dev(echo + u * 64 + l, x); to[u] = x; }
                }
            } else if (!have && ld_dev(echo + blockIdx.x) == tb64) {
                asm volatile("" ::: "memory");
                E = ld_dev(tstart);
                have = true;
                if (l == 0) sm.epoch = E;
                asm volatile("s_waitcnt lgkmcnt(0)" ::: "memory");
                if (l == 0) st_wg(&sm.have, 1u);
            }
        };
        const u64* mine = gofs + ((blockIdx.x >> 3) % NR) * 32;
        unsigned sent = 0, stage = 0, tsh = clk();
        while (stage < 2) {
            const u64 gd = ld_dev(mine) - t64;
            const unsigned go = gd <= (u64)MAXOFF ? (unsigned)gd : 0u;
            const unsigned own = ld_wg(&sm.slip);
            if (go > own && l == 0) __hip_atomic_fetch_max(&sm.slip, go, __ATOMIC_RELAXED, __HIP_MEMORY_SCOPE_WORKGROUP);
            if (own <= go) tsh = clk();
            else if (clk() - tsh > (unsigned)REVT && blockIdx.x >= NSENT) { if (l == 0) st_wg(&sm.slip, go); }
            const unsigned req = ld_wg(&sm.req);
            unsigned want = blockIdx.x < NSENT ? own : 0u;
            if (req == go + SS && own == go) {
                unsigned x = ld_wg(&sm.late[l & 7]);
                x += dppu<0x121>(x);
                x += dppu<0x122>(x);
                x += dppu<0x124>(x);
                want = min(req + (__builtin_amdgcn_readfirstlane(x) * LFAC / 64 & ~15u), (unsigned)MAXOFF);
            }
            if (want > go && want > sent) {
                u64 old = 0;
                if (l == 0) old = __hip_atomic_fetch_max(gofs + 16, t64 + want, __ATOMIC_RELAXED, __HIP_MEMORY_SCOPE_AGENT);
                if (__builtin_amdgcn_readfirstlane((unsigned)(old < t64 + want)) && l < NR)
                    __hip_atomic_fetch_max(gofs + l * 32, t64 + want, __ATOMIC_RELAXED, __HIP_MEMORY_SCOPE_AGENT);
                sent = want;
            }
            chores();
            __builtin_amdgcn_s_sleep(PSLEEP);
            if (ld_wg(&sm.done) > stage) {
                ++stage;
                asm volatile("" : "+s"(code));
                int w = 0;
#pragma unroll
                for (int u = 0; u < 3; ++u) {
                    const int a = (u * 64 + l) * 128;
                    w ^= *(gptr)(code + (a < codelen ? a : 0));
                }
                asm volatile("" :: "v"(w));
            }
        }
        while (!have && clk() - tb < TMO) chores();
        if (l == 0) st_wg(&sm.have, 1u);
        if (blockIdx.x == 0) {
            for (int spin = 1; ld_wg(&sm.fcnt) < WPB; ++spin) {
                __builtin_amdgcn_s_sleep(2);
                if (spin % 1024 == 0) chores();
            }
        }
        return;
    }
    if (__builtin_expect(wave == WPB + 1, 0)) {
        {
            const int k = blockIdx.x >> 3;
            const char* pg = k < 3 ? (const char*)kz + ((size_t)k << 21) : k == 3 ? (const char*)vs : k == 4 ? (const char*)vz
                           : k < 7 ? (const char*)ks + ((size_t)(k - 4) << 21) : (const char*)qk + ((size_t)(k - 7) << 21);
            unsigned x;
            asm volatile("s_load_dword %0, %1, 0x0\n\ts_waitcnt lgkmcnt(0)" : "=s"(x) : "s"(pg + (blockIdx.x & 7) * 256) : "memory");
        }
        __syncthreads();
        asm volatile("" :: "v"(warm));
#pragma nounroll
        for (int i = 0; i < 26; ++i) {
            const size_t pg = (size_t)((blockIdx.x >> 3) + 32 * (i % 12)) << 21;
            const char* tp = i < 12 ? (const char*)qk + pg : i < 24 ? (const char*)qv + pg : i == 24 ? (const char*)ws + ((size_t)((blockIdx.x >> 3) & 1) << 21) : (const char*)out;
            if (i < 25) tp += (((blockIdx.x >> 3) * 29 + i) & 127) * 256;
            unsigned x;
            asm volatile("s_load_dword %0, %1, 0x0\n\ts_waitcnt lgkmcnt(0)" : "=s"(x) : "s"(tp) : "memory");
        }
#pragma nounroll
        for (int c = 0; c < NH / 4; ++c) {
            while (ld_wg(&sm.cnt[NH + c]) < WPB) __builtin_amdgcn_s_sleep(8);
            asm volatile("" ::: "memory");
            v4i* pp = reinterpret_cast<v4i*>(&sm.p[c * 4][0][0]) + lane;
            const v4i u = *pp;
            float x[8];
#pragma unroll
            for (int e = 0; e < 4; ++e) { x[2 * e] = bf_lo(u[e]); x[2 * e + 1] = bf_hi(u[e]); }
            float M = fmaxf(fmaxf(fmaxf(x[0], x[1]), fmaxf(x[2], x[3])), fmaxf(fmaxf(x[4], x[5]), fmaxf(x[6], x[7])));
            M = fmaxf(M, dpp<0x128>(M));
            M = fmaxf(M, dpp<0x124>(M));
            M = fmaxf(M, dpp<0x122>(M));
            M = fmaxf(M, dpp<0x121>(M));
            v4i o;
            float L = 0.f;
#pragma unroll
            for (int e = 0; e < 4; ++e) {
                const unsigned pk = pack(__expf(x[2 * e] - M), __expf(x[2 * e + 1] - M));
                o[e] = (int)pk;
                L += bf_lo(pk) + bf_hi(pk);
            }
            *pp = o;
            L = rowsum16(L);
            if (r == 0) sm.ml[c * 4 + g] = __float_as_uint(M) | __builtin_bit_cast(unsigned short, (_Float16)L);
            asm volatile("" ::: "memory");
            if (lane == 0) st_wg(&sm.prog, c + 1);
        }
        while (ld_wg(&sm.have) == 0) __builtin_amdgcn_s_sleep(8);
        asm volatile("" ::: "memory");
        const unsigned tag = tagof(sm.epoch, lane & 3);
        float* mine = ws + (size_t)blockIdx.x * NH * RS;
#pragma nounroll
        for (int h = 0; h < NH; ++h) {
            const unsigned mlb = (sm.ml[h] >> (lane & 3) * 8) & 0xffu;
            if (h < NH - 1) { while (ld_wg(&sm.cnt[h]) < WPB) __builtin_amdgcn_s_sleep(4); }
            else { while (ld_wg(&sm.cnt[h]) < WPB) {} }
            asm volatile("" ::: "memory");
            v2f c[4][3];
#pragma unroll
            for (int e = 0; e < 4; ++e)
#pragma unroll
                for (int k = 0; k < 3; ++k) c[e][k] = *reinterpret_cast<const v2f*>(&sm.stage[h & 1][(lane >> 5) * 4 + e][(lane & 31) * 6 + k * 2]);
            v4i x;
#pragma unroll
            for (int k = 0; k < 3; ++k) {
                const v2f a = ((c[0][k] + c[1][k]) + (c[2][k] + c[3][k])) * 0x1p22f;
                x[k] = (int)pack(fold32(a.x, a.x), fold32(a.y, a.y));
            }
            x[3] = (int)(tag | mlb);
            if (h == NH - 1) {
                if (lane < 32) st_rec(mine + h * RS, lane * 16, x);
                break;
            }
            sm.rec[h][lane & 31] = x;
            asm volatile("" ::: "memory");
            if (lane == 0) st_wg(&sm.prog, NH / 4 + h + 1);
            if (h == NH - 2) {
                v4i y[NH / 2];
#pragma unroll
                for (int u = 0; u < NH / 2; ++u) y[u] = (&sm.rec[0][0])[u * 64 + lane];
                until(ld_wg(&sm.tref) + TREL);
#pragma unroll
                for (int u = 0; u < NH / 2; ++u)
                    if (u < NH / 2 - 1 || lane < 32) st_rec(mine + u * 2 * RS, lane * 16, y[u]);
            }
        }
        return;
    }

    const int ww = (wave & 3) * 2 + (wave >> 2);
    const int ti = ww * 4 + g, tl = ti & 15;
    const int tev = (ti >> 4) * 32 + tl + (tl >= 5 ? 6 : 0) + (tl >= 10 ? 5 : 0);
    const int tod = (ti >> 4) * 32 + 5 + tl + (tl >= 6 ? 5 : 0) + (tl >= 11 ? 6 : 0);
    constexpr size_t ITER = (size_t)GST * 64 * DH * 4;
    constexpr size_t HEAD = (size_t)SKV * DH * 4;
    auto odd = [&](int S, int i) { return (i ^ (pos / (NWG / 2)) ^ ((6 * (S * 2 * NWG + g0 + (i >> 1) * GST) + 3) >> 14)) & 1; };
    const unsigned offe = tev * (DH * 4) + r * 16, offo = tod * (DH * 4) + r * 16;
    const char* kp = reinterpret_cast<const char*>(qk) + (size_t)g0 * (64 * DH * 4);
    const char* vp = reinterpret_cast<const char*>(qv) + (size_t)g0 * (64 * DH * 4);
    const char* dummy = reinterpret_cast<const char*>(qv) + (size_t)(blockIdx.x * WPB + wave) * 4864;

    v4i buf[RING];
    auto burst = [&](const char* p, int i, unsigned o) {
        p += (size_t)(i >> 1) * ITER;
#pragma unroll
        for (int e = 0; e < 3; ++e) { buf[i * 3 + e] = ldstream(p + o + e * 256); SB(); }
    };

    const unsigned phase = ((pos & (NWG / 2 - 1)) * (BODY16 / 512) + ww * WST * (BODY16 / (12 * 256))) >> 16;
    unsigned base = tb + phase + PSH;
    unsigned sched = 0, off = 0, offv = 0, ex = 0;
    int prev = 0, now = 0, hold = 0, big = 0, body = 0;
    unsigned vc = 0;
    int d_l = 0, d_n = 0, d_f = 0, d_s = 0, d_nb = 0;
    unsigned d_bar = 0, d_x = 0;
    auto pace = [&](int due) {
        int now;
        while ((now = (int)(clk() - base)) < due) __builtin_amdgcn_s_sleep(1);
        return now;
    };
    auto follow = [&](unsigned o2) {
        hold = o2 - off > SLIP / 2 ? HOLDB : o2 != off ? HOLD : max(hold - 1, 0);
        big = o2 - off > SLIP / 2 ? HOLDB : max(big - 1, 0);
        if ((int)(o2 - off) >= SLIP) ++d_nb;
        base += o2 - off;
        off = o2;
    };
    auto pacen = [&](auto N) {
        constexpr int n = decltype(N)::value;
        follow(__builtin_amdgcn_readfirstlane(offv));
        now = pace(slot(n));
        vc = inflight();
        if (__builtin_expect(vc > 3, 0)) {
            asm volatile("s_waitcnt vmcnt(3)");
            now = (int)(clk() - base);
            if (now > slot(n) + THR && prev < PRV && big == 0 && off + SLIP <= MAXOFF && body >= NOSLK) {
                if (lane == 0 && __hip_atomic_fetch_add(&sm.vote[body], 1u, __ATOMIC_RELAXED, __HIP_MEMORY_SCOPE_WORKGROUP) + 1 >= VOTES)
                    __hip_atomic_fetch_max(&sm.slip, off + SLIP, __ATOMIC_RELAXED, __HIP_MEMORY_SCOPE_WORKGROUP);
                unsigned o2;
                while ((o2 = __builtin_amdgcn_readfirstlane(ld_wg(&sm.slip))) == off && (int)(clk() - base) < now + VWAIT) {}
                if (o2 != off) { follow(o2); now = pace(slot(n)); }
            }
            if (hold == 0 && off + SS <= MAXOFF && body >= NOSM && lane == 0) st_wg(&sm.req, off + SS);
        }
        if (d_n >= BA && d_n < BB) d_l = max(d_l, now - slot(n));
        if (now - slot(n) > 30 && d_f == 0) d_f = d_n + 1;
        if (off > 0 && d_s == 0) d_s = d_n + 1;
        ++d_n;
    };
    auto check = [&](auto N) {
        constexpr int n = decltype(N)::value;
        base += ex;
        prev = now - slot(n);
        if (lane == 0) st_wg(&sm.late[wave], (unsigned)prev);
    };
    auto getslip = [&]() { offv = __hip_atomic_load(&sm.slip, __ATOMIC_RELAXED, __HIP_MEMORY_SCOPE_WORKGROUP); };
    auto arrive = [&](int i) {
        asm volatile("" ::: "memory");
        if (lane == 0) __hip_atomic_fetch_add(&sm.cnt[i], 1u, __ATOMIC_RELAXED, __HIP_MEMORY_SCOPE_WORKGROUP);
    };
    auto next_body = [&]() {
        const unsigned s2 = sched + BODY16;
        base += (s2 >> 16) - (sched >> 16);
        sched = s2;
        ++body;
        if (DIAG == 14 && body == 10) d_bar = base - (sched >> 16) - d_nb * SLIP;
    };

    {
        v4i pv[2], pq[2];
        pq[0] = reinterpret_cast<const v4i*>(q)[tid];
        pq[1] = reinterpret_cast<const v4i*>(q)[NT + (tid & 255)];
        const unsigned vo = (((tid >> 4) * (2 * NWG) + ((tid >> 3) & 1) * GST + g0) * 64 + (tid & 7) * 8) * 2;
        PST(1); pace(slot(3)); SB();
        pk[3] = ldstream((const char*)kz + po[0]);
        pk[4] = ldstream((const char*)kz + po[1]);
        pk[5] = ldstream((const char*)kz + po[2]);
        PST(2); pace(slot(6)); SB();
        pv[0] = ldstream((const char*)vs + vo);
        pv[1] = ldstream((const char*)vz + vo);
        PST(3); pace(slot(8)); SB();
        burst(kp, 0, odd(0, 0) ? offo : offe);
        PST(4);
        sm.qt[tid] = pq[0];
        if (tid < 256) sm.qt[NT + tid] = pq[1];
#pragma unroll
        for (int u = 0; u < 3; ++u) sm.ksz[dsti[u]] = pk[u];
#pragma unroll
        for (int u = 0; u < 3; ++u) {
            const v4i x = pk[3 + u];
            sm.ksz[dsti[u] + 24] = v4i{(int)negscale(x.x), (int)negscale(x.y), (int)negscale(x.z), (int)negscale(x.w)};
        }
        PST(5); pace(slot(11)); SB();
        burst(kp, 1, odd(0, 1) ? offo : offe);
        PST(6);
        sm.vsz[tid] = pv[0];
        sm.vsz[NT + tid] = v4i{(int)negscale(pv[1].x), (int)negscale(pv[1].y), (int)negscale(pv[1].z), (int)negscale(pv[1].w)};
    }
    asm volatile("" :: "v"(warm));
    PST(7);
    __syncthreads();
    PST(8);
    if (DST == 20) d_x = sm.dsv;
    d_bar = clk();
    base = __builtin_amdgcn_readfirstlane(sm.t0) + OFF0 + phase;

    const v2f huge = {0x1p127f, 0x1p127f};

    for (int S = 0; S < NH; ++S) {
        ex = S < NSLOW ? EXK : S == 21 ? G21 : 0;
        v2f kz2[6];
        unsigned slo[6], shi[6];
        v2u qq[3];
        const char* nsup = (S + 1 < NH) ? kp + HEAD : vp;
        float mylogit = 0.f;
        float dot = 0.f;
        sfor<12>([&](auto N) {
            constexpr int n = decltype(N)::value;
            constexpr int i = n / 3, j = n % 3;
            if (j == 0) {
                const unsigned o = odd(i < 2 ? S : (S + 1) % NH, i ^ 2) ? offo : offe;
                pacen(N);
                SB();
                burst(i < 2 ? kp : nsup, i ^ 2, o);
                SB();
                check(N);
            }
            if (j == 2) getslip();
            if (i % 2 == 0) {
                const v2u* pk = reinterpret_cast<const v2u*>(&sm.ksz[(S * 2 + n / 6) * 48]);
                const v2u a = pk[j * 16 + r], b = pk[48 + j * 16 + r];
                slo[j * 2 + 0] = a.x & 0xffffu; shi[j * 2 + 0] = a.x & 0xffff0000u;
                slo[j * 2 + 1] = a.y & 0xffffu; shi[j * 2 + 1] = a.y & 0xffff0000u;
                kz2[j * 2 + 0] = v2f{bf_lo(b.x), bf_hi(b.x)};
                kz2[j * 2 + 1] = v2f{bf_lo(b.y), bf_hi(b.y)};
            }
            if (i == 0) qq[j] = reinterpret_cast<const v2u*>(&sm.qt[S * 24])[j * 16 + r];
            if (j == 0) dot = 0.f;
            {
                const v4i x = buf[n % RING];
                const v2f t0 = __builtin_elementwise_fma(v2f{__int_as_float(x.x), __int_as_float(x.y)}, huge, kz2[j * 2]);
                const v2f t1 = __builtin_elementwise_fma(v2f{__int_as_float(x.z), __int_as_float(x.w)}, huge, kz2[j * 2 + 1]);
                float y[4];
                mul4(y, pack(t0.x, t0.y), pack(t1.x, t1.y), slo[j * 2], shi[j * 2], slo[j * 2 + 1], shi[j * 2 + 1]);
                dot = dot2(pack(y[0], y[1]), qq[j].x, dot);
                dot = dot2(pack(y[2], y[3]), qq[j].y, dot);
            }
            if (j == 2) {
                const float d = rowsum16(dot);
                if ((r & 3) == i) mylogit = d;
            }
            asm volatile("" : "+v"(dot), "+v"(mylogit));
            SB();
        });
        if (r < 4) sm.p[S][wave][g * 4 + r] = (unsigned short)pack(rb(mylogit * 0x1p22f) * scale, 0.f);
        if ((S & 3) == 3) arrive(NH + (S >> 2));
        if (S == NH - 3 && tid == 0) st_wg(&sm.done, 1u);
        kp = nsup;
        next_body();
    }
    base += GAP1;

    for (int S = 0; S < NH; ++S) {
        ex = S == 21 ? G21 : 0;
        v2u P;
        unsigned vsl, vsh, vzf;
        v4f acc = {0.f, 0.f, 0.f, 0.f};
        float y0[12];
        unsigned b0[12];
        const bool more = S + 1 < NH;
        const char* nsup = more ? vp + HEAD : dummy;
        unsigned sl, sh;
        v2f z2;
        sfor<12>([&](auto N) {
            constexpr int n = decltype(N)::value;
            constexpr int i = n / 3, j = n % 3;
            if (j == 0) {
                unsigned o = odd(i < 2 ? S : S + 1, i ^ 2) ? offo : offe;
                if (i >= 2 && !more) o = i * 768;
                if (i < 2 || more) pacen(N);
                SB();
                burst(i < 2 ? vp : nsup, i ^ 2, o);
                SB();
                check(N);
            }
            if (n == 0) {
                const unsigned need = S < 2 ? 1 : NH / 4 + S - 1;
                while (__builtin_amdgcn_readfirstlane(ld_wg(&sm.prog)) < need) {}
                asm volatile("" ::: "memory");
                P = *reinterpret_cast<const v2u*>(&sm.p[S][wave][g * 4]);
                const int ltok = S * 128 + ((r >> 1) & 1) * 64 + (odd(S, r & 3) ? tod : tev);
                const unsigned short* pv = reinterpret_cast<const unsigned short*>(&sm.vsz[0]);
                vsl = pv[ltok];
                vsh = vsl << 16;
                vzf = (unsigned)pv[4096 + ltok] << 16;
            }
            if (j == 2) getslip();
            if (j == 0) {
                sl = dppu<0x55 * i>(vsl);
                sh = dppu<0x55 * i>(vsh);
                const float zb = __uint_as_float(dppu<0x55 * i>(vzf));
                z2 = v2f{zb, zb};
            }
            float y[4];
            {
                const v4i x = buf[n];
                const v2f t0 = __builtin_elementwise_fma(v2f{__int_as_float(x.x), __int_as_float(x.y)}, huge, z2);
                const v2f t1 = __builtin_elementwise_fma(v2f{__int_as_float(x.z), __int_as_float(x.w)}, huge, z2);
                mul4(y, pack(t0.x, t0.y), pack(t1.x, t1.y), sl, sh, sl, sh);
            }
#pragma unroll
            for (int e = 0; e < 4; ++e) {
                const int idx = j * 4 + e;
                if (i % 2 == 0) {
                    y0[idx] = y[e];
                    asm volatile("" : "+v"(y0[idx]));
                } else if (i == 1) {
                    b0[idx] = pack(y0[idx], y[e]);
                    asm volatile("" : "+v"(b0[idx]));
                } else {
                    const v2u B = {b0[idx], pack(y0[idx], y[e])};
                    const v2u A = {r == idx ? P.x : 0u, r == idx ? P.y : 0u};
                    acc = __builtin_amdgcn_mfma_f32_16x16x16bf16_1k(__builtin_bit_cast(v4s, A), __builtin_bit_cast(v4s, B), acc, 0, 0, 0);
                }
            }
            if (n == 11) {
                if (S == NH - 2 && tid == 0) sm.tref = base + slot(12) + slot(3);
                if (S == NH - 3 && tid == 0) st_wg(&sm.done, 2u);
                if (lane < 48) *reinterpret_cast<v4f*>(&sm.stage[S & 1][wave][lane * 4]) = acc;
                arrive(S);
            }
            SB();
        });
        vp = nsup;
        next_body();
    }
    const unsigned d_t1 = clk();
    {
        const int mh = (blockIdx.x / 16 * 2 + (blockIdx.x & 7) / 4 + NH - 1) % NH, msl = (blockIdx.x & 3) * 2 + ((blockIdx.x >> 3) & 1);
        const unsigned tag = tagof(sm.epoch, lane & 3);
        const float* src = ws + ((size_t)wave * 32 * NH + mh) * RS + msl * 16;
        const unsigned so = (lane >> 2) * (NH * RS * 4) + (lane & 3) * 16;
        until(base - slot(12) + slot(3) - phase + wave * STG + (mh == NH - 1 ? TPL : TPC));
        v4i x[2];
        for (;;) {
            x[0] = ld_rec(src, so);
            x[1] = ld_rec(src + 16 * NH * RS, so);
            asm volatile("s_waitcnt vmcnt(0)" : "+v"(x[0]), "+v"(x[1]));
            if (__all((((unsigned)x[0].w ^ tag) | ((unsigned)x[1].w ^ tag)) < 256u) || clk() - tb >= TMO) break;
            __builtin_amdgcn_s_sleep(PSL);
        }
#pragma unroll
        for (int d = 0; d < 6; ++d) asm volatile("" :: "v"(buf[d]));
        unsigned ml[2];
#pragma unroll
        for (int k = 0; k < 2; ++k) {
            ml[k] = ((unsigned)x[k].w ^ tag) << (lane & 3) * 8;
            ml[k] |= dppu<0xB1>(ml[k]);
            ml[k] |= dppu<0x4E>(ml[k]);
        }
        const float M = x4max(fmaxf(bf_hi(ml[0]), bf_hi(ml[1])));
        const float w0 = __expf(bf_hi(ml[0]) - M), w1 = __expf(bf_hi(ml[1]) - M);
        float a[6];
#pragma unroll
        for (int e = 0; e < 3; ++e) {
            a[2 * e] = bf_lo(x[0][e]) * w0 + bf_lo(x[1][e]) * w1;
            a[2 * e + 1] = bf_hi(x[0][e]) * w0 + bf_hi(x[1][e]) * w1;
        }
        const float l = (float)__builtin_bit_cast(_Float16, (unsigned short)ml[0]) * w0 + (float)__builtin_bit_cast(_Float16, (unsigned short)ml[1]) * w1;
        float c0 = fold16(fold32(a[0], a[4]), fold32(a[2], l));
        float c1 = fold16(fold32(a[1], a[5]), fold32(a[3], l));
        c0 += dpp<0x124>(c0); c1 += dpp<0x124>(c1);
        c0 += dpp<0x128>(c0); c1 += dpp<0x128>(c1);
        if (g == 3) c1 = M;
        if (r < 4) *reinterpret_cast<v2f*>(&sm.fin[wave][g == 3 ? 24 + r * 2 : r * 6 + g * 2]) = v2f{c0, c1};
        asm volatile("" ::: "memory");
        unsigned arr = 0;
        if (lane == 0) arr = __hip_atomic_fetch_add(&sm.fcnt, 1u, __ATOMIC_RELAXED, __HIP_MEMORY_SCOPE_WORKGROUP);
        asm volatile("" ::: "memory");
        const v4f y = *reinterpret_cast<const volatile v4f*>(&sm.fin[lane >> 3][(lane & 7) * 4]);
        const float mw = *reinterpret_cast<const volatile float*>(&sm.fin[lane >> 3][25]);
        const float w = __expf(mw - x4max(mw));
        float d = fold16(fold32(y.x * w, y.z * w), fold32(y.y * w, y.w * w));
        d += dpp<0x128>(d);
        const float L = __int_as_float(__builtin_amdgcn_readlane(__float_as_int(d), 6));
        const unsigned o = pack(d / L, 0.f);
        asm volatile("" :: "v"(o));
        if (__builtin_amdgcn_readfirstlane(arr) == WPB - 1) {
            if (r < 6) reinterpret_cast<unsigned short*>(out)[mh * DH + msl * SLC + r * 4 + g] = (unsigned short)o;
        }
    }
    {
        unsigned* dg = reinterpret_cast<unsigned*>(ws + WS_T + 65536) + (blockIdx.x * WPB + wave) * 8;
        if (lane == 0) { dg[0] = tb; dg[1] = d_t1; dg[2] = clk(); dg[3] = off; dg[4] = DST ? d_x : d_l; dg[5] = d_f; dg[6] = d_bar - phase; dg[7] = DIAG >= 12 ? d_nb : d_s; }
    }
}
__global__ void kivi_end_marker() { asm volatile("kivi_code_end:"); }

__global__ void diag_kernel(const float* ws) {
    const unsigned* dg = reinterpret_cast<const unsigned*>(ws + WS_T + 65536);
    const unsigned ref = dg[0];
    int mn = 1 << 30, x = -(1 << 30), cnt = 0;
    for (int b = 0; b < 8; ++b) {
        const unsigned* e = dg + (b * 256 + threadIdx.x) * 8;
        mn = min(mn, (int)(e[0] - ref));
        const int v = DIAG == 2 ? (int)(e[0] - ref) : DIAG == 3 ? (int)e[3] : DIAG == 4 ? (int)(e[1] - ref - e[3]) : DIAG == 5 ? (int)(e[2] - ref)
                    : DIAG == 12 ? (int)(e[2] - ref - e[7] * SLIP) : DIAG == 13 ? (int)(e[3] - e[7] * SLIP) : DIAG == 6 ? (int)e[4] : DIAG == 7 || DIAG == 14 ? (int)(e[6] - ref) : DIAG == 8 ? (e[5] ? 1000 - (int)e[5] : 0) : DIAG == 9 ? (e[7] ? 1000 - (int)e[7] : 0) : 0;
        x = max(x, v);
        cnt += DIAG == 11 ? (int)e[4] : (int)e[4] > 30;
    }
    __shared__ int smn, smx, sc;
    if (threadIdx.x == 0) { smn = 1 << 30; smx = -(1 << 30); sc = 0; }
    __syncthreads();
    atomicMin(&smn, mn); atomicMax(&smx, x); atomicAdd(&sc, cnt);
    __syncthreads();
    mn = smn; x = smx;
    int X = 0;
    if (DIAG == 2 || DIAG == 7 || DIAG == 14) X = x - mn;
    if (DIAG == 3 || DIAG == 6 || DIAG == 8 || DIAG == 9) X = x;
    if (DIAG == 4 || DIAG == 5 || DIAG == 12) X = x - mn - 20000;
    if (DIAG == 13) X = x;
    if (DIAG == 10) X = sc;
    if (DIAG == 11) X = sc / 2048;
    X = max(0, min(X, 3000));
    while ((int)(clk() - ref) < mn + 25000 + X * 10) __builtin_amdgcn_s_sleep(1);
}
extern "C" void kernel_launch(void* const* d_in, const int* in_sizes, int n_in,
                              void* d_out, int out_size, void* d_ws, size_t ws_size,
                              hipStream_t stream) {
    const __hip_bfloat16* q = (const __hip_bfloat16*)d_in[0];
    const int* qk = (const int*)d_in[1];
    const int* qv = (const int*)d_in[2];
    const __hip_bfloat16* ks = (const __hip_bfloat16*)d_in[3];
    const __hip_bfloat16* kz = (const __hip_bfloat16*)d_in[4];
    const __hip_bfloat16* vs = (const __hip_bfloat16*)d_in[5];
    const __hip_bfloat16* vz = (const __hip_bfloat16*)d_in[6];
    const float scale = (float)(1.0 / sqrt((double)DH));
    kivi_stream_kernel<<<NWG, NT + 128, 0, stream>>>(q, qk, qv, ks, kz, vs, vz, (float*)d_ws, (__hip_bfloat16*)d_out, scale);
    diag_kernel<<<1, 256, 0, stream>>>((const float*)d_ws);
}
```

```cpp
#include <hip/hip_runtime.h>
#include <hip/hip_bf16.h>
#include <stdint.h>
#include <utility>

#define TGT 199.25
#define WST 16
#define THR 80
#define PRV 16
#define SLIP 250
#define MAXOFF 3000
#define NSENT 8
#define NR 32
#define TPL 260
#define STG 0
#define PSLEEP 16
#define OFF0 380
#define TGATH 150
#define GAP1 10
#define TREL 90
#define NSLOW 1
#define EXK 10
#define NOSLK 2
#define TMO 100000000u
#define TPC 260
#define PSL 2
#define TGMAX 300
#define PSH 0
#define SS 20
#define HOLD 4
#define VOTES 4
#define HOLDB 8
#define REVT 600
#define VWAIT 30
#define LFAC 8
#define NOSM 2
#define H0 6
#define TGS 202.25
#define SPL 2
#define G21 5
#define EXS(i) ((int)(((i) + 1) * (TGS - TGT) * 100.0 / 256.0 + 0.5) - (int)((i) * (TGS - TGT) * 100.0 / 256.0 + 0.5))
#define CAL0 128
#define NRND 4
#define PTH 1000
#define TPRB 150
#define DBM 7
#define DBN 2
#define TQA 500
#define PB 1
#define TLEAD 14
#define GAP2 10
#define NG2 3
#define TECHO 550
#define TQ 300
#define TTAB 1400
#define TXW 200
#define FMASK 0xffffffffu
#define FPOL 0u
#define RING 12
#define SB() __builtin_amdgcn_sched_barrier(0)
#define PST(i) SB()

typedef int v4i __attribute__((ext_vector_type(4)));
typedef unsigned v2u __attribute__((ext_vector_type(2)));
typedef float v2f __attribute__((ext_vector_type(2)));
typedef float v4f __attribute__((ext_vector_type(4)));
typedef short v4s __attribute__((ext_vector_type(4)));
typedef __bf16 v2bf __attribute__((ext_vector_type(2)));

constexpr int DH = 192;
constexpr int NH = 32;
constexpr int SKV = 32768;
constexpr int WPB = 8;
constexpr int NT = WPB * 64;
constexpr int NWG = 256;
constexpr int NSL = 8, SLC = 24;
constexpr int RS = NSL * 16;
static_assert(SKV == 2 * 64 * NWG, "two 64-token groups per workgroup and head");
constexpr int NX = 4;
constexpr int NRC = 17;
constexpr size_t WS_X = (size_t)NRC * NWG * RS;
constexpr size_t WS_T = WS_X + (size_t)NWG * 256;
constexpr unsigned BODY16 = (unsigned)(TGT * 100.0 / 64.0 * 65536.0);
constexpr int slot(int n) { return (int)((n * (BODY16 / 12)) >> 16); }

template <typename F, int... I>
__device__ __forceinline__ void sfor_impl(F&& f, std::integer_sequence<int, I...>) {
    (f(std::integral_constant<int, I>{}), ...);
}
template <int N, typename F>
__device__ __forceinline__ void sfor(F&& f) {
    sfor_impl(f, std::make_integer_sequence<int, N>{});
}

__device__ __forceinline__ float bf_lo(unsigned u) { return __uint_as_float(u << 16); }
__device__ __forceinline__ float bf_hi(unsigned u) { return __uint_as_float(u & 0xffff0000u); }
__device__ __forceinline__ float rb(float x) { return (float)(__bf16)x; }
__device__ __forceinline__ unsigned pack(float lo, float hi) {
    return __builtin_bit_cast(unsigned, __builtin_convertvector(v2f{lo, hi}, v2bf));
}
__device__ __forceinline__ float dot2(unsigned a, unsigned b, float c) {
    return __builtin_amdgcn_fdot2_f32_bf16(__builtin_bit_cast(v2bf, a), __builtin_bit_cast(v2bf, b), c, false);
}
__device__ __forceinline__ void mul4(float (&y)[4], unsigned a, unsigned b, unsigned s0, unsigned s1, unsigned s2, unsigned s3) {
    asm("v_dot2_f32_bf16 %0, %4, %6, 0\n\tv_dot2_f32_bf16 %1, %4, %7, 0\n\t"
        "v_dot2_f32_bf16 %2, %5, %8, 0\n\tv_dot2_f32_bf16 %3, %5, %9, 0\n\ts_nop 2"
        : "=&v"(y[0]), "=&v"(y[1]), "=&v"(y[2]), "=&v"(y[3])
        : "v"(a), "v"(b), "v"(s0), "v"(s1), "v"(s2), "v"(s3));
}
__device__ __forceinline__ unsigned clk() { return (unsigned)__builtin_amdgcn_s_memrealtime(); }
__device__ __forceinline__ unsigned inflight() {
    unsigned x;
    asm volatile("s_getreg_b32 %0, hwreg(HW_REG_IB_STS, 0, 4)" : "=s"(x));
    return x;
}
__device__ __forceinline__ unsigned negscale(unsigned u) { return pack(bf_lo(u) * -0x1p-22f, bf_hi(u) * -0x1p-22f); }

template <int CTRL>
__device__ __forceinline__ unsigned dppu(unsigned x) {
    return (unsigned)__builtin_amdgcn_mov_dpp((int)x, CTRL, 0xf, 0xf, true);
}
template <int CTRL>
__device__ __forceinline__ float dpp(float x) { return __uint_as_float(dppu<CTRL>(__float_as_uint(x))); }
__device__ __forceinline__ float rowsum16(float x) {
    x += dpp<0x128>(x);
    x += dpp<0x124>(x);
    x += dpp<0x122>(x);
    x += dpp<0x121>(x);
    return x;
}
__device__ __forceinline__ v4i ldstream(const void* p) {
    return __builtin_nontemporal_load(reinterpret_cast<const v4i*>(p));
}

__device__ __forceinline__ void st_rec(void* p, unsigned o, v4i a) {
    asm volatile("s_nop 4\n\tglobal_store_dwordx4 %0, %1, %2 sc1\n\ts_nop 1" :: "v"(o), "v"(a), "s"(p) : "memory");
}
__device__ __forceinline__ v4i ld_rec(const void* p, unsigned o) {
    v4i x;
    asm volatile("s_nop 4\n\tglobal_load_dwordx4 %0, %1, %2 sc1" : "=v"(x) : "v"(o), "s"(p) : "memory");
    return x;
}
typedef unsigned long long u64;
__device__ __forceinline__ u64 ld_dev(const u64* p) { return __hip_atomic_load(p, __ATOMIC_RELAXED, __HIP_MEMORY_SCOPE_AGENT); }
__device__ __forceinline__ void st_dev(u64* p, u64 v) { __hip_atomic_store(p, v, __ATOMIC_RELAXED, __HIP_MEMORY_SCOPE_AGENT); }
__device__ __forceinline__ unsigned ld_wg(const unsigned* p) { return __hip_atomic_load(p, __ATOMIC_RELAXED, __HIP_MEMORY_SCOPE_WORKGROUP); }
__device__ __forceinline__ void st_wg(unsigned* p, unsigned v) { __hip_atomic_store(p, v, __ATOMIC_RELAXED, __HIP_MEMORY_SCOPE_WORKGROUP); }
__device__ __forceinline__ float fold32(float a, float b) {
    auto s = __builtin_amdgcn_permlane32_swap(__float_as_uint(a), __float_as_uint(b), false, false);
    return __uint_as_float(s[0]) + __uint_as_float(s[1]);
}
__device__ __forceinline__ float fold16(float a, float b) {
    auto s = __builtin_amdgcn_permlane16_swap(__float_as_uint(a), __float_as_uint(b), false, false);
    return __uint_as_float(s[0]) + __uint_as_float(s[1]);
}
__device__ __forceinline__ float x4max(float x) {
    x = fmaxf(x, dpp<0x124>(x));
    x = fmaxf(x, dpp<0x128>(x));
    auto s = __builtin_amdgcn_permlane16_swap(__float_as_uint(x), __float_as_uint(x), false, false);
    x = fmaxf(__uint_as_float(s[0]), __uint_as_float(s[1]));
    auto t = __builtin_amdgcn_permlane32_swap(__float_as_uint(x), __float_as_uint(x), false, false);
    return fmaxf(__uint_as_float(t[0]), __uint_as_float(t[1]));
}

struct __attribute__((aligned(16))) Smem {
    v4i ksz[(64 + NX) * 48];
    v4i qt[NH * 24];
    v4i vsz[2][(64 + NX) * 8];
    v4i rec[NRC - 1][RS / 4];
    float stage[2][WPB][DH];
    unsigned short p[NH][WPB][16];
    unsigned ml[NH];
    float fin[WPB][32];
    u64 epoch;
    unsigned desc[2][2][2 * NH];
    unsigned ready;
    unsigned have;
    unsigned tref;
    unsigned slip;
    unsigned vote[2 * NH];
    unsigned late[WPB];
    unsigned req;
    unsigned t0;
    unsigned done;
    unsigned prog;
    unsigned fcnt;
    unsigned tick;
    unsigned quiet;
    unsigned cnt[NH + NH / 4];
};

__host__ __device__ constexpr int position(int b) { return (b & ~7) | ((b & 3) << 1) | ((b >> 2) & 1); }
constexpr int groupof(int p) {
    const int r0 = p < 85 ? 0 : p < 171 ? 85 : 171, na = p < 171 ? 43 : 42, t = p - r0;
    return r0 + (t < 64 ? ((t >> 4) & 1) * na + (t >> 5) * 16 + (t & 15) : t < na + 32 ? t - 32 : t);
}
struct Order { unsigned g[NWG / 2]; };

__global__ __launch_bounds__(NT + 128, 3) void kivi_stream_kernel(
    const __hip_bfloat16* __restrict__ q, const int* __restrict__ qk, const int* __restrict__ qv,
    const __hip_bfloat16* __restrict__ ks, const __hip_bfloat16* __restrict__ kz,
    const __hip_bfloat16* __restrict__ vs, const __hip_bfloat16* __restrict__ vz,
    float* __restrict__ ws, __hip_bfloat16* __restrict__ out, float scale,
    Order order) {
    __shared__ Smem sm;

    u64 ka0;
    unsigned ka1, ka2;
    asm volatile("s_load_dwordx2 %0, %3, 0x18\n\ts_load_dword %1, %3, %4 offset:0x4c\n\ts_load_dword %2, %3, 0x40"
                 : "=&s"(ka0), "=&s"(ka1), "=&s"(ka2) : "s"(__builtin_amdgcn_kernarg_segment_ptr()), "s"((blockIdx.x & 127) * 4));
    const u64 tb64 = __builtin_amdgcn_s_memrealtime();
    const unsigned tb = (unsigned)tb64;
    const int tid = threadIdx.x;
    typedef const __attribute__((address_space(1))) int* gptr;
    u64 code;
    int codelen;
    asm volatile("s_getpc_b64 %0\nkivi_code_begin:\n\ts_mov_b32 %1, kivi_code_end-kivi_code_begin-4" : "=s"(code), "=s"(codelen));
    const int warm = *(gptr)(code + min((unsigned)tid * 128u, (unsigned)codelen));
    const int win = blockIdx.x >> 7;
    auto blockof = [&](int it) { return it >> 2 == 10 ? it : (it & ~3) + 2 * win + ((it >> 1) & 1); };
    auto halfof = [&](int it) { return (it >> 2 == 10 ? win : it) & 1; };
    auto group = [&](int it, int x) { return blockof(it) * NWG + ((x >> halfof(it) * 8) & 255); };
    unsigned pb = blockof(tid >> 3) * NWG, ph = halfof(tid >> 3) * 8;
    asm volatile("" : "+v"(pb), "+v"(ph));
    SB();
    asm volatile("s_waitcnt lgkmcnt(0)" : "+s"(ka0), "+s"(ka1), "+s"(ka2));
    const int gx = ka1 & 0xffff, px = ka1 >> 16;
    const unsigned pg = pb + ((ka1 >> ph) & 255), po = pg * (DH * 2) + (tid & 7) * 16;
    v4i pk[6];
    if (tid < NT) {
        typedef const __attribute__((address_space(1))) char* gchar;
        typedef const __attribute__((address_space(1))) v4i* gv4;
#pragma unroll
        for (int u = 0; u < 3; ++u) pk[u] = __builtin_nontemporal_load((gv4)((gchar)ka0 + po + u * 128));
    }
    SB();
    const int wave = __builtin_amdgcn_readfirstlane(tid >> 6);
    u64* tstart = reinterpret_cast<u64*>(ws + WS_T);
    if (tid == NT) st_dev(tstart + blockIdx.x, tb64);
    if (wave == WPB + 1) {
        const unsigned k = blockIdx.x >> 3, k4 = min(k, 7u) * 4;
        const u64 p = ((const __attribute__((address_space(4))) u64*)__builtin_amdgcn_kernarg_segment_ptr())[(0x13365444u >> k4) & 7u];
        unsigned x;
        asm volatile("s_load_dword %0, %1, 0x0\n\ts_waitcnt lgkmcnt(0)" : "=s"(x) : "s"(p + (((k - ((0x74443000u >> k4) & 7u)) << 21) + (blockIdx.x & 7) * 256)) : "memory");
    }
    SB();
    const int lane = tid & 63;
    const int r = lane & 15;
    const int g = lane >> 4;
    const int pos = position(blockIdx.x);
    auto tagof = [](u64 e, int j) {
        e *= 0x9e3779b97f4a7c15ull;
        return (unsigned)(j == 3 ? (e ^ (e >> 29)) >> 8 : e >> (j * 20)) << 8;
    };
    auto until = [&](unsigned t) { while ((int)(clk() - t) < 0) __builtin_amdgcn_s_sleep(1); };
    auto behind = [&]() {
        const unsigned tick = ld_wg(&sm.tick), t = clk();
        while (ld_wg(&sm.tick) == tick && clk() - t < 100u) __builtin_amdgcn_s_sleep(1);
    };
    const int quart = (pos >> 5) & 3, tau = (-((pos >> 1) + quart)) & 15;
    auto shared = [&](int b) { return SPL == 4 || ((b ^ quart) & 1) == 0; };
    auto job = [&](int t, int b, unsigned pol, unsigned ok, unsigned side) {
        const int bd = tau == 10 || (t == 0 && shared(b)) ? b : 2 * tau + (SPL == 4 ? 0 : quart & 1);
        const int reg = (shared(b) ? t : 1) * 12 + (group(2 * bd, pos & 1 ? px : gx) * 3 >> 12);
        return (((ok >> reg) & 1u && (t || b >= H0) ? side ^ (pol >> reg) : quart >> 1) ^ pos) & 1u;
    };
    auto describe = [&](int t, int it, int j, unsigned pol, unsigned ok, unsigned side) {
        const int b = it >> 1, n = it * 2 + j;
        const bool tr = it >> 2 == tau && tau > 0, sh = tr && shared(b);
        const unsigned jb = job(t, tr ? b : 2 * tau, pol, ok, side);
        const unsigned x = tr ? ((sh ? j : jb) ^ pos) & 1u : 0u;
        const unsigned gr = group(it, x ? px : gx), c = 6 * gr + 3;
        const unsigned flip = sh ? 0u : (pol >> (t * 12 + (c >> 13))) & 1u;
        const bool early = !t && b < H0;
        const bool known = (ok >> (t * 12 + (it >> 2) * 3 / 4)) & (ok >> (t * 12 + ((it >> 2) * 3 + 2) / 4)) & 1u;
        const unsigned ex = (early || b < 2 || !known ? (n & 3) == 0 ? EXS(0) : (n & 3) == 1 ? EXS(1) : (n & 3) == 2 ? EXS(2) : EXS(3) : 0) + (early || known ? 0 : n & 1)
                          + (n >= 84 && n < 87 ? G21 : 0) + (t ? n == 0 ? GAP1 : n == NG2 && ((pol ^ (pol >> 12)) & 0xfffu) ? GAP2 : 0 : n >= 3 && n < 3 + 4 * NSLOW ? EXK : 0);
        return gr * (64 * DH * 4) | ex << 4 | (early || known || sh ? 0u : 8u) | x << 2 | flip << 1
             | (((sh ? jb : j ^ win ^ flip) ^ (c >> 8) ^ (c >> 15)) & 1u);
    };

    if (__builtin_expect(wave < WPB, 1)) {
    const int ww = (wave & 3) * 2 + (wave >> 2);
    const int ti = ww * 4 + g;
    const int tev = ti + (ti >= 11 ? 10 : 0) + (ti >= 22 ? 11 : 0), tod = ti + 11 + (ti >= 10 ? 11 : 0) + (ti >= 21 ? 10 : 0);
    const unsigned offe = tev * (DH * 4) + r * 16, offo = tod * (DH * 4) + r * 16;
    unsigned descv[2], dsc[4];
    auto lanes = [=](unsigned d) { return (d & 1u ? offo : offe) + (d & ~0x3fffu); };
    auto fetch = [&](int t) {
        while (__builtin_amdgcn_readfirstlane(ld_wg(&sm.ready)) == 0) __builtin_amdgcn_s_sleep(1);
        asm volatile("" ::: "memory");
#pragma unroll
        for (int j = 0; j < 2; ++j) {
            descv[j] = sm.desc[t][j][lane];
            if (ww >= WPB / 2 && (descv[j] & 8u)) descv[j] ^= 3u;
        }
    };
    const char* dummy = reinterpret_cast<const char*>(qv) + (size_t)(blockIdx.x * WPB + wave) * 4864;

    v4i buf[RING];
    auto burst = [&](const char* p, int i, unsigned o) {
#pragma unroll
        for (int e = 0; e < 3; ++e) { buf[i * 3 + e] = ldstream(p + o + e * 256); SB(); }
    };

    const unsigned phase = ((pos & (NWG / 2 - 1)) * (BODY16 / 512) + ww * WST * (BODY16 / (12 * 256))) >> 16;
    unsigned base = tb + phase + PSH;
    unsigned sched = 0, off = 0, offv = 0;
    int prev = 0, now = 0, hold = 0, big = 0, body = 0;
    unsigned hist = 0;
    unsigned vc = 0;
    auto pace = [&](int due) {
        int now;
        while ((now = (int)(clk() - base)) < due) __builtin_amdgcn_s_sleep(1);
        return now;
    };
    auto follow = [&](unsigned o2) {
        hold = o2 - off > SLIP / 2 ? HOLDB : o2 != off ? HOLD : max(hold - 1, 0);
        big = o2 - off > SLIP / 2 ? HOLDB : max(big - 1, 0);
        base += o2 - off;
        off = o2;
    };
    auto pacen = [&](auto N) {
        constexpr int n = decltype(N)::value;
        follow(__builtin_amdgcn_readfirstlane(offv));
        now = pace(slot(n));
        vc = inflight();
        hist <<= 1;
        if (__builtin_expect(vc > 3, 0)) {
            hist |= 1u;
            asm volatile("s_waitcnt vmcnt(3)");
            now = (int)(clk() - base);
            if (now > slot(n) + THR && prev < PRV && big == 0 && off + SLIP <= MAXOFF && body >= NOSLK
                && (int)(base + now - __builtin_amdgcn_readfirstlane(ld_wg(&sm.quiet))) >= 0) {
                if (lane == 0 && __hip_atomic_fetch_add(&sm.vote[body], 1u, __ATOMIC_RELAXED, __HIP_MEMORY_SCOPE_WORKGROUP) + 1 >= VOTES)
                    __hip_atomic_fetch_max(&sm.slip, off + SLIP, __ATOMIC_RELAXED, __HIP_MEMORY_SCOPE_WORKGROUP);
                unsigned o2;
                while ((o2 = __builtin_amdgcn_readfirstlane(ld_wg(&sm.slip))) == off && (int)(clk() - base) < now + VWAIT) {}
                if (o2 != off) { follow(o2); now = pace(slot(n)); }
            }
            if (hold == 0 && off + SS <= MAXOFF && body >= NOSM && __builtin_popcount(hist & DBM) >= DBN && lane == 0) st_wg(&sm.req, off + SS);
        }
    };
    auto check = [&](auto N) {
        constexpr int n = decltype(N)::value;
        prev = now - slot(n);
        if (lane == 0) st_wg(&sm.late[wave], (unsigned)prev);
    };
    auto getslip = [&]() { offv = __hip_atomic_load(&sm.slip, __ATOMIC_RELAXED, __HIP_MEMORY_SCOPE_WORKGROUP); };
    auto arrive = [&](int i) {
        asm volatile("" ::: "memory");
        if (lane == 0) __hip_atomic_fetch_add(&sm.cnt[i], 1u, __ATOMIC_RELAXED, __HIP_MEMORY_SCOPE_WORKGROUP);
    };
    auto next_body = [&]() {
        const unsigned s2 = sched + BODY16;
        base += (s2 >> 16) - (sched >> 16);
        sched = s2;
        ++body;
    };

    {
        v4i pv[2], pq[2];
        pq[0] = reinterpret_cast<const v4i*>(q)[tid];
        pq[1] = reinterpret_cast<const v4i*>(q)[NT + (tid & 255)];
        const unsigned vo = pg * 128 + (tid & 7) * 16;
        PST(1); pace(slot(3)); SB();
        pk[3] = ldstream((const char*)kz + po);
        pk[4] = ldstream((const char*)kz + po + 128);
        pk[5] = ldstream((const char*)kz + po + 256);
        const int pl = (wave & 1) * 32 + (lane & 31);
        const bool isk = pl < 48, isz = isk ? pl >= 24 : pl >= 56;
        const char* par = (const char*)(isk ? isz ? kz : ks : isz ? vz : vs) + (isk ? pl % 24 : pl & 7) * 16
                        + group(tau * 4 + (wave >> 1), px) * (isk ? DH * 2 : 128);
        asm volatile("" : "+v"(par));
        PST(2); pace(slot(6)); SB();
        pv[0] = ldstream((const char*)vs + vo);
        pv[1] = ldstream((const char*)vz + vo);
        v4i pp = __builtin_nontemporal_load((const __attribute__((address_space(1))) v4i*)par);
        dsc[0] = describe(0, 0, 0, 0u, 0u, 0u);
        dsc[1] = describe(0, 0, 1, 0u, 0u, 0u);
        PST(3); pace(slot(8)); SB();
        burst((const char*)qk, 0, lanes(dsc[0]));
        PST(4);
        sm.qt[tid] = pq[0];
        if (tid < 256) sm.qt[NT + tid] = pq[1];
#pragma unroll
        for (int u = 0; u < 3; ++u) sm.ksz[(tid >> 3) * 48 + u * 8 + (tid & 7)] = pk[u];
#pragma unroll
        for (int u = 0; u < 3; ++u) {
            const v4i x = pk[3 + u];
            sm.ksz[(tid >> 3) * 48 + 24 + u * 8 + (tid & 7)] = v4i{(int)negscale(x.x), (int)negscale(x.y), (int)negscale(x.z), (int)negscale(x.w)};
        }
        PST(5); pace(slot(11)); SB();
        burst((const char*)qk, 1, lanes(dsc[1]));
        PST(6);
        descv[0] = describe(0, lane, 0, 0u, 0u, 0u);
        descv[1] = describe(0, lane, 1, 0u, 0u, 0u);
        SB();
        sm.vsz[0][tid] = pv[0];
        sm.vsz[1][tid] = v4i{(int)negscale(pv[1].x), (int)negscale(pv[1].y), (int)negscale(pv[1].z), (int)negscale(pv[1].w)};
        if (isz) pp = v4i{(int)negscale(pp.x), (int)negscale(pp.y), (int)negscale(pp.z), (int)negscale(pp.w)};
        *(isk ? &sm.ksz[(64 + (wave >> 1)) * 48 + pl] : &sm.vsz[isz][(64 + (wave >> 1)) * 8 + (pl & 7)]) = pp;
    }
    asm volatile("" :: "v"(warm));
    PST(7);
    __syncthreads();
    PST(8);
    base = __builtin_amdgcn_readfirstlane(sm.t0) + OFF0 + phase;

    const v2f huge = {0x1p127f, 0x1p127f};

    for (int S = 0; S < NH; ++S) {
        v2f kz2[6];
        unsigned slo[6], shi[6];
        v2u qq[3];
        const char* nsup = (const char*)(S + 1 < NH ? qk : qv);
        const int head = S >> 1 == 10 ? S : (S & ~1) + win;
        float mylogit = 0.f;
        float dot = 0.f;
        sfor<12>([&](auto N) {
            constexpr int n = decltype(N)::value;
            constexpr int i = n / 3, j = n % 3;
            if (n == 6 && (S == H0 - 1 || S == NH - 1)) fetch(S == NH - 1);
            if (j == 0) {
                const unsigned d = dsc[i ^ 2] = __builtin_amdgcn_readlane(descv[i & 1], (2 * S + 1 + (i >> 1)) & 63);
                const unsigned o = lanes(d);
                base += (d >> 4) & 31u;
                pacen(N);
                SB();
                burst(i < 2 ? (const char*)qk : nsup, i ^ 2, o);
                SB();
                if (tid == NT - 1) st_wg(&sm.tick, S * 4 + i + 1);
                check(N);
            }
            if (j == 2) getslip();
            if (i % 2 == 0 || ((dsc[i] ^ dsc[i ^ 1]) & 4u)) {
                const int it = S * 2 + (i >> 1), sl = dsc[i] & 4u ? 64 + (it & 3) : it;
                const v2u* pk = reinterpret_cast<const v2u*>(&sm.ksz[sl * 48]);
                const v2u a = pk[j * 16 + r], b = pk[48 + j * 16 + r];
                slo[j * 2 + 0] = a.x & 0xffffu; shi[j * 2 + 0] = a.x & 0xffff0000u;
                slo[j * 2 + 1] = a.y & 0xffffu; shi[j * 2 + 1] = a.y & 0xffff0000u;
                kz2[j * 2 + 0] = v2f{bf_lo(b.x), bf_hi(b.x)};
                kz2[j * 2 + 1] = v2f{bf_lo(b.y), bf_hi(b.y)};
            }
            if (i == 0) qq[j] = reinterpret_cast<const v2u*>(&sm.qt[head * 24])[j * 16 + r];
            if (j == 0) dot = 0.f;
            {
                const v4i x = buf[n % RING];
                const v2f t0 = __builtin_elementwise_fma(v2f{__int_as_float(x.x), __int_as_float(x.y)}, huge, kz2[j * 2]);
                const v2f t1 = __builtin_elementwise_fma(v2f{__int_as_float(x.z), __int_as_float(x.w)}, huge, kz2[j * 2 + 1]);
                float y[4];
                mul4(y, pack(t0.x, t0.y), pack(t1.x, t1.y), slo[j * 2], shi[j * 2], slo[j * 2 + 1], shi[j * 2 + 1]);
                dot = dot2(pack(y[0], y[1]), qq[j].x, dot);
                dot = dot2(pack(y[2], y[3]), qq[j].y, dot);
            }
            if (j == 2) {
                const float d = rowsum16(dot);
                if ((r & 3) == (i ^ ((dsc[i] >> 1) & 1))) mylogit = d;
            }
            asm volatile("" : "+v"(dot), "+v"(mylogit));
            SB();
        });
        if (r < 4) sm.p[S][wave][g * 4 + r] = (unsigned short)pack(rb(mylogit * 0x1p22f) * scale, 0.f);
        if ((S & 3) == 3) arrive(NH + (S >> 2));
        if (S == NH - 3 && tid == 0) st_wg(&sm.done, 1u);
        next_body();
    }

    for (int S = 0; S < NH; ++S) {
        v2u P;
        unsigned vsl, vsh, vzf;
        v4f acc = {0.f, 0.f, 0.f, 0.f};
        float y0[12];
        unsigned b0[12];
        const bool more = S + 1 < NH;
        unsigned sl, sh;
        v2f z2;
        sfor<12>([&](auto N) {
            constexpr int n = decltype(N)::value;
            constexpr int i = n / 3, j = n % 3;
            if (j == 0) {
                const unsigned d = dsc[i ^ 2] = __builtin_amdgcn_readlane(descv[i & 1], (2 * S + 1 + (i >> 1)) & 63);
                unsigned o = lanes(d);
                const char* src = (const char*)qv;
                if (i >= 2 && !more) { o = i * 768; src = dummy; }
                if (i < 2 || more) base += (d >> 4) & 31u;
                if (i < 2 || more) pacen(N);
                SB();
                burst(src, i ^ 2, o);
                SB();
                if (tid == NT - 1) st_wg(&sm.tick, 4 * NH + S * 4 + i + 1);
                check(N);
            }
            if (n == 0) {
                const unsigned need = S < 2 ? 1 : S;
                while (__builtin_amdgcn_readfirstlane(ld_wg(&sm.prog)) < need) {}
                asm volatile("" ::: "memory");
                P = *reinterpret_cast<const v2u*>(&sm.p[S][wave][g * 4]);
                P.x = __builtin_amdgcn_alignbit(P.x, P.x, (dsc[0] & 2u) << 3);
                P.y = __builtin_amdgcn_alignbit(P.y, P.y, (dsc[2] & 2u) << 3);
                const unsigned d3 = __builtin_amdgcn_readlane(descv[1], 2 * S + 1);
                const unsigned x = (((dsc[0] & 7u) | (dsc[1] & 7u) << 8) | ((dsc[2] & 7u) << 16 | (d3 & 7u) << 24)) >> (r & 3) * 8;
                const int it = S * 2 + ((r >> 1) & 1);
                const int ltok = (x & 4u ? 64 + (it & 3) : it) * 64 + (x & 1u ? tod : tev);
                vsl = reinterpret_cast<const unsigned short*>(&sm.vsz[0][0])[ltok];
                vsh = vsl << 16;
                vzf = (unsigned)reinterpret_cast<const unsigned short*>(&sm.vsz[1][0])[ltok] << 16;
            }
            if (j == 2) getslip();
            if (j == 0) {
                sl = dppu<0x55 * i>(vsl);
                sh = dppu<0x55 * i>(vsh);
                const float zb = __uint_as_float(dppu<0x55 * i>(vzf));
                z2 = v2f{zb, zb};
            }
            float y[4];
            {
                const v4i x = buf[n];
                const v2f t0 = __builtin_elementwise_fma(v2f{__int_as_float(x.x), __int_as_float(x.y)}, huge, z2);
                const v2f t1 = __builtin_elementwise_fma(v2f{__int_as_float(x.z), __int_as_float(x.w)}, huge, z2);
                mul4(y, pack(t0.x, t0.y), pack(t1.x, t1.y), sl, sh, sl, sh);
            }
#pragma unroll
            for (int e = 0; e < 4; ++e) {
                const int idx = j * 4 + e;
                if (i % 2 == 0) {
                    y0[idx] = y[e];
                    asm volatile("" : "+v"(y0[idx]));
                } else if (i == 1) {
                    b0[idx] = pack(y0[idx], y[e]);
                    asm volatile("" : "+v"(b0[idx]));
                } else {
                    const v2u B = {b0[idx], pack(y0[idx], y[e])};
                    const v2u A = {r == idx ? P.x : 0u, r == idx ? P.y : 0u};
                    acc = __builtin_amdgcn_mfma_f32_16x16x16bf16_1k(__builtin_bit_cast(v4s, A), __builtin_bit_cast(v4s, B), acc, 0, 0, 0);
                }
            }
            if (n == 11) {
                if (S == NH - 2 && tid == 0) sm.tref = base + slot(12) + slot(3);
                if (S == NH - 3 && tid == 0) st_wg(&sm.done, 2u);
                if (lane < 48) *reinterpret_cast<v4f*>(&sm.stage[S & 1][wave][lane * 4]) = acc;
                arrive(S);
            }
            SB();
        });
        next_body();
    }
    {
        const int mh = (blockIdx.x / 16 * 2 + (blockIdx.x & 7) / 4 + 20) % NH, msl = (blockIdx.x & 3) * 2 + ((blockIdx.x >> 3) & 1);
        const bool both = mh >> 1 == 10;
        const unsigned tag = tagof(sm.epoch, lane & 3);
        const float* src = ws + ((size_t)((both ? 0 : mh & 1) * (NWG / 2) + wave * 16) * NRC + (mh >> 1) + (mh > 20)) * RS + msl * 16;
        const unsigned so = (lane >> 2) * (NRC * RS * 4) + (lane & 3) * 16;
        until(base - slot(12) + slot(3) - phase + wave * STG + (mh >= NH - 2 ? TPL : TPC));
        v4i x[2];
        for (;;) {
            x[0] = ld_rec(src, so);
            x[1] = v4i{0, 0, 0, (int)tag};
            if (both) x[1] = ld_rec(src + NWG / 2 * NRC * RS, so);
            asm volatile("s_waitcnt vmcnt(0)" : "+v"(x[0]), "+v"(x[1]));
            if (__all((((unsigned)x[0].w ^ tag) | ((unsigned)x[1].w ^ tag)) < 256u) || clk() - tb >= TMO) break;
            __builtin_amdgcn_s_sleep(PSL);
        }
#pragma unroll
        for (int d = 0; d < 6; ++d) asm volatile("" :: "v"(buf[d]));
        unsigned ml[2];
#pragma unroll
        for (int k = 0; k < 2; ++k) {
            ml[k] = ((unsigned)x[k].w ^ tag) << (lane & 3) * 8;
            ml[k] |= dppu<0xB1>(ml[k]);
            ml[k] |= dppu<0x4E>(ml[k]);
        }
        if (!both) ml[1] = ml[0] & 0xffff0000u;
        const float M = x4max(fmaxf(bf_hi(ml[0]), bf_hi(ml[1])));
        const float w0 = __expf(bf_hi(ml[0]) - M), w1 = __expf(bf_hi(ml[1]) - M);
        float a[6];
#pragma unroll
        for (int e = 0; e < 3; ++e) {
            a[2 * e] = bf_lo(x[0][e]) * w0 + bf_lo(x[1][e]) * w1;
            a[2 * e + 1] = bf_hi(x[0][e]) * w0 + bf_hi(x[1][e]) * w1;
        }
        const float l = (float)__builtin_bit_cast(_Float16, (unsigned short)ml[0]) * w0 + (float)__builtin_bit_cast(_Float16, (unsigned short)ml[1]) * w1;
        float c0 = fold16(fold32(a[0], a[4]), fold32(a[2], l));
        float c1 = fold16(fold32(a[1], a[5]), fold32(a[3], l));
        c0 += dpp<0x124>(c0); c1 += dpp<0x124>(c1);
        c0 += dpp<0x128>(c0); c1 += dpp<0x128>(c1);
        if (g == 3) c1 = M;
        if (r < 4) *reinterpret_cast<v2f*>(&sm.fin[wave][g == 3 ? 24 + r * 2 : r * 6 + g * 2]) = v2f{c0, c1};
        asm volatile("" ::: "memory");
        unsigned arr = 0;
        if (lane == 0) arr = __hip_atomic_fetch_add(&sm.fcnt, 1u, __ATOMIC_RELAXED, __HIP_MEMORY_SCOPE_WORKGROUP);
        asm volatile("" ::: "memory");
        const v4f y = *reinterpret_cast<const v4f*>(&sm.fin[lane >> 3][(lane & 7) * 4]);
        const float mw = sm.fin[lane >> 3][25];
        const float w = __expf(mw - x4max(mw));
        float d = fold16(fold32(y.x * w, y.z * w), fold32(y.y * w, y.w * w));
        d += dpp<0x128>(d);
        const float L = __int_as_float(__builtin_amdgcn_readlane(__float_as_int(d), 6));
        const unsigned o = pack(d / L, 0.f);
        asm volatile("" :: "v"(o));
        if (__builtin_amdgcn_readfirstlane(arr) == WPB - 1) {
            if (r < 6) reinterpret_cast<unsigned short*>(out)[mh * DH + msl * SLC + r * 4 + g] = (unsigned short)o;
        }
    }
    return;
    }
    if (wave == WPB) {
        const int l = lane;
        u64* echo = tstart + NWG;
        u64* gofs = tstart + 3 * NWG;
        if (l == 0) {
            if (blockIdx.x < NR) st_dev(gofs + blockIdx.x * 32, tb64 - 100000ull);
            if (blockIdx.x == NR) st_dev(gofs + 16, tb64 - 100000ull);
            sm.slip = 0;
            sm.req = 0;
            sm.done = 0;
            sm.prog = 0;
            sm.ready = 0;
            sm.tick = 0;
            sm.quiet = tb;
            sm.fcnt = 0;
            sm.epoch = tb64;
            sm.have = blockIdx.x == 0;
        }
        if (l < NH + NH / 4) sm.cnt[l] = 0;
        sm.vote[l] = 0;
        until(tb + TGATH);
        unsigned ld;
        for (;;) {
            const u64 dt = ld_dev(tstart + NWG - 8 + (l & 7)) - tb64 + 1000ull;
            ld = dt < 2000ull ? (unsigned)dt : 0u;
            if (__all(dt < 2000ull) || clk() - tb > (unsigned)TGMAX) break;
        }
        ld = max(ld, dppu<0x124>(ld));
        ld = max(ld, dppu<0x122>(ld));
        ld = max(ld, dppu<0x121>(ld));
        ld = __builtin_amdgcn_readfirstlane(ld);
        const u64 t64 = ld ? tb64 + ld - 1000ull : tb64;
        if (l == 0) sm.t0 = (unsigned)t64;
        u64 to[NWG / 64];
        bool have = blockIdx.x == 0;
        auto chores = [&](bool all) {
            if (blockIdx.x == 0) {
                u64 x[NWG / 64];
#pragma unroll
                for (int u = 0; u < NWG / 64; ++u) x[u] = ld_dev(tstart + u * 64 + l);
#pragma unroll
                for (int u = 0; u < NWG / 64; ++u) {
                    if (all || __any(x[u] != to[u])) st_rec(echo + u * 128, l * 16, v4i{(int)(unsigned)x[u], (int)(unsigned)(x[u] >> 32), (int)tb, (int)(unsigned)(tb64 >> 32)});
                    to[u] = x[u];
                }
            } else if (!have && !all) {
                v4i x = ld_rec(echo + blockIdx.x * 2, 0);
                asm volatile("s_waitcnt vmcnt(0)" : "+v"(x));
                if ((unsigned)x.x == tb && (unsigned)x.y == (unsigned)(tb64 >> 32)) {
                    have = true;
                    if (l == 0) sm.epoch = (u64)(unsigned)x.w << 32 | (unsigned)x.z;
                    asm volatile("s_waitcnt lgkmcnt(0)" ::: "memory");
                    if (l == 0) st_wg(&sm.have, 1u);
                }
            }
        };
        __syncthreads();
        asm volatile("" :: "v"(warm));
        chores(true);
        until(tb + TECHO);
        const u64* mine = NR > 1 ? gofs + ((blockIdx.x >> 3) % NR) * 32 : gofs + 16;
        unsigned sent = 0, stage = 0, tsh = clk();
        int nt = blockIdx.x < 200, spin = 0;
        while (stage < 2) {
            if (PB > 1 && have) __builtin_amdgcn_s_sleep(PSLEEP);
            behind();
            unsigned tch = 0;
            if (nt < 25 && ld_wg(&sm.tick) + TLEAD >= (unsigned)(nt / 12 * 128 + nt % 12 * 4 / 3 * 8 - nt / 24 * 32)) {
                const size_t pg = ((size_t)((blockIdx.x >> 3) + 32 * (nt % 12)) << 21) + ((blockIdx.x * 29 + nt) & 255) * 256;
                const char* tp = nt < 12 ? (const char*)qk + pg : nt < 24 ? (const char*)qv + pg : blockIdx.x < 16 ? (const char*)ws + pg : (const char*)out;
                unsigned x;
                if (nt < 24) asm volatile("s_nop 4\n\tglobal_load_dword %0, %1, %2 nt" : "+v"(tch) : "v"(0), "s"(tp) : "memory");
                else if (blockIdx.x < 24) asm volatile("s_load_dword %0, %1, 0x0\n\ts_waitcnt lgkmcnt(0)" : "=s"(x) : "s"(tp) : "memory");
                ++nt;
            }
            const u64 gd = ld_dev(mine) - t64;
            asm volatile("s_waitcnt vmcnt(0)" : "+v"(tch));
            const unsigned go = gd <= (u64)MAXOFF ? (unsigned)gd : 0u;
            const unsigned own = ld_wg(&sm.slip);
            if (go > own && l == 0) __hip_atomic_fetch_max(&sm.slip, go, __ATOMIC_RELAXED, __HIP_MEMORY_SCOPE_WORKGROUP);
            if (own <= go) tsh = clk();
            else if (clk() - tsh > (unsigned)REVT && blockIdx.x >= NSENT) { if (l == 0) st_wg(&sm.slip, go); }
            unsigned req = ld_wg(&sm.req);
            if ((int)(clk() - ld_wg(&sm.quiet)) < 0) { req = 0; if (l == 0) st_wg(&sm.req, 0u); }
            unsigned want = blockIdx.x < NSENT ? own : 0u;
            if (req == go + SS && own == go) {
                unsigned x = ld_wg(&sm.late[l & 7]);
                x += dppu<0x121>(x);
                x += dppu<0x122>(x);
                x += dppu<0x124>(x);
                want = min(req + (__builtin_amdgcn_readfirstlane(x) * LFAC / 64 & ~15u), (unsigned)MAXOFF);
            }
            if (want > go && want > sent) {
                u64 old = 0;
                behind();
                if (l == 0) st_wg(&sm.quiet, clk() + TQA);
                if (l == 0) old = __hip_atomic_fetch_max(gofs + 16, t64 + want, __ATOMIC_RELAXED, __HIP_MEMORY_SCOPE_AGENT);
                if (NR > 1 && __builtin_amdgcn_readfirstlane((unsigned)(old < t64 + want)) && l < NR)
                    __hip_atomic_fetch_max(gofs + l * 32, t64 + want, __ATOMIC_RELAXED, __HIP_MEMORY_SCOPE_AGENT);
                sent = want;
            }
            if (!have || (blockIdx.x == 0 && ++spin % 8 == 0)) { behind(); chores(false); }
            if (ld_wg(&sm.done) > stage) {
                ++stage;
                behind();
                asm volatile("" : "+s"(code));
                int w = 0;
#pragma unroll
                for (int u = 0; u < 4; ++u) {
                    const int a = (u * 64 + l) * 128;
                    w ^= *(gptr)(code + (a < codelen ? a : 0));
                }
                asm volatile("" :: "v"(w));
            }
        }
        while (!have && clk() - tb < TMO) chores(false);
        if (l == 0) st_wg(&sm.have, 1u);
        if (blockIdx.x == 0) {
            for (spin = 1; ld_wg(&sm.fcnt) < WPB; ++spin) {
                __builtin_amdgcn_s_sleep(2);
                if (spin % 1024 == 0) chores(false);
            }
        }
        return;
    }
    {
        u64* table = tstart + 3 * NWG + NR * 32;
        const unsigned reg = blockIdx.x - CAL0 < 64u ? (blockIdx.x - CAL0) & 31u : 31u;
        int lead = 0, nr = 0;
        const unsigned page = reg % 12 * 32 + (tb * 0x85ebca6bu >> 27);
        const char* const pp = (const char*)(reg < 12 ? qk : qv) + ((size_t)page << 21);
        auto probe = [&]() {
            const unsigned h = (tb + nr) * 0x9e3779b1u;
            const char* a = pp + (h >> 25) * 16384 + ((h >> 8) & 127) * 64;
            u64 t0, t1, t2;
            unsigned x;
            asm volatile("s_memtime %0\n\ts_waitcnt lgkmcnt(0)\n\ts_load_dword %3, %4, 0x0\n\ts_waitcnt lgkmcnt(0)\n\t"
                         "s_memtime %1\n\ts_waitcnt lgkmcnt(0)\n\ts_load_dword %3, %4, 0x2000\n\ts_waitcnt lgkmcnt(0)\n\t"
                         "s_memtime %2\n\ts_waitcnt lgkmcnt(0)"
                         : "=&s"(t0), "=&s"(t1), "=&s"(t2), "=&s"(x) : "s"(a) : "memory");
            const int d0 = (int)(t1 - t0), d1 = (int)(t2 - t1);
            if (min(d0, d1) > 450 && nr > 0) lead += max(-600, min(600, d1 - d0));
            ++nr;
        };
#pragma nounroll
        for (int k = 0; k < 2; ++k) {
#pragma nounroll
            while (reg < 24 && nr <= NRND && (k || clk() - tb < (unsigned)TPRB)) probe();
            if (k == 0) __syncthreads();
        }
        asm volatile("" :: "v"(warm));
        while (ld_wg(&sm.have) == 0 && clk() - tb < TMO) __builtin_amdgcn_s_sleep(4);
        asm volatile("" ::: "memory");
        const u64 E = sm.epoch;
        if (reg < 24) {
            unsigned xcc;
            asm volatile("s_getreg_b32 %0, hwreg(HW_REG_XCC_ID, 0, 3)" : "=s"(xcc));
            if (((xcc >> 2) ^ page ^ (page >> 7)) & 1) lead = -lead;
            behind();
            if (lane == 0) st_wg(&sm.quiet, clk() + TQ);
            if (lane < 8) st_rec(table + (blockIdx.x - CAL0) * 32, lane * 16, v4i{lead, (int)(xcc - blockIdx.x), (int)(unsigned)E, (int)(unsigned)(E >> 32)});
        }
        unsigned jobs = 0;
        v4i cal;
        until(tb + TTAB);
        for (;;) {
            behind();
            cal = ld_rec(table, ((lane & 31) < 24 ? lane : 0) * 256);
            asm volatile("s_waitcnt vmcnt(0)" : "+v"(cal));
            if (__all((unsigned)cal.z == (unsigned)E && (unsigned)cal.w == (unsigned)(E >> 32)) || clk() - tb >= TMO) break;
        }
        {
            auto two = __builtin_amdgcn_permlane32_swap(cal.x, cal.x, false, false);
            int sum = (int)(two[0] + two[1]) + (int)dppu<0xB1>(two[0] + two[1]);
            const int wide = sum + (int)dppu<0x4E>(sum);
            if (abs(sum) < PTH) sum = wide / 3;
            const unsigned pol = (unsigned)__ballot(sum < 0) ^ FPOL, ok = (unsigned)__ballot(abs(sum) >= PTH) & FMASK;
            const unsigned side = ((unsigned)__builtin_amdgcn_readfirstlane(cal.y) + (blockIdx.x & ~4u)) >> 2;
#pragma unroll
            for (int u = 0; u < 4; ++u) {
                sm.desc[u >> 1][u & 1][lane] = describe(u >> 1, lane, u & 1, pol, ok, side);
                jobs |= (tau > 0 ? job(u >> 1, 2 * tau + (u & 1), pol, ok, side) : pos & 1u) << u;
            }
            asm volatile("" ::: "memory");
            if (lane == 0) st_wg(&sm.ready, 1u);
        }
#pragma nounroll
        for (int cc = 0; cc <= NH / 4; ++cc) {
            const int c = cc < NH / 4 ? cc : tau >> 1;
            const bool act = (tau > 0 && c * 2 + (g >> 1) == tau) == (cc == NH / 4);
            while (ld_wg(&sm.cnt[NH + c]) < WPB) __builtin_amdgcn_s_sleep(2);
            asm volatile("" ::: "memory");
            if (cc >= NH / 4 - 1) {
                const bool sw = ((jobs ^ (jobs >> 2)) >> (lane >> 5)) & 1u;
                v2u* mine = reinterpret_cast<v2u*>(&sm.p[2 * tau][0][0]) + lane;
                if (cc < NH / 4) {
                    if (lane == 0) st_wg(&sm.quiet, clk() + TXW + TQ);
                    if (sw) st_rec(ws + WS_X + pos * 256, lane * 16, v4i{(int)mine->x, (int)mine->y, (int)(unsigned)E, (int)(unsigned)(E >> 32)});
                } else if (__any(sw)) {
                    until(clk() + TXW);
                }
                while (cc == NH / 4 && __any(sw)) {
                    v4i x = {};
                    if (sw) asm volatile("s_nop 4\n\tglobal_load_dwordx4 %0, %1, %2 sc1 nt" : "+v"(x) : "v"(lane * 16), "s"(ws + WS_X + (pos ^ 1) * 256) : "memory");
                    asm volatile("s_waitcnt vmcnt(0)" : "+v"(x));
                    if (__all(!sw || ((unsigned)x.z == (unsigned)E && (unsigned)x.w == (unsigned)(E >> 32))) || clk() - tb >= TMO) {
                        if (sw) *mine = v2u{(unsigned)x.x, (unsigned)x.y};
                        break;
                    }
                    __builtin_amdgcn_s_sleep(8);
                }
                asm volatile("" ::: "memory");
            }
            const bool two = c * 2 + (g >> 1) != 10;
            v4i* pp = reinterpret_cast<v4i*>(&sm.p[c * 4][0][0]) + lane;
            const v4i u = *pp;
            float x[8];
#pragma unroll
            for (int e = 0; e < 4; ++e) { x[2 * e] = bf_lo(u[e]); x[2 * e + 1] = bf_hi(u[e]); }
            float M = fmaxf(fmaxf(fmaxf(x[0], x[1]), fmaxf(x[2], x[3])), fmaxf(fmaxf(x[4], x[5]), fmaxf(x[6], x[7])));
            M = fmaxf(M, dpp<0x128>(M));
            M = fmaxf(M, dpp<0x124>(M));
            M = fmaxf(M, dpp<0x122>(M));
            M = fmaxf(M, dpp<0x121>(M));
            {
                auto y = __builtin_amdgcn_permlane16_swap(__float_as_uint(M), __float_as_uint(M), false, false);
                if (two) M = fmaxf(__uint_as_float(y[0]), __uint_as_float(y[1]));
            }
            v4i o;
            float L = 0.f;
#pragma unroll
            for (int e = 0; e < 4; ++e) {
                const unsigned pk = pack(__expf(x[2 * e] - M), __expf(x[2 * e + 1] - M));
                o[e] = (int)pk;
                L += bf_lo(pk) + bf_hi(pk);
            }
            if (act) *pp = o;
            L = rowsum16(L);
            const float L2 = fold16(L, L);
            if (two) L = L2;
            if (r == 0 && act) sm.ml[c * 4 + g] = __float_as_uint(M) | __builtin_bit_cast(unsigned short, (_Float16)L);
            asm volatile("" ::: "memory");
            if (lane == 0) st_wg(&sm.prog, 1u);
        }
        const unsigned tag = tagof(E, lane & 3);
        auto place = [&](int i) {
            const int i0 = tau + (tau > 10);
            const unsigned jb = i == i0 ? jobs >> 2 : i == 11 && tau == 10 ? jobs >> 3 : pos;
            return (((pos & ~1) | (jb & 1)) * NRC + i) * (RS * 4) + (lane & 31) * 16;
        };
        const unsigned last = place(NRC - 1);
        v2f keep[3] = {};
#pragma nounroll
        for (int h = 0; h < NH; ++h) {
            const unsigned mlb = (sm.ml[h] >> (lane & 3) * 8) & 0xffu;
            if (h < NH - 1) { while (ld_wg(&sm.cnt[h]) < WPB) __builtin_amdgcn_s_sleep(4); }
            else { while (ld_wg(&sm.cnt[h]) < WPB) {} }
            asm volatile("" ::: "memory");
            v2f c[4][3];
#pragma unroll
            for (int e = 0; e < 4; ++e)
#pragma unroll
                for (int k = 0; k < 3; ++k) c[e][k] = *reinterpret_cast<const v2f*>(&sm.stage[h & 1][(lane >> 5) * 4 + e][(lane & 31) * 6 + k * 2]);
            v4i x;
            const bool first = (h & 1) == 0 && h != 20;
#pragma unroll
            for (int k = 0; k < 3; ++k) {
                const v2f a = (keep[k] + (c[0][k] + c[1][k])) + (c[2][k] + c[3][k]), b = a * 0x1p22f;
                x[k] = (int)pack(fold32(b.x, b.x), fold32(b.y, b.y));
                keep[k] = first ? a : v2f{0.f, 0.f};
            }
            x[3] = (int)(tag | mlb);
            if (h == NH - 1) {
                if (lane < 32) st_rec(ws, last, x);
                break;
            }
            if (!first) sm.rec[(h >> 1) + (h > 20)][lane & 31] = x;
            asm volatile("" ::: "memory");
            if (lane == 0) st_wg(&sm.prog, h + 2);
            if (h == NH - 2) {
                v4i y[NRC / 2];
                unsigned o[NRC / 2];
#pragma unroll
                for (int u = 0; u < NRC / 2; ++u) { y[u] = (&sm.rec[0][0])[u * 64 + lane]; o[u] = place(u * 2 + (lane >> 5)); }
                until(ld_wg(&sm.tref) + TREL);
#pragma unroll
                for (int u = 0; u < NRC / 2; ++u) st_rec(ws, o[u], y[u]);
            }
        }
        return;
    }

}
__global__ void kivi_end_marker() { asm volatile("kivi_code_end:"); }

extern "C" void kernel_launch(void* const* d_in, const int* in_sizes, int n_in,
                              void* d_out, int out_size, void* d_ws, size_t ws_size,
                              hipStream_t stream) {
    const __hip_bfloat16* q = (const __hip_bfloat16*)d_in[0];
    const int* qk = (const int*)d_in[1];
    const int* qv = (const int*)d_in[2];
    const __hip_bfloat16* ks = (const __hip_bfloat16*)d_in[3];
    const __hip_bfloat16* kz = (const __hip_bfloat16*)d_in[4];
    const __hip_bfloat16* vs = (const __hip_bfloat16*)d_in[5];
    const __hip_bfloat16* vz = (const __hip_bfloat16*)d_in[6];
    const float scale = (float)(1.0 / sqrt((double)DH));
    Order order;
    for (int b = 0; b < NWG / 2; ++b) {
        const int p = position(b);
        order.g[b] = groupof(p) | groupof(p + 128) << 8 | groupof(p ^ 1) << 16 | groupof((p ^ 1) + 128) << 24;
    }
    kivi_stream_kernel<<<NWG, NT + 128, 0, stream>>>(q, qk, qv, ks, kz, vs, vz, (float*)d_ws, (__hip_bfloat16*)d_out, scale, order);
}
```

```cpp
#include <hip/hip_runtime.h>
#include <hip/hip_bf16.h>
#include <stdint.h>
#include <utility>

#define TGT 199.25
#define WST 16
#define THR 80
#define PRV 16
#define SLIP 250
#define MAXOFF 3000
#define NSENT 8
#define NR 32
#define TPL 265
#define STG 4
#define PSLEEP 16
#define OFF0 380
#define TGATH 150
#define GAP1 10
#define TREL 110
#define NSLOW 1
#define EXK 7
#define NOSLK 2
#define TMO 100000000u
#define TPC 265
#define PSL 2
#define TGMAX 300
#define PSH 0
#define SS 20
#define HOLD 4
#define VOTES 4
#define HOLDB 8
#define REVT 600
#define VWAIT 30
#define LFAC 4
#define NOSM 2
#define H0 5
#define TGS 203.0
#define SPL 2
#define G21 5
#define EXS(i) ((int)(((i) + 1) * (TGS - TGT) * 100.0 / 256.0 + 0.5) - (int)((i) * (TGS - TGT) * 100.0 / 256.0 + 0.5))
#define CAL0 128
#define PTH (NMEM * (NMEM - 1) * 150)
#define NMEM 2
#define PCL 500
#define TPRB 580
#define DBM 7
#define DBN 2
#define TQA 500
#define PB 1
#define PTL 8
#define EARLY 0
#define LATEB 20
#define MEANF 0
#define PF 100
#define DRY 0
#define TPB 20
#define PRE 0
#define BNT 0
#define AH 1
#define DUM 1
#define TLEAD 14
#define GAP2 10
#define NG2 3
#define TECHO 600
#define TQ 300
#define TTAB 1050
#define TXW 200
#define FMASK ~0ull
#define FPOL 0ull
#define RING 12
#define SB() __builtin_amdgcn_sched_barrier(0)
#define PST(i) SB()

typedef int v4i __attribute__((ext_vector_type(4)));
typedef unsigned v2u __attribute__((ext_vector_type(2)));
typedef float v2f __attribute__((ext_vector_type(2)));
typedef float v4f __attribute__((ext_vector_type(4)));
typedef short v4s __attribute__((ext_vector_type(4)));
typedef __bf16 v2bf __attribute__((ext_vector_type(2)));

constexpr int DH = 192;
constexpr int NH = 32;
constexpr int SKV = 32768;
constexpr int WPB = 8;
constexpr int NT = WPB * 64;
constexpr int NWG = 256;
constexpr int NSL = 8, SLC = 24;
constexpr int RS = NSL * 16;
static_assert(SKV == 2 * 64 * NWG, "two 64-token groups per workgroup and head");
constexpr int NX = 4;
constexpr int NRC = 17;
constexpr size_t WS_R = 262144;
constexpr size_t WS_X = WS_R + (size_t)NRC * NWG * RS;
constexpr size_t WS_T = WS_X + (size_t)NWG * 256;
constexpr unsigned BODY16 = (unsigned)(TGT * 100.0 / 64.0 * 65536.0);
constexpr int slot(int n) { return (int)((n * (BODY16 / 12)) >> 16); }

template <typename F, int... I>
__device__ __forceinline__ void sfor_impl(F&& f, std::integer_sequence<int, I...>) {
    (f(std::integral_constant<int, I>{}), ...);
}
template <int N, typename F>
__device__ __forceinline__ void sfor(F&& f) {
    sfor_impl(f, std::make_integer_sequence<int, N>{});
}

__device__ __forceinline__ float bf_lo(unsigned u) { return __uint_as_float(u << 16); }
__device__ __forceinline__ float bf_hi(unsigned u) { return __uint_as_float(u & 0xffff0000u); }
__device__ __forceinline__ float rb(float x) { return (float)(__bf16)x; }
__device__ __forceinline__ unsigned pack(float lo, float hi) {
    return __builtin_bit_cast(unsigned, __builtin_convertvector(v2f{lo, hi}, v2bf));
}
__device__ __forceinline__ float dot2(unsigned a, unsigned b, float c) {
    return __builtin_amdgcn_fdot2_f32_bf16(__builtin_bit_cast(v2bf, a), __builtin_bit_cast(v2bf, b), c, false);
}
__device__ __forceinline__ void mul4(float (&y)[4], unsigned a, unsigned b, unsigned s0, unsigned s1, unsigned s2, unsigned s3) {
    asm("v_dot2_f32_bf16 %0, %4, %6, 0\n\tv_dot2_f32_bf16 %1, %4, %7, 0\n\t"
        "v_dot2_f32_bf16 %2, %5, %8, 0\n\tv_dot2_f32_bf16 %3, %5, %9, 0\n\ts_nop 2"
        : "=&v"(y[0]), "=&v"(y[1]), "=&v"(y[2]), "=&v"(y[3])
        : "v"(a), "v"(b), "v"(s0), "v"(s1), "v"(s2), "v"(s3));
}
__device__ __forceinline__ unsigned clk() { return (unsigned)__builtin_amdgcn_s_memrealtime(); }
__device__ __forceinline__ unsigned inflight() {
    unsigned x;
    asm volatile("s_getreg_b32 %0, hwreg(HW_REG_IB_STS, 0, 4)" : "=s"(x));
    return x;
}
__device__ __forceinline__ unsigned negscale(unsigned u) { return pack(bf_lo(u) * -0x1p-22f, bf_hi(u) * -0x1p-22f); }

template <int CTRL>
__device__ __forceinline__ unsigned dppu(unsigned x) {
    return (unsigned)__builtin_amdgcn_mov_dpp((int)x, CTRL, 0xf, 0xf, true);
}
template <int CTRL>
__device__ __forceinline__ float dpp(float x) { return __uint_as_float(dppu<CTRL>(__float_as_uint(x))); }
__device__ __forceinline__ float rowsum16(float x) {
    x += dpp<0x128>(x);
    x += dpp<0x124>(x);
    x += dpp<0x122>(x);
    x += dpp<0x121>(x);
    return x;
}
__device__ __forceinline__ v4i ldstream(const void* p) {
    return __builtin_nontemporal_load(reinterpret_cast<const v4i*>(p));
}

__device__ __forceinline__ void st_rec(void* p, unsigned o, v4i a) {
    asm volatile("s_nop 4\n\tglobal_store_dwordx4 %0, %1, %2 sc1\n\ts_nop 1" :: "v"(o), "v"(a), "s"(p) : "memory");
}
__device__ __forceinline__ v4i ld_rec(const void* p, unsigned o) {
    v4i x;
    asm volatile("s_nop 4\n\tglobal_load_dwordx4 %0, %1, %2 sc1" : "=v"(x) : "v"(o), "s"(p) : "memory");
    return x;
}
typedef unsigned long long u64;
__device__ __forceinline__ u64 ld_dev(const u64* p) { return __hip_atomic_load(p, __ATOMIC_RELAXED, __HIP_MEMORY_SCOPE_AGENT); }
__device__ __forceinline__ void st_dev(u64* p, u64 v) { __hip_atomic_store(p, v, __ATOMIC_RELAXED, __HIP_MEMORY_SCOPE_AGENT); }
__device__ __forceinline__ unsigned ld_wg(const unsigned* p) { return __hip_atomic_load(p, __ATOMIC_RELAXED, __HIP_MEMORY_SCOPE_WORKGROUP); }
__device__ __forceinline__ void st_wg(unsigned* p, unsigned v) { __hip_atomic_store(p, v, __ATOMIC_RELAXED, __HIP_MEMORY_SCOPE_WORKGROUP); }
__device__ __forceinline__ float fold32(float a, float b) {
    auto s = __builtin_amdgcn_permlane32_swap(__float_as_uint(a), __float_as_uint(b), false, false);
    return __uint_as_float(s[0]) + __uint_as_float(s[1]);
}
__device__ __forceinline__ float fold16(float a, float b) {
    auto s = __builtin_amdgcn_permlane16_swap(__float_as_uint(a), __float_as_uint(b), false, false);
    return __uint_as_float(s[0]) + __uint_as_float(s[1]);
}
__device__ __forceinline__ float x4max(float x) {
    float m;
    asm("s_nop 1\n\tv_max_f32_dpp %1, %1, %1 row_ror:4 row_mask:0xf bank_mask:0xf\n\t"
        "s_nop 1\n\tv_max_f32_dpp %1, %1, %1 row_ror:8 row_mask:0xf bank_mask:0xf\n\t"
        "s_nop 1\n\tv_max_f32_dpp %1, %1, %1 row_bcast:15 row_mask:0xa bank_mask:0xf\n\t"
        "s_nop 1\n\tv_max_f32_dpp %1, %1, %1 row_bcast:31 row_mask:0xc bank_mask:0xf\n\t"
        "s_nop 0\n\tv_readlane_b32 %0, %1, 63\n\ts_nop 1" : "=s"(m), "+v"(x));
    return m;
}

struct __attribute__((aligned(16))) Smem {
    v4i ksz[(64 + NX) * 48];
    v4i qt[NH * 24];
    v4i vsz[2][(64 + NX) * 8];
    v4i rec[NRC - 1][RS / 4];
    float stage[2][WPB][DH];
    unsigned short p[NH][WPB][16];
    unsigned ml[NH];
    float fin[WPB + 1][32];
    u64 epoch;
    unsigned desc[2][2][2 * NH];
    unsigned ready;
    unsigned have;
    unsigned tref;
    unsigned slip;
    unsigned vote[2 * NH];
    unsigned late[WPB];
    unsigned req;
    unsigned t0;
    unsigned done;
    unsigned prog;
    unsigned fcnt;
    unsigned tick;
    unsigned quiet;
    unsigned cnt[NH + NH / 4];
};

__host__ __device__ constexpr int position(int b) { return (b & ~7) | ((b & 3) << 1) | ((b >> 2) & 1); }
constexpr int groupof(int p) {
    const int r0 = p < 85 ? 0 : p < 171 ? 85 : 171, na = p < 171 ? 43 : 42, t = p - r0;
    return r0 + (t < 64 ? ((t >> 4) & 1) * na + (t >> 5) * 16 + (t & 15) : t < na + 32 ? t - 32 : t);
}
struct Order { unsigned g[NWG / 2]; };

__global__ __launch_bounds__(NT + 128, 3) void kivi_stream_kernel(
    const __hip_bfloat16* __restrict__ q, const int* __restrict__ qk, const int* __restrict__ qv,
    const __hip_bfloat16* __restrict__ ks, const __hip_bfloat16* __restrict__ kz,
    const __hip_bfloat16* __restrict__ vs, const __hip_bfloat16* __restrict__ vz,
    float* __restrict__ ws, __hip_bfloat16* __restrict__ out, float scale,
    Order order) {
    __shared__ Smem sm;

    u64 ka0;
    unsigned ka1, ka2;
    asm volatile("s_load_dwordx2 %0, %3, 0x18\n\ts_load_dword %1, %3, %4 offset:0x4c\n\ts_load_dword %2, %3, 0x40"
                 : "=&s"(ka0), "=&s"(ka1), "=&s"(ka2) : "s"(__builtin_amdgcn_kernarg_segment_ptr()), "s"((blockIdx.x & 127) * 4));
    const int tid = threadIdx.x;
    typedef const __attribute__((address_space(1))) int* gptr;
    u64 code;
    int codelen;
    asm volatile("s_getpc_b64 %0\nkivi_code_begin:\n\ts_mov_b32 %1, kivi_code_end-kivi_code_begin-4" : "=s"(code), "=s"(codelen));
    const int warm = *(gptr)(code + min((unsigned)tid * 128u, (unsigned)codelen));
    const int win = blockIdx.x >> 7;
    auto blockof = [&](int it) { return it >> 2 == 10 ? it : (it & ~3) + 2 * win + ((it >> 1) & 1); };
    auto halfof = [&](int it) { return (it >> 2 == 10 ? win : it) & 1; };
    auto group = [&](int it, int x) { return blockof(it) * NWG + ((x >> halfof(it) * 8) & 255); };
    unsigned pb = blockof(tid >> 3) * NWG, ph = halfof(tid >> 3) * 8;
    asm volatile("" : "+v"(pb), "+v"(ph));
    SB();
    asm volatile("s_waitcnt lgkmcnt(0)" : "+s"(ka0), "+s"(ka1), "+s"(ka2));
    const int gx = ka1 & 0xffff, px = ka1 >> 16;
    const unsigned pg = pb + ((ka1 >> ph) & 255), po = pg * (DH * 2) + (tid & 7) * 16;
    v4i pk[6];
    if (tid < NT) {
        typedef const __attribute__((address_space(1))) char* gchar;
        typedef const __attribute__((address_space(1))) v4i* gv4;
#pragma unroll
        for (int u = 0; u < 3; ++u) pk[u] = __builtin_nontemporal_load((gv4)((gchar)ka0 + po + u * 128));
    }
    SB();
    const u64 tb64 = __builtin_amdgcn_s_memrealtime() - LATEB;
    const unsigned tb = (unsigned)tb64;
    const int wave = __builtin_amdgcn_readfirstlane(tid >> 6);
    u64* tstart = reinterpret_cast<u64*>(ws + WS_T);
    if (tid == NT) st_dev(tstart + blockIdx.x, tb64);
    if (wave == WPB + 1) {
        const unsigned k = blockIdx.x >> 3, k4 = min(k, 7u) * 4;
        const u64 p = ((const __attribute__((address_space(4))) u64*)__builtin_amdgcn_kernarg_segment_ptr())[(0x13365444u >> k4) & 7u];
        unsigned x;
        asm volatile("s_load_dword %0, %1, 0x0\n\ts_waitcnt lgkmcnt(0)" : "=s"(x) : "s"(p + (((k - ((0x74443000u >> k4) & 7u)) << 21) + (blockIdx.x & 7) * 256)) : "memory");
    }
    SB();
    const int lane = tid & 63;
    const int r = lane & 15;
    const int g = lane >> 4;
    const int pos = position(blockIdx.x);
    auto tagof = [](u64 e, int j) {
        e *= 0x9e3779b97f4a7c15ull;
        return (unsigned)(j == 3 ? (e ^ (e >> 29)) >> 8 : e >> (j * 20)) << 8;
    };
    auto until = [&](unsigned t) { while ((int)(clk() - t) < 0) __builtin_amdgcn_s_sleep(1); };
    auto behind = [&]() {
        const unsigned tick = ld_wg(&sm.tick), t = clk();
        while (ld_wg(&sm.tick) == tick && clk() - t < 100u) __builtin_amdgcn_s_sleep(1);
    };
    const int quart = (pos >> 5) & 3, tau = (-((pos >> 1) + quart)) & 15;
    auto shared = [&](int b) { return SPL == 4 || ((b ^ quart) & 1) == 0; };
    auto job = [&](int t, int b, u64 pol, u64 ok, unsigned side) {
        const int bd = tau == 10 || (t == 0 && shared(b)) ? b : 2 * tau + (SPL == 4 ? 0 : quart & 1);
        const int reg = (shared(b) ? t : 1) * 32 + (group(2 * bd, pos & 1 ? px : gx) * 3 >> 13) * 4;
        return (((ok >> reg) & 1u && (t || b >= H0) ? side ^ (unsigned)(pol >> reg) : quart >> 1) ^ pos) & 1u;
    };
    auto describe = [&](int t, int it, int j, u64 pol, u64 ok, unsigned side) {
        const int b = it >> 1, n = it * 2 + j;
        const bool tr = it >> 2 == tau && tau > 0, sh = tr && shared(b);
        const unsigned jb = job(t, tr ? b : 2 * tau, pol, ok, side);
        const unsigned x = tr ? ((sh ? j : jb) ^ pos) & 1u : 0u;
        const unsigned gr = group(it, x ? px : gx), c = 6 * gr + 3;
        const unsigned flip = sh ? 0u : ((unsigned)(pol >> t * 32) >> (c >> 14) * 4) & 1u;
        const bool early = !t && b < H0;
        const unsigned okt = (unsigned)(ok >> t * 32);
        const bool known = (okt >> (it >> 2) * 3 / 8 * 4) & (okt >> ((it >> 2) * 3 + 2) / 8 * 4) & 1u;
        const unsigned ex = (early || b < 2 || !known ? (n & 3) == 0 ? EXS(0) : (n & 3) == 1 ? EXS(1) : (n & 3) == 2 ? EXS(2) : EXS(3) : 0) + (early || known ? 0 : n & 1)
                          + (n >= 84 && n < 87 ? G21 : 0) + (t ? n == 0 ? GAP1 : n == NG2 && ((pol ^ (pol >> 32)) & 0x111111u) ? GAP2 : 0 : n >= 3 && n < 3 + 4 * NSLOW ? EXK : 0);
        return gr * (64 * DH * 4) | ex << 4 | (early || known || sh ? 0u : 8u) | x << 2 | flip << 1
             | (((sh ? jb : j ^ win ^ flip) ^ (c >> 8) ^ (c >> 15)) & 1u);
    };

    const int mh = (blockIdx.x / 16 * 2 + (blockIdx.x & 7) / 4 + 20) % NH, msl = (blockIdx.x & 3) * 2 + ((blockIdx.x >> 3) & 1);
    const bool both = mh >> 1 == 10;
    v4i x[2] = {};
    unsigned fa[4], mls, one, oo;
    auto prepare = [&]() {
        auto lds = [](const void* p) { return (unsigned)(size_t)(const __attribute__((address_space(3))) char*)p; };
        fa[0] = lds(&sm.fin[wave][g == 3 ? 24 + (r & 3) * 2 : (r & 3) * 6 + g * 2]);
        fa[1] = lane == 0 ? lds(&sm.fcnt) : lds(&sm.vote[lane]);
        fa[2] = lds(&sm.fin[lane >> 3][(lane & 7) * 4]);
        fa[3] = lds(&sm.fin[lane >> 3][25]);
        mls = (lane & 3) * 8;
        one = tid < NT && lane == 0;
        oo = (mh * DH + msl * SLC + r * 4 + g) * 2;
        asm volatile("" : "+v"(fa[0]), "+v"(fa[1]), "+v"(fa[2]), "+v"(fa[3]), "+v"(mls), "+v"(one), "+v"(oo));
    };
    if (__builtin_expect(wave < WPB, 1)) {
    const int ww = (wave & 3) * 2 + (wave >> 2);
    const int ti = ww * 4 + g;
    const int tev = ti + (ti >= 11 ? 10 : 0) + (ti >= 22 ? 11 : 0), tod = ti + 11 + (ti >= 10 ? 11 : 0) + (ti >= 21 ? 10 : 0);
    const unsigned offe = tev * (DH * 4) + r * 16, offo = tod * (DH * 4) + r * 16;
    unsigned descv[2], dsc[4];
    auto lanes = [=](unsigned d) { return (d & 1u ? offo : offe) + (d & ~0x3fffu); };
    auto fetch = [&](int t) {
        while (__builtin_amdgcn_readfirstlane(ld_wg(&sm.ready)) == 0) __builtin_amdgcn_s_sleep(1);
        asm volatile("" ::: "memory");
#pragma unroll
        for (int j = 0; j < 2; ++j) {
            descv[j] = sm.desc[t][j][lane];
            if (ww >= WPB / 2 && (descv[j] & 8u)) descv[j] ^= 3u;
        }
    };
    const char* dummy = reinterpret_cast<const char*>(qv) + (size_t)(blockIdx.x * WPB + wave) * 4864;

    v4i buf[RING];
    auto burst = [&](const char* p, int i, unsigned o) {
#pragma unroll
        for (int e = 0; e < 3; ++e) { buf[i * 3 + e] = ldstream(p + o + e * 256); SB(); }
    };

    const unsigned phase = ((pos & (NWG / 2 - 1)) * (BODY16 / 512) + ww * WST * (BODY16 / (12 * 256))) >> 16;
    unsigned base = tb + phase + PSH;
    unsigned sched = 0, off = 0, offv = 0;
    int prev = 0, now = 0, hold = 0, big = 0, body = 0;
    unsigned hist = 0;
    unsigned vc = 0;
    auto pace = [&](int due) {
        int now;
        while ((now = (int)(clk() - base)) < due) __builtin_amdgcn_s_sleep(1);
        return now;
    };
    auto follow = [&](unsigned o2) {
        hold = o2 - off > SLIP / 2 ? HOLDB : o2 != off ? HOLD : max(hold - 1, 0);
        big = o2 - off > SLIP / 2 ? HOLDB : max(big - 1, 0);
        base += o2 - off;
        off = o2;
    };
    auto pacen = [&](auto N) {
        constexpr int n = decltype(N)::value;
        follow(__builtin_amdgcn_readfirstlane(offv));
        now = pace(slot(n));
        vc = inflight();
        hist <<= 1;
        if (__builtin_expect(vc > 3, 0)) {
            hist |= 1u;
            asm volatile("s_waitcnt vmcnt(3)");
            now = (int)(clk() - base);
            if (now > slot(n) + THR && prev < PRV && big == 0 && off + SLIP <= MAXOFF && body >= NOSLK
                && (int)(base + now - __builtin_amdgcn_readfirstlane(ld_wg(&sm.quiet))) >= 0) {
                if (lane == 0 && __hip_atomic_fetch_add(&sm.vote[body], 1u, __ATOMIC_RELAXED, __HIP_MEMORY_SCOPE_WORKGROUP) + 1 >= VOTES)
                    __hip_atomic_fetch_max(&sm.slip, off + SLIP, __ATOMIC_RELAXED, __HIP_MEMORY_SCOPE_WORKGROUP);
                unsigned o2;
                while ((o2 = __builtin_amdgcn_readfirstlane(ld_wg(&sm.slip))) == off && (int)(clk() - base) < now + VWAIT) {}
                if (o2 != off) { follow(o2); now = pace(slot(n)); }
            }
            if (hold == 0 && off + SS <= MAXOFF && body >= NOSM && body % NH != 21 && __builtin_popcount(hist & DBM) >= DBN && lane == 0) st_wg(&sm.req, off + SS);
        }
    };
    auto check = [&](auto N) {
        constexpr int n = decltype(N)::value;
        prev = now - slot(n);
        if (lane == 0) st_wg(&sm.late[wave], (unsigned)prev);
    };
    auto getslip = [&]() { offv = __hip_atomic_load(&sm.slip, __ATOMIC_RELAXED, __HIP_MEMORY_SCOPE_WORKGROUP); };
    auto arrive = [&](int i) {
        asm volatile("" ::: "memory");
        if (lane == 0) __hip_atomic_fetch_add(&sm.cnt[i], 1u, __ATOMIC_RELAXED, __HIP_MEMORY_SCOPE_WORKGROUP);
    };
    auto next_body = [&]() {
        const unsigned s2 = sched + BODY16;
        base += (s2 >> 16) - (sched >> 16);
        sched = s2;
        ++body;
    };

    {
        v4i pv[2], pq[2];
        pq[0] = reinterpret_cast<const v4i*>(q)[tid];
        pq[1] = reinterpret_cast<const v4i*>(q)[NT + (tid & 255)];
        const unsigned vo = pg * 128 + (tid & 7) * 16;
        PST(1); pace(slot(3) * PF / 100); SB();
        pk[3] = ldstream((const char*)kz + po);
        pk[4] = ldstream((const char*)kz + po + 128);
        pk[5] = ldstream((const char*)kz + po + 256);
        const int pl = (wave & 1) * 32 + (lane & 31);
        const bool isk = pl < 48, isz = isk ? pl >= 24 : pl >= 56;
        const char* par = (const char*)(isk ? isz ? kz : ks : isz ? vz : vs) + (isk ? pl % 24 : pl & 7) * 16
                        + group(tau * 4 + (wave >> 1), px) * (isk ? DH * 2 : 128);
        asm volatile("" : "+v"(par));
        PST(2); pace(slot(6) * PF / 100); SB();
        pv[0] = ldstream((const char*)vs + vo);
        pv[1] = ldstream((const char*)vz + vo);
        v4i pp = __builtin_nontemporal_load((const __attribute__((address_space(1))) v4i*)par);
        dsc[0] = describe(0, 0, 0, 0u, 0u, 0u);
        dsc[1] = describe(0, 0, 1, 0u, 0u, 0u);
        PST(3); pace(slot(8) * PF / 100); SB();
        burst((const char*)qk, 0, lanes(dsc[0]));
        PST(4);
        sm.qt[tid] = pq[0];
        if (tid < 256) sm.qt[NT + tid] = pq[1];
#pragma unroll
        for (int u = 0; u < 3; ++u) sm.ksz[(tid >> 3) * 48 + u * 8 + (tid & 7)] = pk[u];
#pragma unroll
        for (int u = 0; u < 3; ++u) {
            const v4i x = pk[3 + u];
            sm.ksz[(tid >> 3) * 48 + 24 + u * 8 + (tid & 7)] = v4i{(int)negscale(x.x), (int)negscale(x.y), (int)negscale(x.z), (int)negscale(x.w)};
        }
        PST(5); pace(slot(11) * PF / 100); SB();
        burst((const char*)qk, 1, lanes(dsc[1]));
        PST(6);
        descv[0] = describe(0, lane, 0, 0u, 0u, 0u);
        descv[1] = describe(0, lane, 1, 0u, 0u, 0u);
        SB();
        sm.vsz[0][tid] = pv[0];
        sm.vsz[1][tid] = v4i{(int)negscale(pv[1].x), (int)negscale(pv[1].y), (int)negscale(pv[1].z), (int)negscale(pv[1].w)};
        if (isz) pp = v4i{(int)negscale(pp.x), (int)negscale(pp.y), (int)negscale(pp.z), (int)negscale(pp.w)};
        *(isk ? &sm.ksz[(64 + (wave >> 1)) * 48 + pl] : &sm.vsz[isz][(64 + (wave >> 1)) * 8 + (pl & 7)]) = pp;
    }
    asm volatile("" :: "v"(warm));
    PST(7);
    __syncthreads();
    PST(8);
    base = __builtin_amdgcn_readfirstlane(sm.t0) + OFF0 + phase;

    const v2f huge = {0x1p127f, 0x1p127f};

    for (int S = 0; S < NH; ++S) {
        v2f kz2[6];
        unsigned slo[6], shi[6];
        v2u qq[3];
        const char* nsup = (const char*)(S + 1 < NH ? qk : qv);
        const int head = S >> 1 == 10 ? S : (S & ~1) + win;
        float mylogit = 0.f;
        float dot = 0.f;
        sfor<12>([&](auto N) {
            constexpr int n = decltype(N)::value;
            constexpr int i = n / 3, j = n % 3;
            if (n == 6 && (S == H0 - 1 || S == NH - 1)) fetch(S == NH - 1);
            if (j == 0) {
                const unsigned d = dsc[i ^ 2] = __builtin_amdgcn_readlane(descv[i & 1], (2 * S + 1 + (i >> 1)) & 63);
                const unsigned o = lanes(d);
                base += (d >> 4) & 31u;
                pacen(N);
                SB();
                burst(i < 2 ? (const char*)qk : nsup, i ^ 2, o);
                SB();
                if (tid == NT - 1) st_wg(&sm.tick, S * 4 + i + 1);
                check(N);
            }
            if (j == 2) getslip();
            if (i % 2 == 0 || ((dsc[i] ^ dsc[i ^ 1]) & 4u)) {
                const int it = S * 2 + (i >> 1), sl = dsc[i] & 4u ? 64 + (it & 3) : it;
                const v2u* pk = reinterpret_cast<const v2u*>(&sm.ksz[sl * 48]);
                const v2u a = pk[j * 16 + r], b = pk[48 + j * 16 + r];
                slo[j * 2 + 0] = a.x & 0xffffu; shi[j * 2 + 0] = a.x & 0xffff0000u;
                slo[j * 2 + 1] = a.y & 0xffffu; shi[j * 2 + 1] = a.y & 0xffff0000u;
                kz2[j * 2 + 0] = v2f{bf_lo(b.x), bf_hi(b.x)};
                kz2[j * 2 + 1] = v2f{bf_lo(b.y), bf_hi(b.y)};
            }
            if (i == 0) qq[j] = reinterpret_cast<const v2u*>(&sm.qt[head * 24])[j * 16 + r];
            if (j == 0) dot = 0.f;
            {
                const v4i x = buf[n % RING];
                const v2f t0 = __builtin_elementwise_fma(v2f{__int_as_float(x.x), __int_as_float(x.y)}, huge, kz2[j * 2]);
                const v2f t1 = __builtin_elementwise_fma(v2f{__int_as_float(x.z), __int_as_float(x.w)}, huge, kz2[j * 2 + 1]);
                float y[4];
                mul4(y, pack(t0.x, t0.y), pack(t1.x, t1.y), slo[j * 2], shi[j * 2], slo[j * 2 + 1], shi[j * 2 + 1]);
                dot = dot2(pack(y[0], y[1]), qq[j].x, dot);
                dot = dot2(pack(y[2], y[3]), qq[j].y, dot);
            }
            if (j == 2) {
                const float d = rowsum16(dot);
                if ((r & 3) == (i ^ ((dsc[i] >> 1) & 1))) mylogit = d;
            }
            asm volatile("" : "+v"(dot), "+v"(mylogit));
            SB();
        });
        if (r < 4) sm.p[S][wave][g * 4 + r] = (unsigned short)pack(rb(mylogit * 0x1p22f) * scale, 0.f);
        if ((S & 3) == 3) arrive(NH + (S >> 2));
        if (S == NH - 3 && tid == 0) st_wg(&sm.done, 1u);
        next_body();
    }

    for (int S = 0; S < NH; ++S) {
        v2u P;
        unsigned vsl, vsh, vzf;
        v4f acc = {0.f, 0.f, 0.f, 0.f};
        float y0[12];
        unsigned b0[12];
        const bool more = S + 1 < NH;
        unsigned sl, sh;
        v2u A[12];
        v2f z2;
        sfor<12>([&](auto N) {
            constexpr int n = decltype(N)::value;
            constexpr int i = n / 3, j = n % 3;
            if (j == 0) {
                const unsigned d = dsc[i ^ 2] = __builtin_amdgcn_readlane(descv[i & 1], (2 * S + 1 + (i >> 1)) & 63);
                unsigned o = lanes(d);
                const char* src = (const char*)qv;
                if (i >= 2 && !more) {
                    o = DUM ? 0 : i * 768;
                    src = !DUM ? dummy : i == 3 && wave == 0 ? (const char*)out + min(mh, NH - 2) * (DH * 2) : (const char*)ws + (i & 1) * (15 << 18) + blockIdx.x * 768;
                }
                if (i < 2 || more) base += (d >> 4) & 31u;
                if (i < 2 || more) pacen(N);
                SB();
                burst(src, i ^ 2, o);
                SB();
                if (tid == NT - 1) st_wg(&sm.tick, 4 * NH + S * 4 + i + 1);
                check(N);
            }
            if (n == 0) {
                const unsigned need = S < 2 ? 1 : S;
                while (__builtin_amdgcn_readfirstlane(ld_wg(&sm.prog)) < need) {}
                asm volatile("" ::: "memory");
                P = *reinterpret_cast<const v2u*>(&sm.p[S][wave][g * 4]);
                P.x = __builtin_amdgcn_alignbit(P.x, P.x, (dsc[0] & 2u) << 3);
                P.y = __builtin_amdgcn_alignbit(P.y, P.y, (dsc[2] & 2u) << 3);
                const unsigned d3 = __builtin_amdgcn_readlane(descv[1], 2 * S + 1);
                const unsigned x = (((dsc[0] & 7u) | (dsc[1] & 7u) << 8) | ((dsc[2] & 7u) << 16 | (d3 & 7u) << 24)) >> (r & 3) * 8;
                const int it = S * 2 + ((r >> 1) & 1);
                const int ltok = (x & 4u ? 64 + (it & 3) : it) * 64 + (x & 1u ? tod : tev);
                vsl = reinterpret_cast<const unsigned short*>(&sm.vsz[0][0])[ltok];
                vsh = vsl << 16;
                vzf = (unsigned)reinterpret_cast<const unsigned short*>(&sm.vsz[1][0])[ltok] << 16;
            }
            if (j == 2) getslip();
            if (j == 0) {
                sl = dppu<0x55 * i>(vsl);
                sh = dppu<0x55 * i>(vsh);
                const float zb = __uint_as_float(dppu<0x55 * i>(vzf));
                z2 = v2f{zb, zb};
            }
            float y[4];
            {
                const v4i x = buf[n];
                const v2f t0 = __builtin_elementwise_fma(v2f{__int_as_float(x.x), __int_as_float(x.y)}, huge, z2);
                const v2f t1 = __builtin_elementwise_fma(v2f{__int_as_float(x.z), __int_as_float(x.w)}, huge, z2);
                mul4(y, pack(t0.x, t0.y), pack(t1.x, t1.y), sl, sh, sl, sh);
            }
#pragma unroll
            for (int e = 0; e < 4; ++e) {
                const int idx = j * 4 + e;
                if (i % 2 == 0) {
                    y0[idx] = y[e];
                    asm volatile("" : "+v"(y0[idx]));
                    if (i == 2) {
                        A[idx] = v2u{r == idx ? P.x : 0u, r == idx ? P.y : 0u};
                        if (AH) asm volatile("" : "+v"(A[idx]));
                    }
                } else if (i == 1) {
                    b0[idx] = pack(y0[idx], y[e]);
                    asm volatile("" : "+v"(b0[idx]));
                } else {
                    const v2u B = {b0[idx], pack(y0[idx], y[e])};
                    acc = __builtin_amdgcn_mfma_f32_16x16x16bf16_1k(__builtin_bit_cast(v4s, A[idx]), __builtin_bit_cast(v4s, B), acc, 0, 0, 0);
                }
            }
            if (n == 11) {
                if (S == NH - 2 && tid == 0) sm.tref = base + slot(12) + slot(3);
                if (S == NH - 3 && tid == 0) st_wg(&sm.done, 2u);
                if (lane < 48) *reinterpret_cast<v4f*>(&sm.stage[S & 1][wave][lane * 4]) = acc;
                arrive(S);
            }
            SB();
        });
        next_body();
    }
    {
        const unsigned tag = tagof(sm.epoch, lane & 3);
        const float* src = ws + WS_R + ((size_t)((both ? 0 : mh & 1) * (NWG / 2) + wave * 16) * NRC + (mh >> 1) + (mh > 20)) * RS + msl * 16;
        const unsigned so = (lane >> 2) * (NRC * RS * 4) + (lane & 3) * 16;
        prepare();
        until(base - slot(12) + slot(3) - phase + (wave - (WPB - 1)) * STG + (mh >= NH - 2 ? TPL : both ? TPC - TPB : TPC));
        for (;;) {
            x[0] = ld_rec(src, so);
            x[1] = v4i{0, 0, 0, (int)tag};
            if (both) x[1] = ld_rec(src + NWG / 2 * NRC * RS, so);
            asm volatile("s_waitcnt vmcnt(0)" : "+v"(x[0]), "+v"(x[1]));
            if (__all((((unsigned)x[0].w ^ tag) | ((unsigned)x[1].w ^ tag)) < 256u) || clk() - tb >= TMO) break;
            __builtin_amdgcn_s_sleep(PSL);
        }
#pragma unroll
        for (int d = 5; d >= 0; --d) asm volatile("" :: "v"(buf[d]));
        x[0].w ^= tag;
        x[1].w ^= tag;
    }
    } else
    if (wave == WPB) {
        const int l = lane;
        u64* echo = tstart + NWG;
        u64* gofs = tstart + 3 * NWG;
        if (l == 0) {
            if (blockIdx.x < NR) st_dev(gofs + blockIdx.x * 32, tb64 - 100000ull);
            if (blockIdx.x == NR) st_dev(gofs + 16, tb64 - 100000ull);
            sm.slip = 0;
            sm.req = 0;
            sm.done = 0;
            sm.prog = 0;
            sm.ready = 0;
            sm.tick = 0;
            sm.quiet = tb;
            sm.fcnt = 0;
            sm.epoch = tb64;
            sm.have = blockIdx.x == 0;
        }
        if (l < NH + NH / 4) sm.cnt[l] = 0;
        sm.vote[l] = 0;
        until(tb + TGATH);
        unsigned ld;
        for (;;) {
            const u64 dt = ld_dev(tstart + NWG - 8 + (l & 7)) - tb64 + 1000ull;
            ld = dt < 2000ull ? (unsigned)dt : 0u;
            if (__all(dt < 2000ull) || clk() - tb > (unsigned)TGMAX) break;
        }
        unsigned sum = ld + dppu<0x124>(ld);
        sum += dppu<0x122>(sum);
        sum += dppu<0x121>(sum);
        const bool allv = __all(ld != 0);
        ld = max(ld, dppu<0x124>(ld));
        ld = max(ld, dppu<0x122>(ld));
        ld = max(ld, dppu<0x121>(ld));
        if (MEANF && allv) ld -= (ld - sum / 8) * MEANF / 4;
        ld = __builtin_amdgcn_readfirstlane(ld);
        const u64 t64 = ld ? tb64 + ld - 1000ull : tb64;
        if (l == 0) sm.t0 = (unsigned)t64;
        u64 to[NWG / 64];
        bool have = blockIdx.x == 0;
        auto chores = [&](bool all) {
            if (blockIdx.x == 0) {
                u64 x[NWG / 64];
#pragma unroll
                for (int u = 0; u < NWG / 64; ++u) x[u] = ld_dev(tstart + u * 64 + l);
#pragma unroll
                for (int u = 0; u < NWG / 64; ++u) {
                    if (all || __any(x[u] != to[u])) st_rec(echo + u * 128, l * 16, v4i{(int)(unsigned)x[u], (int)(unsigned)(x[u] >> 32), (int)tb, (int)(unsigned)(tb64 >> 32)});
                    to[u] = x[u];
                }
            } else if (!have && !all) {
                v4i x = ld_rec(echo + blockIdx.x * 2, 0);
                asm volatile("s_waitcnt vmcnt(0)" : "+v"(x));
                if ((unsigned)x.x == tb && (unsigned)x.y == (unsigned)(tb64 >> 32)) {
                    have = true;
                    if (l == 0) sm.epoch = (u64)(unsigned)x.w << 32 | (unsigned)x.z;
                    asm volatile("s_waitcnt lgkmcnt(0)" ::: "memory");
                    if (l == 0) st_wg(&sm.have, 1u);
                }
            }
        };
        if (EARLY) chores(true);
        asm volatile("s_waitcnt lgkmcnt(0)\n\ts_barrier" ::: "memory");
        asm volatile("" :: "v"(warm));
        if (!EARLY) chores(true);
        until(tb + TECHO);
        behind();
        chores(false);
        const u64* mine = NR > 1 ? gofs + ((blockIdx.x >> 3) % NR) * 32 : gofs + 16;
        unsigned sent = 0, stage = 0, tsh = clk();
        int nt = blockIdx.x < 200, spin = 0;
        while (stage < 2) {
            if (PB > 1 && have) __builtin_amdgcn_s_sleep(PSLEEP);
            behind();
            unsigned tch = 0;
            if (nt < 25) {
                const unsigned pg = (blockIdx.x >> 3) + 32 * (nt % 12);
                const char* tp = (nt < 12 ? (const char*)qk : nt < 24 ? (const char*)qv : (const char*)ws) + ((size_t)pg << 21);
                const bool first = PTL && (pg == 0 || (((size_t)tp >> 21) & (PTL - 1)) == 0);
                tp += (((first ? blockIdx.x >> 3 : blockIdx.x) * 29 + nt) & 255) * 256;
                if (nt == 24 && blockIdx.x >= 16) tp = (const char*)out + min((pg - 2) * 4096, NH * DH * 2 - 4u);
                if (ld_wg(&sm.tick) + (first ? TLEAD + 4 : TLEAD) >= (unsigned)(nt / 12 * 128 + nt % 12 * 4 / 3 * 8 - nt / 24 * 32)) {
                    unsigned x;
                    if (nt < 24 && !first) asm volatile("s_nop 4\n\tglobal_load_dword %0, %1, %2 nt" : "+v"(tch) : "v"(0), "s"(tp) : "memory");
                    else if (nt < 24 || blockIdx.x < 48) asm volatile("s_load_dword %0, %1, 0x0\n\ts_waitcnt lgkmcnt(0)" : "=s"(x) : "s"(tp) : "memory");
                    ++nt;
                }
            }
            const u64 gd = ld_dev(mine) - t64;
            asm volatile("s_waitcnt vmcnt(0)" : "+v"(tch));
            const unsigned go = gd <= (u64)MAXOFF ? (unsigned)gd : 0u;
            const unsigned own = ld_wg(&sm.slip);
            if (go > own && l == 0) __hip_atomic_fetch_max(&sm.slip, go, __ATOMIC_RELAXED, __HIP_MEMORY_SCOPE_WORKGROUP);
            if (own <= go) tsh = clk();
            else if (clk() - tsh > (unsigned)REVT && blockIdx.x >= NSENT) { if (l == 0) st_wg(&sm.slip, go); }
            unsigned req = ld_wg(&sm.req);
            if ((int)(clk() - ld_wg(&sm.quiet)) < 0) { req = 0; if (l == 0) st_wg(&sm.req, 0u); }
            unsigned want = blockIdx.x < NSENT ? own : 0u;
            if (req == go + SS && own == go) {
                unsigned x = ld_wg(&sm.late[l & 7]);
                x += dppu<0x121>(x);
                x += dppu<0x122>(x);
                x += dppu<0x124>(x);
                want = min(req + (__builtin_amdgcn_readfirstlane(x) * LFAC / 64 & ~15u), (unsigned)MAXOFF);
            }
            if (want > go && want > sent) {
                u64 old = 0;
                behind();
                if (l == 0) st_wg(&sm.quiet, clk() + TQA);
                if (l == 0) old = __hip_atomic_fetch_max(gofs + 16, t64 + want, __ATOMIC_RELAXED, __HIP_MEMORY_SCOPE_AGENT);
                if (NR > 1 && __builtin_amdgcn_readfirstlane((unsigned)(old < t64 + want)) && l < NR)
                    __hip_atomic_fetch_max(gofs + l * 32, t64 + want, __ATOMIC_RELAXED, __HIP_MEMORY_SCOPE_AGENT);
                sent = want;
            }
            if (!have || (blockIdx.x == 0 && ++spin % 8 == 0)) { behind(); chores(false); }
            if (ld_wg(&sm.done) > stage) {
                ++stage;
                behind();
                asm volatile("" : "+s"(code));
                int w = 0;
#pragma unroll
                for (int u = 0; u < 4; ++u) {
                    const int a = (u * 64 + l) * 128;
                    w ^= *(gptr)(code + (a < codelen ? a : 0));
                }
                asm volatile("" :: "v"(w));
            }
        }
        while (!have && clk() - tb < TMO) chores(false);
        if (l == 0) st_wg(&sm.have, 1u);
        if (blockIdx.x == 0) {
            for (spin = 1; ld_wg(&sm.fcnt) < WPB; ++spin) {
                __builtin_amdgcn_s_sleep(2);
                if (spin % 1024 == 0) chores(false);
            }
            return;
        }
        if (!DRY) return;
        prepare();
    } else {
        u64* table = tstart + 3 * NWG + NR * 32;
        const unsigned ent = blockIdx.x - CAL0;
        int lead = 0, lat = 0;
        if (PRE) {
            v4i y[PRE ? PRE : 1];
#pragma unroll
            for (int u = 0; u < PRE; ++u) y[u] = *(const v4i*)((const char*)(ws + WS_R) + (pos * NRC + NRC - 1 - (PRE > 1 ? u * 2 + (lane >> 5) : 0)) * (RS * 4) + (lane & 31) * 16);
#pragma unroll
            for (int u = 0; u < PRE; ++u) asm volatile("" :: "v"(y[u]));
        }
        __syncthreads();
        if (ent < 64 && (ent & 31) < 24 && (ent & 3) < NMEM) {
            const unsigned t0 = __builtin_amdgcn_readfirstlane(sm.t0);
            unsigned page, x[4];
            auto pair = [&](unsigned k) {
                const unsigned h = (t0 + k + (ent & ~3u) * 2) * 0x9e3779b1u;
                page = (ent & 28) * 16 + (k & 1) * 32 + 24 + ((t0 + (k & 1)) * 0x85ebca6bu >> 29);
                return (const char*)(ent < 32 ? +qk : +qv) + ((size_t)page << 21) + (h >> 25) * 16384 + ((h >> 8) & 127) * 64;
            };
            asm volatile("s_load_dword %0, %4, 0x0\n\ts_load_dword %1, %4, 0x2000\n\ts_load_dword %2, %5, 0x0\n\t"
                         "s_load_dword %3, %5, 0x2000\n\ts_waitcnt lgkmcnt(0)"
                         : "=&s"(x[0]), "=&s"(x[1]), "=&s"(x[2]), "=&s"(x[3]) : "s"(pair(ent % 4 * 2)), "s"(pair(ent % 4 * 2 + 1)) : "memory");
            until(t0 + TPRB);
#pragma nounroll
            for (unsigned k = 2; k < 2 * NMEM; ++k) {
                u64 ta, tc, td;
                asm volatile("s_memtime %0\n\ts_waitcnt lgkmcnt(0)\n\ts_load_dword %3, %4, 0x0\n\ts_waitcnt lgkmcnt(0)\n\t"
                             "s_memtime %1\n\ts_waitcnt lgkmcnt(0)\n\ts_load_dword %3, %4, 0x2000\n\ts_waitcnt lgkmcnt(0)\n\t"
                             "s_memtime %2\n\ts_waitcnt lgkmcnt(0)"
                             : "=&s"(ta), "=&s"(tc), "=&s"(td), "=&s"(x[0]) : "s"(pair((ent % 4 * 2 + k) % (2 * NMEM))) : "memory");
                const int d0 = (int)(tc - ta), d1 = (int)(td - tc), d = max(-PCL, min(PCL, d1 - d0));
                if (min(d0, d1) > 350 && d0 + d1 < 1700) lead += (page ^ (page >> 7)) & 1 ? -d : d;
                lat += d0 + d1;
            }
        }
        asm volatile("" :: "v"(warm));
        while (ld_wg(&sm.have) == 0 && clk() - tb < TMO) __builtin_amdgcn_s_sleep(4);
        asm volatile("" ::: "memory");
        const u64 E = sm.epoch;
        if (ent < 64 && (ent & 31) < 24 && (ent & 3) < NMEM) {
            unsigned xcc;
            asm volatile("s_getreg_b32 %0, hwreg(HW_REG_XCC_ID, 0, 3)" : "=s"(xcc));
            if (xcc & 4) lead = -lead;
            behind();
            if (lane == 0) st_wg(&sm.quiet, clk() + TQ);
            if (lane < 8) st_rec(table + ent * 32, lane * 16, v4i{lead, (int)(xcc - blockIdx.x), (int)(unsigned)E, (int)(unsigned)(E >> 32)});
        }
        unsigned jobs = 0;
        v4i cal;
        until(tb + TTAB);
        for (;;) {
            behind();
            cal = ld_rec(table, ((lane & 31) < 24 && (lane & 3) < NMEM ? lane : 0) * 256);
            asm volatile("s_waitcnt vmcnt(0)" : "+v"(cal));
            if (__all((unsigned)cal.z == (unsigned)E && (unsigned)cal.w == (unsigned)(E >> 32)) || clk() - tb >= TMO) break;
        }
        {
            int sum = (lane & 3) < NMEM ? cal.x : 0;
            sum += (int)dppu<0xB1>(sum);
            sum += (int)dppu<0x4E>(sum);
            const u64 pol = __ballot(sum < 0) ^ FPOL, ok = __ballot(abs(sum) >= PTH) & FMASK;
            const unsigned side = ((unsigned)__builtin_amdgcn_readfirstlane(cal.y) + (blockIdx.x & ~4u)) >> 2;
#pragma unroll
            for (int u = 0; u < 4; ++u) {
                sm.desc[u >> 1][u & 1][lane] = describe(u >> 1, lane, u & 1, pol, ok, side);
                jobs |= (tau > 0 ? job(u >> 1, 2 * tau + (u & 1), pol, ok, side) : pos & 1u) << u;
            }
            asm volatile("" ::: "memory");
            if (lane == 0) st_wg(&sm.ready, 1u);
        }
#pragma nounroll
        for (int cc = 0; cc <= NH / 4; ++cc) {
            const int c = cc < NH / 4 ? cc : tau >> 1;
            const bool act = (tau > 0 && c * 2 + (g >> 1) == tau) == (cc == NH / 4);
            while (ld_wg(&sm.cnt[NH + c]) < WPB) __builtin_amdgcn_s_sleep(2);
            asm volatile("" ::: "memory");
            if (cc >= NH / 4 - 1) {
                const bool sw = ((jobs ^ (jobs >> 2)) >> (lane >> 5)) & 1u;
                v2u* mine = reinterpret_cast<v2u*>(&sm.p[2 * tau][0][0]) + lane;
                if (cc < NH / 4) {
                    if (lane == 0) st_wg(&sm.quiet, clk() + TXW + TQ);
                    if (sw) st_rec(ws + WS_X + pos * 256, lane * 16, v4i{(int)mine->x, (int)mine->y, (int)(unsigned)E, (int)(unsigned)(E >> 32)});
                } else if (__any(sw)) {
                    until(clk() + TXW);
                }
                while (cc == NH / 4 && __any(sw)) {
                    v4i x = {};
                    if (sw) asm volatile("s_nop 4\n\tglobal_load_dwordx4 %0, %1, %2 sc1 nt" : "+v"(x) : "v"(lane * 16), "s"(ws + WS_X + (pos ^ 1) * 256) : "memory");
                    asm volatile("s_waitcnt vmcnt(0)" : "+v"(x));
                    if (__all(!sw || ((unsigned)x.z == (unsigned)E && (unsigned)x.w == (unsigned)(E >> 32))) || clk() - tb >= TMO) {
                        if (sw) *mine = v2u{(unsigned)x.x, (unsigned)x.y};
                        break;
                    }
                    __builtin_amdgcn_s_sleep(8);
                }
                asm volatile("" ::: "memory");
            }
            const bool two = c * 2 + (g >> 1) != 10;
            v4i* pp = reinterpret_cast<v4i*>(&sm.p[c * 4][0][0]) + lane;
            const v4i u = *pp;
            float x[8];
#pragma unroll
            for (int e = 0; e < 4; ++e) { x[2 * e] = bf_lo(u[e]); x[2 * e + 1] = bf_hi(u[e]); }
            float M = fmaxf(fmaxf(fmaxf(x[0], x[1]), fmaxf(x[2], x[3])), fmaxf(fmaxf(x[4], x[5]), fmaxf(x[6], x[7])));
            M = fmaxf(M, dpp<0x128>(M));
            M = fmaxf(M, dpp<0x124>(M));
            M = fmaxf(M, dpp<0x122>(M));
            M = fmaxf(M, dpp<0x121>(M));
            {
                auto y = __builtin_amdgcn_permlane16_swap(__float_as_uint(M), __float_as_uint(M), false, false);
                if (two) M = fmaxf(__uint_as_float(y[0]), __uint_as_float(y[1]));
            }
            v4i o;
            float L = 0.f;
#pragma unroll
            for (int e = 0; e < 4; ++e) {
                const unsigned pk = pack(__expf(x[2 * e] - M), __expf(x[2 * e + 1] - M));
                o[e] = (int)pk;
                L += bf_lo(pk) + bf_hi(pk);
            }
            if (act) *pp = o;
            L = rowsum16(L);
            const float L2 = fold16(L, L);
            if (two) L = L2;
            if (r == 0 && act) sm.ml[c * 4 + g] = __float_as_uint(M) | __builtin_bit_cast(unsigned short, (_Float16)L);
            asm volatile("" ::: "memory");
            if (lane == 0) st_wg(&sm.prog, 1u);
        }
        const unsigned tag = tagof(E, lane & 3);
        auto place = [&](int i) {
            const int i0 = tau + (tau > 10);
            const unsigned jb = i == i0 ? jobs >> 2 : i == 11 && tau == 10 ? jobs >> 3 : pos;
            return (((pos & ~1) | (jb & 1)) * NRC + i) * (RS * 4) + (lane & 31) * 16;
        };
        const unsigned last = place(NRC - 1);
        v2f keep[3] = {};
#pragma nounroll
        for (int h = 0; h < NH; ++h) {
            const unsigned mlb = (sm.ml[h] >> (lane & 3) * 8) & 0xffu;
            if (h < NH - 1) { while (ld_wg(&sm.cnt[h]) < WPB) __builtin_amdgcn_s_sleep(4); }
            else { while (ld_wg(&sm.cnt[h]) < WPB) {} }
            asm volatile("" ::: "memory");
            v2f c[4][3];
#pragma unroll
            for (int e = 0; e < 4; ++e)
#pragma unroll
                for (int k = 0; k < 3; ++k) c[e][k] = *reinterpret_cast<const v2f*>(&sm.stage[h & 1][(lane >> 5) * 4 + e][(lane & 31) * 6 + k * 2]);
            v4i x;
            const bool first = (h & 1) == 0 && h != 20;
#pragma unroll
            for (int k = 0; k < 3; ++k) {
                const v2f a = (keep[k] + (c[0][k] + c[1][k])) + (c[2][k] + c[3][k]), b = a * 0x1p22f;
                x[k] = (int)pack(fold32(b.x, b.x), fold32(b.y, b.y));
                keep[k] = first ? a : v2f{0.f, 0.f};
            }
            x[3] = (int)(tag | mlb);
            if (h == NH - 1) {
                if (lane < 32) {
                    if (BNT & 2) asm volatile("s_nop 4\n\tglobal_store_dwordx4 %0, %1, %2 sc1 nt\n\ts_nop 1" :: "v"(last), "v"(x), "s"(ws + WS_R) : "memory");
                    else st_rec(ws + WS_R, last, x);
                }
                break;
            }
            if (!first) sm.rec[(h >> 1) + (h > 20)][lane & 31] = x;
            asm volatile("" ::: "memory");
            if (lane == 0) st_wg(&sm.prog, h + 2);
            if (h == NH - 2) {
                v4i y[NRC / 2];
                unsigned o[NRC / 2];
#pragma unroll
                for (int u = 0; u < NRC / 2; ++u) { y[u] = (&sm.rec[0][0])[u * 64 + lane]; o[u] = place(u * 2 + (lane >> 5)); }
                until(ld_wg(&sm.tref) + TREL);
#pragma unroll
                for (int u = 0; u < NRC / 2; ++u) {
                    if (BNT & 1) asm volatile("s_nop 4\n\tglobal_store_dwordx4 %0, %1, %2 sc1 nt\n\ts_nop 1" :: "v"(o[u]), "v"(y[u]), "s"(ws + WS_R) : "memory");
                    else st_rec(ws + WS_R, o[u], y[u]);
                }
            }
        }
        return;
    }

    {
        asm volatile("" : "+v"(x[0]), "+v"(x[1]));
        auto gather = [&](unsigned w) {
            w <<= mls;
            w |= dppu<0xB1>(w);
            return w | dppu<0x4E>(w);
        };
        auto half = [](unsigned u) { return (float)__builtin_bit_cast(_Float16, (unsigned short)u); };
        const unsigned ml = gather(x[0].w);
        float m = bf_hi(ml), l = half(ml), a[6];
#pragma unroll
        for (int e = 0; e < 3; ++e) { a[2 * e] = bf_lo(x[0][e]); a[2 * e + 1] = bf_hi(x[0][e]); }
        if (both) {
            const unsigned m2 = gather(x[1].w);
            const float mx = fmaxf(m, bf_hi(m2)), w0 = __expf(m - mx), w1 = __expf(bf_hi(m2) - mx);
#pragma unroll
            for (int e = 0; e < 3; ++e) {
                a[2 * e] = a[2 * e] * w0 + bf_lo(x[1][e]) * w1;
                a[2 * e + 1] = a[2 * e + 1] * w0 + bf_hi(x[1][e]) * w1;
            }
            l = l * w0 + half(m2) * w1;
            m = mx;
        }
        const float M = x4max(m), w0 = __expf(m - M);
        l *= w0;
        float c0 = fold16(fold32(a[0] * w0, a[4] * w0), fold32(a[2] * w0, l));
        float c1 = fold16(fold32(a[1] * w0, a[5] * w0), fold32(a[3] * w0, l));
        c0 += dpp<0x128>(c0); c1 += dpp<0x128>(c1);
        c0 += dpp<0x124>(c0); c1 += dpp<0x124>(c1);
        if (g == 3) c1 = M;
        unsigned arr;
        v4f y;
        float mw;
        asm volatile("ds_write_b64 %3, %4\n\tds_add_rtn_u32 %0, %5, %6\n\tds_read_b32 %2, %8\n\tds_read_b128 %1, %7\n\ts_waitcnt lgkmcnt(0)"
                     : "=&v"(arr), "=&v"(y), "=&v"(mw) : "v"(fa[0]), "v"(v2f{c0, c1}), "v"(fa[1]), "v"(one), "v"(fa[2]), "v"(fa[3]) : "memory");
        const float w = __expf(mw - x4max(mw));
        float d = fold16(fold32(y.x * w, y.z * w), fold32(y.y * w, y.w * w));
        d += dpp<0x128>(d);
        const float L = __int_as_float(__builtin_amdgcn_readlane(__float_as_int(d), 6));
        const unsigned o = pack(d * __builtin_amdgcn_rcpf(L), 0.f);
        if (__builtin_amdgcn_readfirstlane(arr) == WPB - 1 && wave < WPB) {
            if (r < 6) *(unsigned short*)((char*)out + oo) = (unsigned short)o;
        }
    }
}
__global__ void kivi_end_marker() { asm volatile("kivi_code_end:"); }

extern "C" void kernel_launch(void* const* d_in, const int* in_sizes, int n_in,
                              void* d_out, int out_size, void* d_ws, size_t ws_size,
                              hipStream_t stream) {
    const __hip_bfloat16* q = (const __hip_bfloat16*)d_in[0];
    const int* qk = (const int*)d_in[1];
    const int* qv = (const int*)d_in[2];
    const __hip_bfloat16* ks = (const __hip_bfloat16*)d_in[3];
    const __hip_bfloat16* kz = (const __hip_bfloat16*)d_in[4];
    const __hip_bfloat16* vs = (const __hip_bfloat16*)d_in[5];
    const __hip_bfloat16* vz = (const __hip_bfloat16*)d_in[6];
    const float scale = (float)(1.0 / sqrt((double)DH));
    Order order;
    for (int b = 0; b < NWG / 2; ++b) {
        const int p = position(b);
        order.g[b] = groupof(p) | groupof(p + 128) << 8 | groupof(p ^ 1) << 16 | groupof((p ^ 1) + 128) << 24;
    }
    kivi_stream_kernel<<<NWG, NT + 128, 0, stream>>>(q, qk, qv, ks, kz, vs, vz, (float*)d_ws, (__hip_bfloat16*)d_out, scale, order);
}
```

```cpp
#define DIAG 14
#include <hip/hip_runtime.h>
#include <hip/hip_bf16.h>
#include <stdint.h>
#include <utility>

#define TGT 199.25
#define WST 16
#define THR 80
#define PRV 16
#define SLIP 250
#define MAXOFF 3000
#define NSENT 8
#define NR 32
#define TPL 265
#define STG 4
#define PSLEEP 16
#define OFF0 380
#define TGATH 150
#define GAP1 10
#define TREL 110
#define NSLOW 1
#define EXK 7
#define NOSLK 2
#define TMO 100000000u
#define TPC 265
#define PSL 2
#define DST 0
#define BA 2
#define BB 16
#define TGMAX 300
#define PSH 0
#define SS 20
#define HOLD 4
#define VOTES 4
#define HOLDB 8
#define REVT 600
#define VWAIT 30
#define LFAC 4
#define NOSM 2
#define H0 5
#define TGS 203.0
#define SPL 2
#define G21 5
#define EXS(i) ((int)(((i) + 1) * (TGS - TGT) * 100.0 / 256.0 + 0.5) - (int)((i) * (TGS - TGT) * 100.0 / 256.0 + 0.5))
#define CAL0 128
#define PTH (NMEM * (NMEM - 1) * 150)
#define NMEM 2
#define PCL 500
#define TPRB 580
#define DBM 7
#define DBN 2
#define TQA 500
#define PB 1
#define PTL 8
#define EARLY 0
#define LATEB 20
#define MEANF 0
#define PF 100
#define DRY 0
#define TPB 20
#define PRE 0
#define BNT 0
#define AH 1
#define DUM 1
#define TLEAD 14
#define GAP2 10
#define NG2 3
#define TECHO 600
#define TQ 300
#define TTAB 1050
#define TXW 200
#define FMASK ~0ull
#define FPOL 0ull
#define RING 12
#define SB() __builtin_amdgcn_sched_barrier(0)
#define PST(i) SB()
#undef PST
#define PST(i) do { SB(); if (DST == i + 1) d_x = clk() - base; SB(); } while (0)

typedef int v4i __attribute__((ext_vector_type(4)));
typedef unsigned v2u __attribute__((ext_vector_type(2)));
typedef float v2f __attribute__((ext_vector_type(2)));
typedef float v4f __attribute__((ext_vector_type(4)));
typedef short v4s __attribute__((ext_vector_type(4)));
typedef __bf16 v2bf __attribute__((ext_vector_type(2)));

constexpr int DH = 192;
constexpr int NH = 32;
constexpr int SKV = 32768;
constexpr int WPB = 8;
constexpr int NT = WPB * 64;
constexpr int NWG = 256;
constexpr int NSL = 8, SLC = 24;
constexpr int RS = NSL * 16;
static_assert(SKV == 2 * 64 * NWG, "two 64-token groups per workgroup and head");
constexpr int NX = 4;
constexpr int NRC = 17;
constexpr size_t WS_R = 262144;
constexpr size_t WS_X = WS_R + (size_t)NRC * NWG * RS;
constexpr size_t WS_T = WS_X + (size_t)NWG * 256;
constexpr unsigned BODY16 = (unsigned)(TGT * 100.0 / 64.0 * 65536.0);
constexpr int slot(int n) { return (int)((n * (BODY16 / 12)) >> 16); }

template <typename F, int... I>
__device__ __forceinline__ void sfor_impl(F&& f, std::integer_sequence<int, I...>) {
    (f(std::integral_constant<int, I>{}), ...);
}
template <int N, typename F>
__device__ __forceinline__ void sfor(F&& f) {
    sfor_impl(f, std::make_integer_sequence<int, N>{});
}

__device__ __forceinline__ float bf_lo(unsigned u) { return __uint_as_float(u << 16); }
__device__ __forceinline__ float bf_hi(unsigned u) { return __uint_as_float(u & 0xffff0000u); }
__device__ __forceinline__ float rb(float x) { return (float)(__bf16)x; }
__device__ __forceinline__ unsigned pack(float lo, float hi) {
    return __builtin_bit_cast(unsigned, __builtin_convertvector(v2f{lo, hi}, v2bf));
}
__device__ __forceinline__ float dot2(unsigned a, unsigned b, float c) {
    return __builtin_amdgcn_fdot2_f32_bf16(__builtin_bit_cast(v2bf, a), __builtin_bit_cast(v2bf, b), c, false);
}
__device__ __forceinline__ void mul4(float (&y)[4], unsigned a, unsigned b, unsigned s0, unsigned s1, unsigned s2, unsigned s3) {
    asm("v_dot2_f32_bf16 %0, %4, %6, 0\n\tv_dot2_f32_bf16 %1, %4, %7, 0\n\t"
        "v_dot2_f32_bf16 %2, %5, %8, 0\n\tv_dot2_f32_bf16 %3, %5, %9, 0\n\ts_nop 2"
        : "=&v"(y[0]), "=&v"(y[1]), "=&v"(y[2]), "=&v"(y[3])
        : "v"(a), "v"(b), "v"(s0), "v"(s1), "v"(s2), "v"(s3));
}
__device__ __forceinline__ unsigned clk() { return (unsigned)__builtin_amdgcn_s_memrealtime(); }
__device__ __forceinline__ unsigned inflight() {
    unsigned x;
    asm volatile("s_getreg_b32 %0, hwreg(HW_REG_IB_STS, 0, 4)" : "=s"(x));
    return x;
}
__device__ __forceinline__ unsigned negscale(unsigned u) { return pack(bf_lo(u) * -0x1p-22f, bf_hi(u) * -0x1p-22f); }

template <int CTRL>
__device__ __forceinline__ unsigned dppu(unsigned x) {
    return (unsigned)__builtin_amdgcn_mov_dpp((int)x, CTRL, 0xf, 0xf, true);
}
template <int CTRL>
__device__ __forceinline__ float dpp(float x) { return __uint_as_float(dppu<CTRL>(__float_as_uint(x))); }
__device__ __forceinline__ float rowsum16(float x) {
    x += dpp<0x128>(x);
    x += dpp<0x124>(x);
    x += dpp<0x122>(x);
    x += dpp<0x121>(x);
    return x;
}
__device__ __forceinline__ v4i ldstream(const void* p) {
    return __builtin_nontemporal_load(reinterpret_cast<const v4i*>(p));
}

__device__ __forceinline__ void st_rec(void* p, unsigned o, v4i a) {
    asm volatile("s_nop 4\n\tglobal_store_dwordx4 %0, %1, %2 sc1\n\ts_nop 1" :: "v"(o), "v"(a), "s"(p) : "memory");
}
__device__ __forceinline__ v4i ld_rec(const void* p, unsigned o) {
    v4i x;
    asm volatile("s_nop 4\n\tglobal_load_dwordx4 %0, %1, %2 sc1" : "=v"(x) : "v"(o), "s"(p) : "memory");
    return x;
}
typedef unsigned long long u64;
__device__ __forceinline__ u64 ld_dev(const u64* p) { return __hip_atomic_load(p, __ATOMIC_RELAXED, __HIP_MEMORY_SCOPE_AGENT); }
__device__ __forceinline__ void st_dev(u64* p, u64 v) { __hip_atomic_store(p, v, __ATOMIC_RELAXED, __HIP_MEMORY_SCOPE_AGENT); }
__device__ __forceinline__ unsigned ld_wg(const unsigned* p) { return __hip_atomic_load(p, __ATOMIC_RELAXED, __HIP_MEMORY_SCOPE_WORKGROUP); }
__device__ __forceinline__ void st_wg(unsigned* p, unsigned v) { __hip_atomic_store(p, v, __ATOMIC_RELAXED, __HIP_MEMORY_SCOPE_WORKGROUP); }
__device__ __forceinline__ float fold32(float a, float b) {
    auto s = __builtin_amdgcn_permlane32_swap(__float_as_uint(a), __float_as_uint(b), false, false);
    return __uint_as_float(s[0]) + __uint_as_float(s[1]);
}
__device__ __forceinline__ float fold16(float a, float b) {
    auto s = __builtin_amdgcn_permlane16_swap(__float_as_uint(a), __float_as_uint(b), false, false);
    return __uint_as_float(s[0]) + __uint_as_float(s[1]);
}
__device__ __forceinline__ float x4max(float x) {
    float m;
    asm("s_nop 1\n\tv_max_f32_dpp %1, %1, %1 row_ror:4 row_mask:0xf bank_mask:0xf\n\t"
        "s_nop 1\n\tv_max_f32_dpp %1, %1, %1 row_ror:8 row_mask:0xf bank_mask:0xf\n\t"
        "s_nop 1\n\tv_max_f32_dpp %1, %1, %1 row_bcast:15 row_mask:0xa bank_mask:0xf\n\t"
        "s_nop 1\n\tv_max_f32_dpp %1, %1, %1 row_bcast:31 row_mask:0xc bank_mask:0xf\n\t"
        "s_nop 0\n\tv_readlane_b32 %0, %1, 63\n\ts_nop 1" : "=s"(m), "+v"(x));
    return m;
}

struct __attribute__((aligned(16))) Smem {
    v4i ksz[(64 + NX) * 48];
    v4i qt[NH * 24];
    v4i vsz[2][(64 + NX) * 8];
    v4i rec[NRC - 1][RS / 4];
    float stage[2][WPB][DH];
    unsigned short p[NH][WPB][16];
    unsigned ml[NH];
    float fin[WPB + 1][32];
    u64 epoch;
    unsigned desc[2][2][2 * NH];
    unsigned ready;
    unsigned have;
    unsigned tref;
    unsigned slip;
    unsigned vote[2 * NH];
    unsigned late[WPB];
    unsigned req;
    unsigned t0;
    unsigned done;
    unsigned prog;
    unsigned fcnt;
    unsigned dsv;
    unsigned tick;
    unsigned quiet;
    unsigned cnt[NH + NH / 4];
};

__host__ __device__ constexpr int position(int b) { return (b & ~7) | ((b & 3) << 1) | ((b >> 2) & 1); }
constexpr int groupof(int p) {
    const int r0 = p < 85 ? 0 : p < 171 ? 85 : 171, na = p < 171 ? 43 : 42, t = p - r0;
    return r0 + (t < 64 ? ((t >> 4) & 1) * na + (t >> 5) * 16 + (t & 15) : t < na + 32 ? t - 32 : t);
}
struct Order { unsigned g[NWG / 2]; };

__global__ __launch_bounds__(NT + 128, 3) void kivi_stream_kernel(
    const __hip_bfloat16* __restrict__ q, const int* __restrict__ qk, const int* __restrict__ qv,
    const __hip_bfloat16* __restrict__ ks, const __hip_bfloat16* __restrict__ kz,
    const __hip_bfloat16* __restrict__ vs, const __hip_bfloat16* __restrict__ vz,
    float* __restrict__ ws, __hip_bfloat16* __restrict__ out, float scale,
    Order order) {
    __shared__ Smem sm;

    u64 ka0;
    unsigned ka1, ka2;
    asm volatile("s_load_dwordx2 %0, %3, 0x18\n\ts_load_dword %1, %3, %4 offset:0x4c\n\ts_load_dword %2, %3, 0x40"
                 : "=&s"(ka0), "=&s"(ka1), "=&s"(ka2) : "s"(__builtin_amdgcn_kernarg_segment_ptr()), "s"((blockIdx.x & 127) * 4));
    const u64 T0 = __builtin_amdgcn_s_memrealtime();
    const int tid = threadIdx.x;
    typedef const __attribute__((address_space(1))) int* gptr;
    u64 code;
    int codelen;
    asm volatile("s_getpc_b64 %0\nkivi_code_begin:\n\ts_mov_b32 %1, kivi_code_end-kivi_code_begin-4" : "=s"(code), "=s"(codelen));
    const int warm = *(gptr)(code + min((unsigned)tid * 128u, (unsigned)codelen));
    const int win = blockIdx.x >> 7;
    auto blockof = [&](int it) { return it >> 2 == 10 ? it : (it & ~3) + 2 * win + ((it >> 1) & 1); };
    auto halfof = [&](int it) { return (it >> 2 == 10 ? win : it) & 1; };
    auto group = [&](int it, int x) { return blockof(it) * NWG + ((x >> halfof(it) * 8) & 255); };
    unsigned pb = blockof(tid >> 3) * NWG, ph = halfof(tid >> 3) * 8;
    asm volatile("" : "+v"(pb), "+v"(ph));
    SB();
    asm volatile("s_waitcnt lgkmcnt(0)" : "+s"(ka0), "+s"(ka1), "+s"(ka2));
    const int gx = ka1 & 0xffff, px = ka1 >> 16;
    const unsigned pg = pb + ((ka1 >> ph) & 255), po = pg * (DH * 2) + (tid & 7) * 16;
    v4i pk[6];
    if (tid < NT) {
        typedef const __attribute__((address_space(1))) char* gchar;
        typedef const __attribute__((address_space(1))) v4i* gv4;
#pragma unroll
        for (int u = 0; u < 3; ++u) pk[u] = __builtin_nontemporal_load((gv4)((gchar)ka0 + po + u * 128));
    }
    SB();
    const u64 tb64 = __builtin_amdgcn_s_memrealtime() - LATEB;
    const unsigned tb = (unsigned)tb64;
    const int wave = __builtin_amdgcn_readfirstlane(tid >> 6);
    u64* tstart = reinterpret_cast<u64*>(ws + WS_T);
    if (tid == NT) st_dev(tstart + blockIdx.x, tb64);
    if (wave == WPB + 1) {
        const unsigned k = blockIdx.x >> 3, k4 = min(k, 7u) * 4;
        const u64 p = ((const __attribute__((address_space(4))) u64*)__builtin_amdgcn_kernarg_segment_ptr())[(0x13365444u >> k4) & 7u];
        unsigned x;
        asm volatile("s_load_dword %0, %1, 0x0\n\ts_waitcnt lgkmcnt(0)" : "=s"(x) : "s"(p + (((k - ((0x74443000u >> k4) & 7u)) << 21) + (blockIdx.x & 7) * 256)) : "memory");
    }
    SB();
    const int lane = tid & 63;
    const int r = lane & 15;
    const int g = lane >> 4;
    const int pos = position(blockIdx.x);
    auto tagof = [](u64 e, int j) {
        e *= 0x9e3779b97f4a7c15ull;
        return (unsigned)(j == 3 ? (e ^ (e >> 29)) >> 8 : e >> (j * 20)) << 8;
    };
    auto until = [&](unsigned t) { while ((int)(clk() - t) < 0) __builtin_amdgcn_s_sleep(1); };
    auto behind = [&]() {
        const unsigned tick = ld_wg(&sm.tick), t = clk();
        while (ld_wg(&sm.tick) == tick && clk() - t < 100u) __builtin_amdgcn_s_sleep(1);
    };
    const int quart = (pos >> 5) & 3, tau = (-((pos >> 1) + quart)) & 15;
    auto shared = [&](int b) { return SPL == 4 || ((b ^ quart) & 1) == 0; };
    auto job = [&](int t, int b, u64 pol, u64 ok, unsigned side) {
        const int bd = tau == 10 || (t == 0 && shared(b)) ? b : 2 * tau + (SPL == 4 ? 0 : quart & 1);
        const int reg = (shared(b) ? t : 1) * 32 + (group(2 * bd, pos & 1 ? px : gx) * 3 >> 13) * 4;
        return (((ok >> reg) & 1u && (t || b >= H0) ? side ^ (unsigned)(pol >> reg) : quart >> 1) ^ pos) & 1u;
    };
    auto describe = [&](int t, int it, int j, u64 pol, u64 ok, unsigned side) {
        const int b = it >> 1, n = it * 2 + j;
        const bool tr = it >> 2 == tau && tau > 0, sh = tr && shared(b);
        const unsigned jb = job(t, tr ? b : 2 * tau, pol, ok, side);
        const unsigned x = tr ? ((sh ? j : jb) ^ pos) & 1u : 0u;
        const unsigned gr = group(it, x ? px : gx), c = 6 * gr + 3;
        const unsigned flip = sh ? 0u : ((unsigned)(pol >> t * 32) >> (c >> 14) * 4) & 1u;
        const bool early = !t && b < H0;
        const unsigned okt = (unsigned)(ok >> t * 32);
        const bool known = (okt >> (it >> 2) * 3 / 8 * 4) & (okt >> ((it >> 2) * 3 + 2) / 8 * 4) & 1u;
        const unsigned ex = (early || b < 2 || !known ? (n & 3) == 0 ? EXS(0) : (n & 3) == 1 ? EXS(1) : (n & 3) == 2 ? EXS(2) : EXS(3) : 0) + (early || known ? 0 : n & 1)
                          + (n >= 84 && n < 87 ? G21 : 0) + (t ? n == 0 ? GAP1 : n == NG2 && ((pol ^ (pol >> 32)) & 0x111111u) ? GAP2 : 0 : n >= 3 && n < 3 + 4 * NSLOW ? EXK : 0);
        return gr * (64 * DH * 4) | ex << 4 | (early || known || sh ? 0u : 8u) | x << 2 | flip << 1
             | (((sh ? jb : j ^ win ^ flip) ^ (c >> 8) ^ (c >> 15)) & 1u);
    };

    const int mh = (blockIdx.x / 16 * 2 + (blockIdx.x & 7) / 4 + 20) % NH, msl = (blockIdx.x & 3) * 2 + ((blockIdx.x >> 3) & 1);
    const bool both = mh >> 1 == 10;
    v4i x[2] = {};
    unsigned fa[4], mls, one, oo;
    auto prepare = [&]() {
        auto lds = [](const void* p) { return (unsigned)(size_t)(const __attribute__((address_space(3))) char*)p; };
        fa[0] = lds(&sm.fin[wave][g == 3 ? 24 + (r & 3) * 2 : (r & 3) * 6 + g * 2]);
        fa[1] = lane == 0 ? lds(&sm.fcnt) : lds(&sm.vote[lane]);
        fa[2] = lds(&sm.fin[lane >> 3][(lane & 7) * 4]);
        fa[3] = lds(&sm.fin[lane >> 3][25]);
        mls = (lane & 3) * 8;
        one = tid < NT && lane == 0;
        oo = (mh * DH + msl * SLC + r * 4 + g) * 2;
        asm volatile("" : "+v"(fa[0]), "+v"(fa[1]), "+v"(fa[2]), "+v"(fa[3]), "+v"(mls), "+v"(one), "+v"(oo));
    };
    unsigned d_t1 = 0, d_o[5] = {};
    if (__builtin_expect(wave < WPB, 1)) {
    const int ww = (wave & 3) * 2 + (wave >> 2);
    const int ti = ww * 4 + g;
    const int tev = ti + (ti >= 11 ? 10 : 0) + (ti >= 22 ? 11 : 0), tod = ti + 11 + (ti >= 10 ? 11 : 0) + (ti >= 21 ? 10 : 0);
    const unsigned offe = tev * (DH * 4) + r * 16, offo = tod * (DH * 4) + r * 16;
    unsigned descv[2], dsc[4];
    auto lanes = [=](unsigned d) { return (d & 1u ? offo : offe) + (d & ~0x3fffu); };
    auto fetch = [&](int t) {
        while (__builtin_amdgcn_readfirstlane(ld_wg(&sm.ready)) == 0) __builtin_amdgcn_s_sleep(1);
        asm volatile("" ::: "memory");
#pragma unroll
        for (int j = 0; j < 2; ++j) {
            descv[j] = sm.desc[t][j][lane];
            if (ww >= WPB / 2 && (descv[j] & 8u)) descv[j] ^= 3u;
        }
    };
    const char* dummy = reinterpret_cast<const char*>(qv) + (size_t)(blockIdx.x * WPB + wave) * 4864;

    v4i buf[RING];
    auto burst = [&](const char* p, int i, unsigned o) {
#pragma unroll
        for (int e = 0; e < 3; ++e) { buf[i * 3 + e] = ldstream(p + o + e * 256); SB(); }
    };

    const unsigned phase = ((pos & (NWG / 2 - 1)) * (BODY16 / 512) + ww * WST * (BODY16 / (12 * 256))) >> 16;
    unsigned base = tb + phase + PSH;
    unsigned sched = 0, off = 0, offv = 0;
    int prev = 0, now = 0, hold = 0, big = 0, body = 0;
    unsigned hist = 0;
    unsigned vc = 0;
    int d_l = 0, d_n = 0, d_f = 0, d_s = 0, d_nb = 0;
    unsigned d_bar = 0, d_x = 0;
    auto pace = [&](int due) {
        int now;
        while ((now = (int)(clk() - base)) < due) __builtin_amdgcn_s_sleep(1);
        return now;
    };
    auto follow = [&](unsigned o2) {
        hold = o2 - off > SLIP / 2 ? HOLDB : o2 != off ? HOLD : max(hold - 1, 0);
        big = o2 - off > SLIP / 2 ? HOLDB : max(big - 1, 0);
        if ((int)(o2 - off) >= SLIP) ++d_nb;
        base += o2 - off;
        off = o2;
    };
    auto pacen = [&](auto N) {
        constexpr int n = decltype(N)::value;
        follow(__builtin_amdgcn_readfirstlane(offv));
        now = pace(slot(n));
        vc = inflight();
        hist <<= 1;
        if (__builtin_expect(vc > 3, 0)) {
            hist |= 1u;
            asm volatile("s_waitcnt vmcnt(3)");
            now = (int)(clk() - base);
            if (now > slot(n) + THR && prev < PRV && big == 0 && off + SLIP <= MAXOFF && body >= NOSLK
                && (int)(base + now - __builtin_amdgcn_readfirstlane(ld_wg(&sm.quiet))) >= 0) {
                if (lane == 0 && __hip_atomic_fetch_add(&sm.vote[body], 1u, __ATOMIC_RELAXED, __HIP_MEMORY_SCOPE_WORKGROUP) + 1 >= VOTES)
                    __hip_atomic_fetch_max(&sm.slip, off + SLIP, __ATOMIC_RELAXED, __HIP_MEMORY_SCOPE_WORKGROUP);
                unsigned o2;
                while ((o2 = __builtin_amdgcn_readfirstlane(ld_wg(&sm.slip))) == off && (int)(clk() - base) < now + VWAIT) {}
                if (o2 != off) { follow(o2); now = pace(slot(n)); }
            }
            if (hold == 0 && off + SS <= MAXOFF && body >= NOSM && body % NH != 21 && __builtin_popcount(hist & DBM) >= DBN && lane == 0) st_wg(&sm.req, off + SS);
        }
        if (d_n >= BA && d_n < BB) d_l = max(d_l, now - slot(n));
        if (now - slot(n) > 30 && d_f == 0) d_f = d_n + 1;
        if (off > 0 && d_s == 0) d_s = d_n + 1;
        ++d_n;
    };
    auto check = [&](auto N) {
        constexpr int n = decltype(N)::value;
        prev = now - slot(n);
        if (lane == 0) st_wg(&sm.late[wave], (unsigned)prev);
    };
    auto getslip = [&]() { offv = __hip_atomic_load(&sm.slip, __ATOMIC_RELAXED, __HIP_MEMORY_SCOPE_WORKGROUP); };
    auto arrive = [&](int i) {
        asm volatile("" ::: "memory");
        if (lane == 0) __hip_atomic_fetch_add(&sm.cnt[i], 1u, __ATOMIC_RELAXED, __HIP_MEMORY_SCOPE_WORKGROUP);
    };
    auto next_body = [&]() {
        const unsigned s2 = sched + BODY16;
        base += (s2 >> 16) - (sched >> 16);
        sched = s2;
        ++body;
        if (DIAG == 14 && body == BA) d_bar = base - (sched >> 16) - d_nb * SLIP;
    };

    {
        v4i pv[2], pq[2];
        pq[0] = reinterpret_cast<const v4i*>(q)[tid];
        pq[1] = reinterpret_cast<const v4i*>(q)[NT + (tid & 255)];
        const unsigned vo = pg * 128 + (tid & 7) * 16;
        PST(1); pace(slot(3) * PF / 100); SB();
        pk[3] = ldstream((const char*)kz + po);
        pk[4] = ldstream((const char*)kz + po + 128);
        pk[5] = ldstream((const char*)kz + po + 256);
        const int pl = (wave & 1) * 32 + (lane & 31);
        const bool isk = pl < 48, isz = isk ? pl >= 24 : pl >= 56;
        const char* par = (const char*)(isk ? isz ? kz : ks : isz ? vz : vs) + (isk ? pl % 24 : pl & 7) * 16
                        + group(tau * 4 + (wave >> 1), px) * (isk ? DH * 2 : 128);
        asm volatile("" : "+v"(par));
        PST(2); pace(slot(6) * PF / 100); SB();
        pv[0] = ldstream((const char*)vs + vo);
        pv[1] = ldstream((const char*)vz + vo);
        v4i pp = __builtin_nontemporal_load((const __attribute__((address_space(1))) v4i*)par);
        dsc[0] = describe(0, 0, 0, 0u, 0u, 0u);
        dsc[1] = describe(0, 0, 1, 0u, 0u, 0u);
        PST(3); pace(slot(8) * PF / 100); SB();
        burst((const char*)qk, 0, lanes(dsc[0]));
        PST(4);
        sm.qt[tid] = pq[0];
        if (tid < 256) sm.qt[NT + tid] = pq[1];
#pragma unroll
        for (int u = 0; u < 3; ++u) sm.ksz[(tid >> 3) * 48 + u * 8 + (tid & 7)] = pk[u];
#pragma unroll
        for (int u = 0; u < 3; ++u) {
            const v4i x = pk[3 + u];
            sm.ksz[(tid >> 3) * 48 + 24 + u * 8 + (tid & 7)] = v4i{(int)negscale(x.x), (int)negscale(x.y), (int)negscale(x.z), (int)negscale(x.w)};
        }
        PST(5); pace(slot(11) * PF / 100); SB();
        burst((const char*)qk, 1, lanes(dsc[1]));
        PST(6);
        descv[0] = describe(0, lane, 0, 0u, 0u, 0u);
        descv[1] = describe(0, lane, 1, 0u, 0u, 0u);
        SB();
        sm.vsz[0][tid] = pv[0];
        sm.vsz[1][tid] = v4i{(int)negscale(pv[1].x), (int)negscale(pv[1].y), (int)negscale(pv[1].z), (int)negscale(pv[1].w)};
        if (isz) pp = v4i{(int)negscale(pp.x), (int)negscale(pp.y), (int)negscale(pp.z), (int)negscale(pp.w)};
        *(isk ? &sm.ksz[(64 + (wave >> 1)) * 48 + pl] : &sm.vsz[isz][(64 + (wave >> 1)) * 8 + (pl & 7)]) = pp;
    }
    asm volatile("" :: "v"(warm));
    PST(7);
    __syncthreads();
    PST(8);
    if (DST == 20) d_x = sm.dsv;
    d_bar = clk();
    base = __builtin_amdgcn_readfirstlane(sm.t0) + OFF0 + phase;

    const v2f huge = {0x1p127f, 0x1p127f};

    for (int S = 0; S < NH; ++S) {
        v2f kz2[6];
        unsigned slo[6], shi[6];
        v2u qq[3];
        const char* nsup = (const char*)(S + 1 < NH ? qk : qv);
        const int head = S >> 1 == 10 ? S : (S & ~1) + win;
        float mylogit = 0.f;
        float dot = 0.f;
        sfor<12>([&](auto N) {
            constexpr int n = decltype(N)::value;
            constexpr int i = n / 3, j = n % 3;
            if (n == 6 && (S == H0 - 1 || S == NH - 1)) fetch(S == NH - 1);
            if (j == 0) {
                const unsigned d = dsc[i ^ 2] = __builtin_amdgcn_readlane(descv[i & 1], (2 * S + 1 + (i >> 1)) & 63);
                const unsigned o = lanes(d);
                base += (d >> 4) & 31u;
                pacen(N);
                SB();
                burst(i < 2 ? (const char*)qk : nsup, i ^ 2, o);
                SB();
                if (tid == NT - 1) st_wg(&sm.tick, S * 4 + i + 1);
                check(N);
            }
            if (j == 2) getslip();
            if (i % 2 == 0 || ((dsc[i] ^ dsc[i ^ 1]) & 4u)) {
                const int it = S * 2 + (i >> 1), sl = dsc[i] & 4u ? 64 + (it & 3) : it;
                const v2u* pk = reinterpret_cast<const v2u*>(&sm.ksz[sl * 48]);
                const v2u a = pk[j * 16 + r], b = pk[48 + j * 16 + r];
                slo[j * 2 + 0] = a.x & 0xffffu; shi[j * 2 + 0] = a.x & 0xffff0000u;
                slo[j * 2 + 1] = a.y & 0xffffu; shi[j * 2 + 1] = a.y & 0xffff0000u;
                kz2[j * 2 + 0] = v2f{bf_lo(b.x), bf_hi(b.x)};
                kz2[j * 2 + 1] = v2f{bf_lo(b.y), bf_hi(b.y)};
            }
            if (i == 0) qq[j] = reinterpret_cast<const v2u*>(&sm.qt[head * 24])[j * 16 + r];
            if (j == 0) dot = 0.f;
            {
                const v4i x = buf[n % RING];
                const v2f t0 = __builtin_elementwise_fma(v2f{__int_as_float(x.x), __int_as_float(x.y)}, huge, kz2[j * 2]);
                const v2f t1 = __builtin_elementwise_fma(v2f{__int_as_float(x.z), __int_as_float(x.w)}, huge, kz2[j * 2 + 1]);
                float y[4];
                mul4(y, pack(t0.x, t0.y), pack(t1.x, t1.y), slo[j * 2], shi[j * 2], slo[j * 2 + 1], shi[j * 2 + 1]);
                dot = dot2(pack(y[0], y[1]), qq[j].x, dot);
                dot = dot2(pack(y[2], y[3]), qq[j].y, dot);
            }
            if (j == 2) {
                const float d = rowsum16(dot);
                if ((r & 3) == (i ^ ((dsc[i] >> 1) & 1))) mylogit = d;
            }
            asm volatile("" : "+v"(dot), "+v"(mylogit));
            SB();
        });
        if (r < 4) sm.p[S][wave][g * 4 + r] = (unsigned short)pack(rb(mylogit * 0x1p22f) * scale, 0.f);
        if ((S & 3) == 3) arrive(NH + (S >> 2));
        if (S == NH - 3 && tid == 0) st_wg(&sm.done, 1u);
        next_body();
    }

    for (int S = 0; S < NH; ++S) {
        v2u P;
        unsigned vsl, vsh, vzf;
        v4f acc = {0.f, 0.f, 0.f, 0.f};
        float y0[12];
        unsigned b0[12];
        const bool more = S + 1 < NH;
        unsigned sl, sh;
        v2u A[12];
        v2f z2;
        sfor<12>([&](auto N) {
            constexpr int n = decltype(N)::value;
            constexpr int i = n / 3, j = n % 3;
            if (j == 0) {
                const unsigned d = dsc[i ^ 2] = __builtin_amdgcn_readlane(descv[i & 1], (2 * S + 1 + (i >> 1)) & 63);
                unsigned o = lanes(d);
                const char* src = (const char*)qv;
                if (i >= 2 && !more) {
                    o = DUM ? 0 : i * 768;
                    src = !DUM ? dummy : i == 3 && wave == 0 ? (const char*)out + min(mh, NH - 2) * (DH * 2) : (const char*)ws + (i & 1) * (15 << 18) + blockIdx.x * 768;
                }
                if (i < 2 || more) base += (d >> 4) & 31u;
                if (i < 2 || more) pacen(N);
                SB();
                burst(src, i ^ 2, o);
                SB();
                if (tid == NT - 1) st_wg(&sm.tick, 4 * NH + S * 4 + i + 1);
                check(N);
            }
            if (n == 0) {
                const unsigned need = S < 2 ? 1 : S;
                while (__builtin_amdgcn_readfirstlane(ld_wg(&sm.prog)) < need) {}
                asm volatile("" ::: "memory");
                P = *reinterpret_cast<const v2u*>(&sm.p[S][wave][g * 4]);
                P.x = __builtin_amdgcn_alignbit(P.x, P.x, (dsc[0] & 2u) << 3);
                P.y = __builtin_amdgcn_alignbit(P.y, P.y, (dsc[2] & 2u) << 3);
                const unsigned d3 = __builtin_amdgcn_readlane(descv[1], 2 * S + 1);
                const unsigned x = (((dsc[0] & 7u) | (dsc[1] & 7u) << 8) | ((dsc[2] & 7u) << 16 | (d3 & 7u) << 24)) >> (r & 3) * 8;
                const int it = S * 2 + ((r >> 1) & 1);
                const int ltok = (x & 4u ? 64 + (it & 3) : it) * 64 + (x & 1u ? tod : tev);
                vsl = reinterpret_cast<const unsigned short*>(&sm.vsz[0][0])[ltok];
                vsh = vsl << 16;
                vzf = (unsigned)reinterpret_cast<const unsigned short*>(&sm.vsz[1][0])[ltok] << 16;
            }
            if (j == 2) getslip();
            if (j == 0) {
                sl = dppu<0x55 * i>(vsl);
                sh = dppu<0x55 * i>(vsh);
                const float zb = __uint_as_float(dppu<0x55 * i>(vzf));
                z2 = v2f{zb, zb};
            }
            float y[4];
            {
                const v4i x = buf[n];
                const v2f t0 = __builtin_elementwise_fma(v2f{__int_as_float(x.x), __int_as_float(x.y)}, huge, z2);
                const v2f t1 = __builtin_elementwise_fma(v2f{__int_as_float(x.z), __int_as_float(x.w)}, huge, z2);
                mul4(y, pack(t0.x, t0.y), pack(t1.x, t1.y), sl, sh, sl, sh);
            }
#pragma unroll
            for (int e = 0; e < 4; ++e) {
                const int idx = j * 4 + e;
                if (i % 2 == 0) {
                    y0[idx] = y[e];
                    asm volatile("" : "+v"(y0[idx]));
                    if (i == 2) {
                        A[idx] = v2u{r == idx ? P.x : 0u, r == idx ? P.y : 0u};
                        if (AH) asm volatile("" : "+v"(A[idx]));
                    }
                } else if (i == 1) {
                    b0[idx] = pack(y0[idx], y[e]);
                    asm volatile("" : "+v"(b0[idx]));
                } else {
                    const v2u B = {b0[idx], pack(y0[idx], y[e])};
                    acc = __builtin_amdgcn_mfma_f32_16x16x16bf16_1k(__builtin_bit_cast(v4s, A[idx]), __builtin_bit_cast(v4s, B), acc, 0, 0, 0);
                }
            }
            if (n == 11) {
                if (S == NH - 2 && tid == 0) sm.tref = base + slot(12) + slot(3);
                if (S == NH - 3 && tid == 0) st_wg(&sm.done, 2u);
                if (lane < 48) *reinterpret_cast<v4f*>(&sm.stage[S & 1][wave][lane * 4]) = acc;
                arrive(S);
            }
            SB();
        });
        next_body();
    }
    if (DST >= 21) d_x = sm.dsv;
    d_t1 = clk();
    d_o[0] = off; d_o[1] = DST ? d_x : d_l; d_o[2] = d_f; d_o[3] = d_bar - phase; d_o[4] = DIAG >= 12 ? d_nb : d_s;
    {
        const unsigned tag = tagof(sm.epoch, lane & 3);
        const float* src = ws + WS_R + ((size_t)((both ? 0 : mh & 1) * (NWG / 2) + wave * 16) * NRC + (mh >> 1) + (mh > 20)) * RS + msl * 16;
        const unsigned so = (lane >> 2) * (NRC * RS * 4) + (lane & 3) * 16;
        prepare();
        until(base - slot(12) + slot(3) - phase + (wave - (WPB - 1)) * STG + (mh >= NH - 2 ? TPL : both ? TPC - TPB : TPC));
        for (;;) {
            x[0] = ld_rec(src, so);
            x[1] = v4i{0, 0, 0, (int)tag};
            if (both) x[1] = ld_rec(src + NWG / 2 * NRC * RS, so);
            asm volatile("s_waitcnt vmcnt(0)" : "+v"(x[0]), "+v"(x[1]));
            if (__all((((unsigned)x[0].w ^ tag) | ((unsigned)x[1].w ^ tag)) < 256u) || clk() - tb >= TMO) break;
            __builtin_amdgcn_s_sleep(PSL);
        }
#pragma unroll
        for (int d = 5; d >= 0; --d) asm volatile("" :: "v"(buf[d]));
        x[0].w ^= tag;
        x[1].w ^= tag;
    }
    } else
    if (wave == WPB) {
        const int l = lane;
        u64* echo = tstart + NWG;
        u64* gofs = tstart + 3 * NWG;
        if (l == 0) {
            if (blockIdx.x < NR) st_dev(gofs + blockIdx.x * 32, tb64 - 100000ull);
            if (blockIdx.x == NR) st_dev(gofs + 16, tb64 - 100000ull);
            sm.slip = 0;
            sm.req = 0;
            sm.done = 0;
            sm.prog = 0;
            sm.ready = 0;
            sm.tick = 0;
            sm.quiet = tb;
            sm.fcnt = 0;
            sm.epoch = tb64;
            sm.have = blockIdx.x == 0;
        }
        if (l < NH + NH / 4) sm.cnt[l] = 0;
        sm.vote[l] = 0;
        until(tb + TGATH);
        unsigned ld;
        for (;;) {
            const u64 dt = ld_dev(tstart + NWG - 8 + (l & 7)) - tb64 + 1000ull;
            ld = dt < 2000ull ? (unsigned)dt : 0u;
            if (__all(dt < 2000ull) || clk() - tb > (unsigned)TGMAX) break;
        }
        unsigned sum = ld + dppu<0x124>(ld);
        sum += dppu<0x122>(sum);
        sum += dppu<0x121>(sum);
        const bool allv = __all(ld != 0);
        ld = max(ld, dppu<0x124>(ld));
        ld = max(ld, dppu<0x122>(ld));
        ld = max(ld, dppu<0x121>(ld));
        if (MEANF && allv) ld -= (ld - sum / 8) * MEANF / 4;
        ld = __builtin_amdgcn_readfirstlane(ld);
        const u64 t64 = ld ? tb64 + ld - 1000ull : tb64;
        if (l == 0) sm.t0 = (unsigned)t64;
        if (l == 0) sm.dsv = clk() - tb;
        u64 to[NWG / 64];
        bool have = blockIdx.x == 0;
        auto chores = [&](bool all) {
            if (blockIdx.x == 0) {
                u64 x[NWG / 64];
#pragma unroll
                for (int u = 0; u < NWG / 64; ++u) x[u] = ld_dev(tstart + u * 64 + l);
#pragma unroll
                for (int u = 0; u < NWG / 64; ++u) {
                    if (all || __any(x[u] != to[u])) st_rec(echo + u * 128, l * 16, v4i{(int)(unsigned)x[u], (int)(unsigned)(x[u] >> 32), (int)tb, (int)(unsigned)(tb64 >> 32)});
                    to[u] = x[u];
                }
            } else if (!have && !all) {
                v4i x = ld_rec(echo + blockIdx.x * 2, 0);
                asm volatile("s_waitcnt vmcnt(0)" : "+v"(x));
                if ((unsigned)x.x == tb && (unsigned)x.y == (unsigned)(tb64 >> 32)) {
                    have = true;
                    if (l == 0) sm.epoch = (u64)(unsigned)x.w << 32 | (unsigned)x.z;
                    asm volatile("s_waitcnt lgkmcnt(0)" ::: "memory");
                    if (l == 0) st_wg(&sm.have, 1u);
                }
            }
        };
        if (EARLY) chores(true);
        asm volatile("s_waitcnt lgkmcnt(0)\n\ts_barrier" ::: "memory");
        asm volatile("" :: "v"(warm));
        if (!EARLY) chores(true);
        until(tb + TECHO);
        behind();
        chores(false);
        const u64* mine = NR > 1 ? gofs + ((blockIdx.x >> 3) % NR) * 32 : gofs + 16;
        unsigned sent = 0, stage = 0, tsh = clk();
        int nt = blockIdx.x < 200, spin = 0;
        while (stage < 2) {
            if (PB > 1 && have) __builtin_amdgcn_s_sleep(PSLEEP);
            behind();
            unsigned tch = 0;
            if (nt < 25) {
                const unsigned pg = (blockIdx.x >> 3) + 32 * (nt % 12);
                const char* tp = (nt < 12 ? (const char*)qk : nt < 24 ? (const char*)qv : (const char*)ws) + ((size_t)pg << 21);
                const bool first = PTL && (pg == 0 || (((size_t)tp >> 21) & (PTL - 1)) == 0);
                tp += (((first ? blockIdx.x >> 3 : blockIdx.x) * 29 + nt) & 255) * 256;
                if (nt == 24 && blockIdx.x >= 16) tp = (const char*)out + min((pg - 2) * 4096, NH * DH * 2 - 4u);
                if (ld_wg(&sm.tick) + (first ? TLEAD + 4 : TLEAD) >= (unsigned)(nt / 12 * 128 + nt % 12 * 4 / 3 * 8 - nt / 24 * 32)) {
                    unsigned x;
                    if (nt < 24 && !first) asm volatile("s_nop 4\n\tglobal_load_dword %0, %1, %2 nt" : "+v"(tch) : "v"(0), "s"(tp) : "memory");
                    else if (nt < 24 || blockIdx.x < 48) asm volatile("s_load_dword %0, %1, 0x0\n\ts_waitcnt lgkmcnt(0)" : "=s"(x) : "s"(tp) : "memory");
                    ++nt;
                }
            }
            const u64 gd = ld_dev(mine) - t64;
            asm volatile("s_waitcnt vmcnt(0)" : "+v"(tch));
            const unsigned go = gd <= (u64)MAXOFF ? (unsigned)gd : 0u;
            const unsigned own = ld_wg(&sm.slip);
            if (go > own && l == 0) __hip_atomic_fetch_max(&sm.slip, go, __ATOMIC_RELAXED, __HIP_MEMORY_SCOPE_WORKGROUP);
            if (own <= go) tsh = clk();
            else if (clk() - tsh > (unsigned)REVT && blockIdx.x >= NSENT) { if (l == 0) st_wg(&sm.slip, go); }
            unsigned req = ld_wg(&sm.req);
            if ((int)(clk() - ld_wg(&sm.quiet)) < 0) { req = 0; if (l == 0) st_wg(&sm.req, 0u); }
            unsigned want = blockIdx.x < NSENT ? own : 0u;
            if (req == go + SS && own == go) {
                unsigned x = ld_wg(&sm.late[l & 7]);
                x += dppu<0x121>(x);
                x += dppu<0x122>(x);
                x += dppu<0x124>(x);
                want = min(req + (__builtin_amdgcn_readfirstlane(x) * LFAC / 64 & ~15u), (unsigned)MAXOFF);
            }
            if (want > go && want > sent) {
                u64 old = 0;
                behind();
                if (l == 0) st_wg(&sm.quiet, clk() + TQA);
                if (l == 0) old = __hip_atomic_fetch_max(gofs + 16, t64 + want, __ATOMIC_RELAXED, __HIP_MEMORY_SCOPE_AGENT);
                if (NR > 1 && __builtin_amdgcn_readfirstlane((unsigned)(old < t64 + want)) && l < NR)
                    __hip_atomic_fetch_max(gofs + l * 32, t64 + want, __ATOMIC_RELAXED, __HIP_MEMORY_SCOPE_AGENT);
                sent = want;
            }
            if (!have || (blockIdx.x == 0 && ++spin % 8 == 0)) { behind(); chores(false); }
            if (ld_wg(&sm.done) > stage) {
                ++stage;
                behind();
                asm volatile("" : "+s"(code));
                int w = 0;
#pragma unroll
                for (int u = 0; u < 4; ++u) {
                    const int a = (u * 64 + l) * 128;
                    w ^= *(gptr)(code + (a < codelen ? a : 0));
                }
                asm volatile("" :: "v"(w));
            }
        }
        while (!have && clk() - tb < TMO) chores(false);
        if (l == 0) st_wg(&sm.have, 1u);
        if (blockIdx.x == 0) {
            for (spin = 1; ld_wg(&sm.fcnt) < WPB; ++spin) {
                __builtin_amdgcn_s_sleep(2);
                if (spin % 1024 == 0) chores(false);
            }
            return;
        }
        if (!DRY) return;
        prepare();
    } else {
        u64* table = tstart + 3 * NWG + NR * 32;
        const unsigned ent = blockIdx.x - CAL0;
        int lead = 0, lat = 0;
        if (PRE) {
            v4i y[PRE ? PRE : 1];
#pragma unroll
            for (int u = 0; u < PRE; ++u) y[u] = *(const v4i*)((const char*)(ws + WS_R) + (pos * NRC + NRC - 1 - (PRE > 1 ? u * 2 + (lane >> 5) : 0)) * (RS * 4) + (lane & 31) * 16);
#pragma unroll
            for (int u = 0; u < PRE; ++u) asm volatile("" :: "v"(y[u]));
        }
        __syncthreads();
        if (ent < 64 && (ent & 31) < 24 && (ent & 3) < NMEM) {
            const unsigned t0 = __builtin_amdgcn_readfirstlane(sm.t0);
            unsigned page, x[4];
            auto pair = [&](unsigned k) {
                const unsigned h = (t0 + k + (ent & ~3u) * 2) * 0x9e3779b1u;
                page = (ent & 28) * 16 + (k & 1) * 32 + 24 + ((t0 + (k & 1)) * 0x85ebca6bu >> 29);
                return (const char*)(ent < 32 ? +qk : +qv) + ((size_t)page << 21) + (h >> 25) * 16384 + ((h >> 8) & 127) * 64;
            };
            asm volatile("s_load_dword %0, %4, 0x0\n\ts_load_dword %1, %4, 0x2000\n\ts_load_dword %2, %5, 0x0\n\t"
                         "s_load_dword %3, %5, 0x2000\n\ts_waitcnt lgkmcnt(0)"
                         : "=&s"(x[0]), "=&s"(x[1]), "=&s"(x[2]), "=&s"(x[3]) : "s"(pair(ent % 4 * 2)), "s"(pair(ent % 4 * 2 + 1)) : "memory");
            until(t0 + TPRB);
#pragma nounroll
            for (unsigned k = 2; k < 2 * NMEM; ++k) {
                u64 ta, tc, td;
                asm volatile("s_memtime %0\n\ts_waitcnt lgkmcnt(0)\n\ts_load_dword %3, %4, 0x0\n\ts_waitcnt lgkmcnt(0)\n\t"
                             "s_memtime %1\n\ts_waitcnt lgkmcnt(0)\n\ts_load_dword %3, %4, 0x2000\n\ts_waitcnt lgkmcnt(0)\n\t"
                             "s_memtime %2\n\ts_waitcnt lgkmcnt(0)"
                             : "=&s"(ta), "=&s"(tc), "=&s"(td), "=&s"(x[0]) : "s"(pair((ent % 4 * 2 + k) % (2 * NMEM))) : "memory");
                const int d0 = (int)(tc - ta), d1 = (int)(td - tc), d = max(-PCL, min(PCL, d1 - d0));
                if (min(d0, d1) > 350 && d0 + d1 < 1700) lead += (page ^ (page >> 7)) & 1 ? -d : d;
                lat += d0 + d1;
            }
        }
        asm volatile("" :: "v"(warm));
        while (ld_wg(&sm.have) == 0 && clk() - tb < TMO) __builtin_amdgcn_s_sleep(4);
        asm volatile("" ::: "memory");
        const u64 E = sm.epoch;
        if (ent < 64 && (ent & 31) < 24 && (ent & 3) < NMEM) {
            unsigned xcc;
            asm volatile("s_getreg_b32 %0, hwreg(HW_REG_XCC_ID, 0, 3)" : "=s"(xcc));
            if (xcc & 4) lead = -lead;
            behind();
            if (lane == 0) st_wg(&sm.quiet, clk() + TQ);
            if (lane < 8) st_rec(table + ent * 32, lane * 16, v4i{lead, (int)(xcc - blockIdx.x), (int)(unsigned)E, (int)(unsigned)(E >> 32)});
        }
        unsigned jobs = 0;
        v4i cal;
        until(tb + TTAB);
        for (;;) {
            behind();
            cal = ld_rec(table, ((lane & 31) < 24 && (lane & 3) < NMEM ? lane : 0) * 256);
            asm volatile("s_waitcnt vmcnt(0)" : "+v"(cal));
            if (__all((unsigned)cal.z == (unsigned)E && (unsigned)cal.w == (unsigned)(E >> 32)) || clk() - tb >= TMO) break;
        }
        {
            int sum = (lane & 3) < NMEM ? cal.x : 0;
            sum += (int)dppu<0xB1>(sum);
            sum += (int)dppu<0x4E>(sum);
            const u64 pol = __ballot(sum < 0) ^ FPOL, ok = __ballot(abs(sum) >= PTH) & FMASK;
            const unsigned side = ((unsigned)__builtin_amdgcn_readfirstlane(cal.y) + (blockIdx.x & ~4u)) >> 2;
#pragma unroll
            for (int u = 0; u < 4; ++u) {
                sm.desc[u >> 1][u & 1][lane] = describe(u >> 1, lane, u & 1, pol, ok, side);
                jobs |= (tau > 0 ? job(u >> 1, 2 * tau + (u & 1), pol, ok, side) : pos & 1u) << u;
            }
            asm volatile("" ::: "memory");
            if (lane == 0 && DST == 21) sm.dsv = clk() - tb;
            if (lane == 0 && DST == 22) sm.dsv = (12 - __popcll(ok & 0x11111100111111ull)) * 10;
            if (lane == 0) st_wg(&sm.ready, 1u);
        }
#pragma nounroll
        for (int cc = 0; cc <= NH / 4; ++cc) {
            const int c = cc < NH / 4 ? cc : tau >> 1;
            const bool act = (tau > 0 && c * 2 + (g >> 1) == tau) == (cc == NH / 4);
            while (ld_wg(&sm.cnt[NH + c]) < WPB) __builtin_amdgcn_s_sleep(2);
            asm volatile("" ::: "memory");
            if (cc >= NH / 4 - 1) {
                const bool sw = ((jobs ^ (jobs >> 2)) >> (lane >> 5)) & 1u;
                v2u* mine = reinterpret_cast<v2u*>(&sm.p[2 * tau][0][0]) + lane;
                if (cc < NH / 4) {
                    if (lane == 0) st_wg(&sm.quiet, clk() + TXW + TQ);
                    if (sw) st_rec(ws + WS_X + pos * 256, lane * 16, v4i{(int)mine->x, (int)mine->y, (int)(unsigned)E, (int)(unsigned)(E >> 32)});
                } else if (__any(sw)) {
                    until(clk() + TXW);
                }
                while (cc == NH / 4 && __any(sw)) {
                    v4i x = {};
                    if (sw) asm volatile("s_nop 4\n\tglobal_load_dwordx4 %0, %1, %2 sc1 nt" : "+v"(x) : "v"(lane * 16), "s"(ws + WS_X + (pos ^ 1) * 256) : "memory");
                    asm volatile("s_waitcnt vmcnt(0)" : "+v"(x));
                    if (__all(!sw || ((unsigned)x.z == (unsigned)E && (unsigned)x.w == (unsigned)(E >> 32))) || clk() - tb >= TMO) {
                        if (sw) *mine = v2u{(unsigned)x.x, (unsigned)x.y};
                        break;
                    }
                    __builtin_amdgcn_s_sleep(8);
                }
                asm volatile("" ::: "memory");
            }
            const bool two = c * 2 + (g >> 1) != 10;
            v4i* pp = reinterpret_cast<v4i*>(&sm.p[c * 4][0][0]) + lane;
            const v4i u = *pp;
            float x[8];
#pragma unroll
            for (int e = 0; e < 4; ++e) { x[2 * e] = bf_lo(u[e]); x[2 * e + 1] = bf_hi(u[e]); }
            float M = fmaxf(fmaxf(fmaxf(x[0], x[1]), fmaxf(x[2], x[3])), fmaxf(fmaxf(x[4], x[5]), fmaxf(x[6], x[7])));
            M = fmaxf(M, dpp<0x128>(M));
            M = fmaxf(M, dpp<0x124>(M));
            M = fmaxf(M, dpp<0x122>(M));
            M = fmaxf(M, dpp<0x121>(M));
            {
                auto y = __builtin_amdgcn_permlane16_swap(__float_as_uint(M), __float_as_uint(M), false, false);
                if (two) M = fmaxf(__uint_as_float(y[0]), __uint_as_float(y[1]));
            }
            v4i o;
            float L = 0.f;
#pragma unroll
            for (int e = 0; e < 4; ++e) {
                const unsigned pk = pack(__expf(x[2 * e] - M), __expf(x[2 * e + 1] - M));
                o[e] = (int)pk;
                L += bf_lo(pk) + bf_hi(pk);
            }
            if (act) *pp = o;
            L = rowsum16(L);
            const float L2 = fold16(L, L);
            if (two) L = L2;
            if (r == 0 && act) sm.ml[c * 4 + g] = __float_as_uint(M) | __builtin_bit_cast(unsigned short, (_Float16)L);
            asm volatile("" ::: "memory");
            if (lane == 0) st_wg(&sm.prog, 1u);
        }
        const unsigned tag = tagof(E, lane & 3);
        auto place = [&](int i) {
            const int i0 = tau + (tau > 10);
            const unsigned jb = i == i0 ? jobs >> 2 : i == 11 && tau == 10 ? jobs >> 3 : pos;
            return (((pos & ~1) | (jb & 1)) * NRC + i) * (RS * 4) + (lane & 31) * 16;
        };
        const unsigned last = place(NRC - 1);
        v2f keep[3] = {};
#pragma nounroll
        for (int h = 0; h < NH; ++h) {
            const unsigned mlb = (sm.ml[h] >> (lane & 3) * 8) & 0xffu;
            if (h < NH - 1) { while (ld_wg(&sm.cnt[h]) < WPB) __builtin_amdgcn_s_sleep(4); }
            else { while (ld_wg(&sm.cnt[h]) < WPB) {} }
            asm volatile("" ::: "memory");
            v2f c[4][3];
#pragma unroll
            for (int e = 0; e < 4; ++e)
#pragma unroll
                for (int k = 0; k < 3; ++k) c[e][k] = *reinterpret_cast<const v2f*>(&sm.stage[h & 1][(lane >> 5) * 4 + e][(lane & 31) * 6 + k * 2]);
            v4i x;
            const bool first = (h & 1) == 0 && h != 20;
#pragma unroll
            for (int k = 0; k < 3; ++k) {
                const v2f a = (keep[k] + (c[0][k] + c[1][k])) + (c[2][k] + c[3][k]), b = a * 0x1p22f;
                x[k] = (int)pack(fold32(b.x, b.x), fold32(b.y, b.y));
                keep[k] = first ? a : v2f{0.f, 0.f};
            }
            x[3] = (int)(tag | mlb);
            if (h == NH - 1) {
                if (lane < 32) {
                    if (BNT & 2) asm volatile("s_nop 4\n\tglobal_store_dwordx4 %0, %1, %2 sc1 nt\n\ts_nop 1" :: "v"(last), "v"(x), "s"(ws + WS_R) : "memory");
                    else st_rec(ws + WS_R, last, x);
                }
                break;
            }
            if (!first) sm.rec[(h >> 1) + (h > 20)][lane & 31] = x;
            asm volatile("" ::: "memory");
            if (lane == 0) st_wg(&sm.prog, h + 2);
            if (h == NH - 2) {
                v4i y[NRC / 2];
                unsigned o[NRC / 2];
#pragma unroll
                for (int u = 0; u < NRC / 2; ++u) { y[u] = (&sm.rec[0][0])[u * 64 + lane]; o[u] = place(u * 2 + (lane >> 5)); }
                until(ld_wg(&sm.tref) + TREL);
#pragma unroll
                for (int u = 0; u < NRC / 2; ++u) {
                    if (BNT & 1) asm volatile("s_nop 4\n\tglobal_store_dwordx4 %0, %1, %2 sc1 nt\n\ts_nop 1" :: "v"(o[u]), "v"(y[u]), "s"(ws + WS_R) : "memory");
                    else st_rec(ws + WS_R, o[u], y[u]);
                }
            }
        }
        return;
    }

    {
        asm volatile("" : "+v"(x[0]), "+v"(x[1]));
        auto gather = [&](unsigned w) {
            w <<= mls;
            w |= dppu<0xB1>(w);
            return w | dppu<0x4E>(w);
        };
        auto half = [](unsigned u) { return (float)__builtin_bit_cast(_Float16, (unsigned short)u); };
        const unsigned ml = gather(x[0].w);
        float m = bf_hi(ml), l = half(ml), a[6];
#pragma unroll
        for (int e = 0; e < 3; ++e) { a[2 * e] = bf_lo(x[0][e]); a[2 * e + 1] = bf_hi(x[0][e]); }
        if (both) {
            const unsigned m2 = gather(x[1].w);
            const float mx = fmaxf(m, bf_hi(m2)), w0 = __expf(m - mx), w1 = __expf(bf_hi(m2) - mx);
#pragma unroll
            for (int e = 0; e < 3; ++e) {
                a[2 * e] = a[2 * e] * w0 + bf_lo(x[1][e]) * w1;
                a[2 * e + 1] = a[2 * e + 1] * w0 + bf_hi(x[1][e]) * w1;
            }
            l = l * w0 + half(m2) * w1;
            m = mx;
        }
        const float M = x4max(m), w0 = __expf(m - M);
        l *= w0;
        float c0 = fold16(fold32(a[0] * w0, a[4] * w0), fold32(a[2] * w0, l));
        float c1 = fold16(fold32(a[1] * w0, a[5] * w0), fold32(a[3] * w0, l));
        c0 += dpp<0x128>(c0); c1 += dpp<0x128>(c1);
        c0 += dpp<0x124>(c0); c1 += dpp<0x124>(c1);
        if (g == 3) c1 = M;
        unsigned arr;
        v4f y;
        float mw;
        asm volatile("ds_write_b64 %3, %4\n\tds_add_rtn_u32 %0, %5, %6\n\tds_read_b32 %2, %8\n\tds_read_b128 %1, %7\n\ts_waitcnt lgkmcnt(0)"
                     : "=&v"(arr), "=&v"(y), "=&v"(mw) : "v"(fa[0]), "v"(v2f{c0, c1}), "v"(fa[1]), "v"(one), "v"(fa[2]), "v"(fa[3]) : "memory");
        const float w = __expf(mw - x4max(mw));
        float d = fold16(fold32(y.x * w, y.z * w), fold32(y.y * w, y.w * w));
        d += dpp<0x128>(d);
        const float L = __int_as_float(__builtin_amdgcn_readlane(__float_as_int(d), 6));
        const unsigned o = pack(d * __builtin_amdgcn_rcpf(L), 0.f);
        if (__builtin_amdgcn_readfirstlane(arr) == WPB - 1 && wave < WPB) {
            if (r < 6) *(unsigned short*)((char*)out + oo) = (unsigned short)o;
        }
    }
    if (wave < WPB) {
        unsigned* dg = reinterpret_cast<unsigned*>(ws + WS_T + 655360) + (blockIdx.x * WPB + wave) * 8;
        if (lane == 0) { dg[0] = (unsigned)T0; dg[1] = d_t1; dg[2] = clk(); for (int i = 0; i < 5; ++i) dg[3 + i] = d_o[i]; }
    }
}
__global__ void kivi_end_marker() { asm volatile("kivi_code_end:"); }

__global__ void diag_kernel(const float* ws) {
    const unsigned* dg = reinterpret_cast<const unsigned*>(ws + WS_T + 655360);
    const unsigned ref = dg[0];
    int mn = 1 << 30, x = -(1 << 30), cnt = 0;
    for (int b = 0; b < 8; ++b) {
        const unsigned* e = dg + (b * 256 + threadIdx.x) * 8;
        mn = min(mn, (int)(e[0] - ref));
        const int v = DIAG == 2 ? (int)(e[0] - ref) : DIAG == 3 ? (int)e[3] : DIAG == 4 ? (int)(e[1] - ref - e[3]) : DIAG == 5 ? (int)(e[2] - ref)
                    : DIAG == 12 ? (int)(e[2] - ref - e[7] * SLIP) : DIAG == 13 ? (int)(e[3] - e[7] * SLIP) : DIAG == 6 ? (int)e[4] : DIAG == 7 || DIAG == 14 ? (int)(e[6] - ref) : DIAG == 8 ? (e[5] ? 1000 - (int)e[5] : 0) : DIAG == 9 ? (e[7] ? 1000 - (int)e[7] : 0) : 0;
        x = max(x, v);
        cnt += DIAG == 11 ? (int)e[4] : (int)e[4] > 30;
    }
    __shared__ int smn, smx, sc;
    if (threadIdx.x == 0) { smn = 1 << 30; smx = -(1 << 30); sc = 0; }
    __syncthreads();
    atomicMin(&smn, mn); atomicMax(&smx, x); atomicAdd(&sc, cnt);
    __syncthreads();
    mn = smn; x = smx;
    int X = 0;
    if (DIAG == 2 || DIAG == 7 || DIAG == 14) X = x - mn;
    if (DIAG == 3 || DIAG == 6 || DIAG == 8 || DIAG == 9) X = x;
    if (DIAG == 4 || DIAG == 5 || DIAG == 12) X = x - mn - 20000;
    if (DIAG == 13) X = x;
    if (DIAG == 10) X = sc;
    if (DIAG == 11) X = sc / 2048;
    X = max(0, min(X, 3000));
    while ((int)(clk() - ref) < mn + 25000 + X * 10) __builtin_amdgcn_s_sleep(1);
}
extern "C" void kernel_launch(void* const* d_in, const int* in_sizes, int n_in,
                              void* d_out, int out_size, void* d_ws, size_t ws_size,
                              hipStream_t stream) {
    const __hip_bfloat16* q = (const __hip_bfloat16*)d_in[0];
    const int* qk = (const int*)d_in[1];
    const int* qv = (const int*)d_in[2];
    const __hip_bfloat16* ks = (const __hip_bfloat16*)d_in[3];
    const __hip_bfloat16* kz = (const __hip_bfloat16*)d_in[4];
    const __hip_bfloat16* vs = (const __hip_bfloat16*)d_in[5];
    const __hip_bfloat16* vz = (const __hip_bfloat16*)d_in[6];
    const float scale = (float)(1.0 / sqrt((double)DH));
    Order order;
    for (int b = 0; b < NWG / 2; ++b) {
        const int p = position(b);
        order.g[b] = groupof(p) | groupof(p + 128) << 8 | groupof(p ^ 1) << 16 | groupof((p ^ 1) + 128) << 24;
    }
    kivi_stream_kernel<<<NWG, NT + 128, 0, stream>>>(q, qk, qv, ks, kz, vs, vz, (float*)d_ws, (__hip_bfloat16*)d_out, scale, order);
    diag_kernel<<<1, 256, 0, stream>>>((const float*)d_ws);
}
```

```cpp
#define DIAG 14
#include <hip/hip_runtime.h>
#include <hip/hip_bf16.h>
#include <stdint.h>
#include <utility>

#define TGT 199.25
#define WST 16
#define THR 80
#define PRV 16
#define SLIP 250
#define MAXOFF 3000
#define NSENT 8
#define NR 32
#define TPL 265
#define STG 4
#define PSLEEP 16
#define OFF0 380
#define TGATH 150
#define GAP1 10
#define TREL 110
#define NSLOW 1
#define EXK 7
#define NOSLK 2
#define TMO 100000000u
#define TPC 265
#define PSL 2
#define DST 0
#define BA 8
#define BB 16
#define TGMAX 300
#define PSH 0
#define SS 20
#define HOLD 4
#define VOTES 4
#define HOLDB 8
#define REVT 600
#define VWAIT 30
#define LFAC 4
#define NOSM 2
#define H0 5
#define TGS 203.0
#define SPL 2
#define G21 5
#define EXS(i) ((int)(((i) + 1) * (TGS - TGT) * 100.0 / 256.0 + 0.5) - (int)((i) * (TGS - TGT) * 100.0 / 256.0 + 0.5))
#define CAL0 128
#define PTH (NMEM * (NMEM - 1) * 150)
#define NMEM 2
#define PCL 500
#define TPRB 580
#define DBM 7
#define DBN 2
#define TQA 500
#define PB 1
#define PTL 8
#define EARLY 0
#define LATEB 20
#define MEANF 0
#define PF 100
#define DRY 0
#define TPB 20
#define PRE 0
#define BNT 0
#define PVAR 0
#define PFP 50
#define KD 0
#define ETCH 1
#define TF0 2
#define AH 1
#define DUM 1
#define TLEAD 14
#define GAP2 10
#define NG2 3
#define TECHO 600
#define TQ 300
#define TTAB 1050
#define TXW 200
#define FMASK ~0ull
#define FPOL 0ull
#define RING 12
#define SB() __builtin_amdgcn_sched_barrier(0)
#define PST(i) SB()
#undef PST
#define PST(i) do { SB(); if (DST == i + 1) d_x = clk() - base; SB(); } while (0)

typedef int v4i __attribute__((ext_vector_type(4)));
typedef unsigned v2u __attribute__((ext_vector_type(2)));
typedef float v2f __attribute__((ext_vector_type(2)));
typedef float v4f __attribute__((ext_vector_type(4)));
typedef short v4s __attribute__((ext_vector_type(4)));
typedef __bf16 v2bf __attribute__((ext_vector_type(2)));

constexpr int DH = 192;
constexpr int NH = 32;
constexpr int SKV = 32768;
constexpr int WPB = 8;
constexpr int NT = WPB * 64;
constexpr int NWG = 256;
constexpr int NSL = 8, SLC = 24;
constexpr int RS = NSL * 16;
static_assert(SKV == 2 * 64 * NWG, "two 64-token groups per workgroup and head");
constexpr int NX = 4;
constexpr int NRC = 17;
constexpr size_t WS_R = 262144;
constexpr size_t WS_X = WS_R + (size_t)NRC * NWG * RS;
constexpr size_t WS_T = WS_X + (size_t)NWG * 256;
constexpr unsigned BODY16 = (unsigned)(TGT * 100.0 / 64.0 * 65536.0);
constexpr int slot(int n) { return (int)((n * (BODY16 / 12)) >> 16); }

template <typename F, int... I>
__device__ __forceinline__ void sfor_impl(F&& f, std::integer_sequence<int, I...>) {
    (f(std::integral_constant<int, I>{}), ...);
}
template <int N, typename F>
__device__ __forceinline__ void sfor(F&& f) {
    sfor_impl(f, std::make_integer_sequence<int, N>{});
}

__device__ __forceinline__ float bf_lo(unsigned u) { return __uint_as_float(u << 16); }
__device__ __forceinline__ float bf_hi(unsigned u) { return __uint_as_float(u & 0xffff0000u); }
__device__ __forceinline__ float rb(float x) { return (float)(__bf16)x; }
__device__ __forceinline__ unsigned pack(float lo, float hi) {
    return __builtin_bit_cast(unsigned, __builtin_convertvector(v2f{lo, hi}, v2bf));
}
__device__ __forceinline__ float dot2(unsigned a, unsigned b, float c) {
    return __builtin_amdgcn_fdot2_f32_bf16(__builtin_bit_cast(v2bf, a), __builtin_bit_cast(v2bf, b), c, false);
}
__device__ __forceinline__ void mul4(float (&y)[4], unsigned a, unsigned b, unsigned s0, unsigned s1, unsigned s2, unsigned s3) {
    asm("v_dot2_f32_bf16 %0, %4, %6, 0\n\tv_dot2_f32_bf16 %1, %4, %7, 0\n\t"
        "v_dot2_f32_bf16 %2, %5, %8, 0\n\tv_dot2_f32_bf16 %3, %5, %9, 0\n\ts_nop 2"
        : "=&v"(y[0]), "=&v"(y[1]), "=&v"(y[2]), "=&v"(y[3])
        : "v"(a), "v"(b), "v"(s0), "v"(s1), "v"(s2), "v"(s3));
}
__device__ __forceinline__ unsigned clk() { return (unsigned)__builtin_amdgcn_s_memrealtime(); }
__device__ __forceinline__ unsigned inflight() {
    unsigned x;
    asm volatile("s_getreg_b32 %0, hwreg(HW_REG_IB_STS, 0, 4)" : "=s"(x));
    return x;
}
__device__ __forceinline__ unsigned negscale(unsigned u) { return pack(bf_lo(u) * -0x1p-22f, bf_hi(u) * -0x1p-22f); }

template <int CTRL>
__device__ __forceinline__ unsigned dppu(unsigned x) {
    return (unsigned)__builtin_amdgcn_mov_dpp((int)x, CTRL, 0xf, 0xf, true);
}
template <int CTRL>
__device__ __forceinline__ float dpp(float x) { return __uint_as_float(dppu<CTRL>(__float_as_uint(x))); }
__device__ __forceinline__ float rowsum16(float x) {
    x += dpp<0x128>(x);
    x += dpp<0x124>(x);
    x += dpp<0x122>(x);
    x += dpp<0x121>(x);
    return x;
}
__device__ __forceinline__ v4i ldstream(const void* p) {
    return __builtin_nontemporal_load(reinterpret_cast<const v4i*>(p));
}

__device__ __forceinline__ void st_rec(void* p, unsigned o, v4i a) {
    asm volatile("s_nop 4\n\tglobal_store_dwordx4 %0, %1, %2 sc1\n\ts_nop 1" :: "v"(o), "v"(a), "s"(p) : "memory");
}
__device__ __forceinline__ v4i ld_rec(const void* p, unsigned o) {
    v4i x;
    asm volatile("s_nop 4\n\tglobal_load_dwordx4 %0, %1, %2 sc1" : "=v"(x) : "v"(o), "s"(p) : "memory");
    return x;
}
typedef unsigned long long u64;
__device__ __forceinline__ u64 ld_dev(const u64* p) { return __hip_atomic_load(p, __ATOMIC_RELAXED, __HIP_MEMORY_SCOPE_AGENT); }
__device__ __forceinline__ void st_dev(u64* p, u64 v) { __hip_atomic_store(p, v, __ATOMIC_RELAXED, __HIP_MEMORY_SCOPE_AGENT); }
__device__ __forceinline__ unsigned ld_wg(const unsigned* p) { return __hip_atomic_load(p, __ATOMIC_RELAXED, __HIP_MEMORY_SCOPE_WORKGROUP); }
__device__ __forceinline__ void st_wg(unsigned* p, unsigned v) { __hip_atomic_store(p, v, __ATOMIC_RELAXED, __HIP_MEMORY_SCOPE_WORKGROUP); }
__device__ __forceinline__ float fold32(float a, float b) {
    auto s = __builtin_amdgcn_permlane32_swap(__float_as_uint(a), __float_as_uint(b), false, false);
    return __uint_as_float(s[0]) + __uint_as_float(s[1]);
}
__device__ __forceinline__ float fold16(float a, float b) {
    auto s = __builtin_amdgcn_permlane16_swap(__float_as_uint(a), __float_as_uint(b), false, false);
    return __uint_as_float(s[0]) + __uint_as_float(s[1]);
}
__device__ __forceinline__ float x4max(float x) {
    float m;
    asm("s_nop 1\n\tv_max_f32_dpp %1, %1, %1 row_ror:4 row_mask:0xf bank_mask:0xf\n\t"
        "s_nop 1\n\tv_max_f32_dpp %1, %1, %1 row_ror:8 row_mask:0xf bank_mask:0xf\n\t"
        "s_nop 1\n\tv_max_f32_dpp %1, %1, %1 row_bcast:15 row_mask:0xa bank_mask:0xf\n\t"
        "s_nop 1\n\tv_max_f32_dpp %1, %1, %1 row_bcast:31 row_mask:0xc bank_mask:0xf\n\t"
        "s_nop 0\n\tv_readlane_b32 %0, %1, 63\n\ts_nop 1" : "=s"(m), "+v"(x));
    return m;
}

struct __attribute__((aligned(16))) Smem {
    v4i ksz[(64 + NX) * 48];
    v4i qt[NH * 24];
    v4i vsz[2][(64 + NX) * 8];
    v4i rec[NRC - 1][RS / 4];
    float stage[2][WPB][DH];
    unsigned short p[NH][WPB][16];
    unsigned ml[NH];
    float fin[WPB + 1][32];
    u64 epoch;
    unsigned desc[2][2][2 * NH];
    unsigned ready;
    unsigned have;
    unsigned tref;
    unsigned slip;
    unsigned vote[2 * NH];
    unsigned late[WPB];
    unsigned req;
    unsigned t0;
    unsigned done;
    unsigned prog;
    unsigned fcnt;
    unsigned dsv;
    unsigned tick;
    unsigned quiet;
    unsigned cnt[NH + NH / 4];
};

__host__ __device__ constexpr int position(int b) { return (b & ~7) | ((b & 3) << 1) | ((b >> 2) & 1); }
constexpr int groupof(int p) {
    const int r0 = p < 85 ? 0 : p < 171 ? 85 : 171, na = p < 171 ? 43 : 42, t = p - r0;
    return r0 + (t < 64 ? ((t >> 4) & 1) * na + (t >> 5) * 16 + (t & 15) : t < na + 32 ? t - 32 : t);
}
struct Order { unsigned g[NWG / 2]; };

__global__ __launch_bounds__(NT + 128, 3) void kivi_stream_kernel(
    const __hip_bfloat16* __restrict__ q, const int* __restrict__ qk, const int* __restrict__ qv,
    const __hip_bfloat16* __restrict__ ks, const __hip_bfloat16* __restrict__ kz,
    const __hip_bfloat16* __restrict__ vs, const __hip_bfloat16* __restrict__ vz,
    float* __restrict__ ws, __hip_bfloat16* __restrict__ out, float scale,
    Order order) {
    __shared__ Smem sm;

    u64 ka0;
    unsigned ka1, ka2;
    asm volatile("s_load_dwordx2 %0, %3, 0x18\n\ts_load_dword %1, %3, %4 offset:0x4c\n\ts_load_dword %2, %3, 0x40"
                 : "=&s"(ka0), "=&s"(ka1), "=&s"(ka2) : "s"(__builtin_amdgcn_kernarg_segment_ptr()), "s"((blockIdx.x & 127) * 4));
    const u64 T0 = __builtin_amdgcn_s_memrealtime();
    const int tid = threadIdx.x;
    typedef const __attribute__((address_space(1))) int* gptr;
    u64 code;
    int codelen;
    asm volatile("s_getpc_b64 %0\nkivi_code_begin:\n\ts_mov_b32 %1, kivi_code_end-kivi_code_begin-4" : "=s"(code), "=s"(codelen));
    const int warm = *(gptr)(code + min((unsigned)tid * 128u, (unsigned)codelen));
    const int win = blockIdx.x >> 7;
    auto blockof = [&](int it) { return it >> 2 == 10 ? it : (it & ~3) + 2 * win + ((it >> 1) & 1); };
    auto halfof = [&](int it) { return (it >> 2 == 10 ? win : it) & 1; };
    auto group = [&](int it, int x) { return blockof(it) * NWG + ((x >> halfof(it) * 8) & 255); };
    unsigned pb = blockof(tid >> 3) * NWG, ph = halfof(tid >> 3) * 8;
    asm volatile("" : "+v"(pb), "+v"(ph));
    SB();
    asm volatile("s_waitcnt lgkmcnt(0)" : "+s"(ka0), "+s"(ka1), "+s"(ka2));
    const int gx = ka1 & 0xffff, px = ka1 >> 16;
    const unsigned pg = pb + ((ka1 >> ph) & 255), po = pg * (DH * 2) + (tid & 7) * 16;
    v4i pk[6];
    if (tid < NT) {
        typedef const __attribute__((address_space(1))) char* gchar;
        typedef const __attribute__((address_space(1))) v4i* gv4;
#pragma unroll
        for (int u = 0; u < 3; ++u) pk[u] = __builtin_nontemporal_load((gv4)((gchar)ka0 + po + u * 128));
    }
    SB();
    const u64 tb64 = __builtin_amdgcn_s_memrealtime() - LATEB;
    const unsigned tb = (unsigned)tb64;
    const int wave = __builtin_amdgcn_readfirstlane(tid >> 6);
    u64* tstart = reinterpret_cast<u64*>(ws + WS_T);
    if (tid == NT) st_dev(tstart + blockIdx.x, tb64);
    if (wave == WPB + 1) {
        const unsigned k = blockIdx.x >> 3, k4 = min(k, 7u) * 4;
        const u64 p = ((const __attribute__((address_space(4))) u64*)__builtin_amdgcn_kernarg_segment_ptr())[(0x13365444u >> k4) & 7u];
        unsigned x;
        asm volatile("s_load_dword %0, %1, 0x0\n\ts_waitcnt lgkmcnt(0)" : "=s"(x) : "s"(p + (((k - ((0x74443000u >> k4) & 7u)) << 21) + (blockIdx.x & 7) * 256)) : "memory");
    }
    SB();
    const int lane = tid & 63;
    const int r = lane & 15;
    const int g = lane >> 4;
    const int pos = position(blockIdx.x);
    auto tagof = [](u64 e, int j) {
        e *= 0x9e3779b97f4a7c15ull;
        return (unsigned)(j == 3 ? (e ^ (e >> 29)) >> 8 : e >> (j * 20)) << 8;
    };
    auto until = [&](unsigned t) { while ((int)(clk() - t) < 0) __builtin_amdgcn_s_sleep(1); };
    auto behind = [&]() {
        const unsigned tick = ld_wg(&sm.tick), t = clk();
        while (ld_wg(&sm.tick) == tick && clk() - t < 100u) __builtin_amdgcn_s_sleep(1);
    };
    const int quart = (pos >> 5) & 3, tau = (-((pos >> 1) + quart)) & 15;
    auto shared = [&](int b) { return SPL == 4 || ((b ^ quart) & 1) == 0; };
    auto job = [&](int t, int b, u64 pol, u64 ok, unsigned side) {
        const int bd = tau == 10 || (t == 0 && shared(b)) ? b : 2 * tau + (SPL == 4 ? 0 : quart & 1);
        const int reg = (shared(b) ? t : 1) * 32 + (group(2 * bd, pos & 1 ? px : gx) * 3 >> 13) * 4;
        return (((ok >> reg) & 1u && (t || b >= H0) ? side ^ (unsigned)(pol >> reg) : quart >> 1) ^ pos) & 1u;
    };
    auto describe = [&](int t, int it, int j, u64 pol, u64 ok, unsigned side) {
        const int b = it >> 1, n = it * 2 + j;
        const bool tr = it >> 2 == tau && tau > 0, sh = tr && shared(b);
        const unsigned jb = job(t, tr ? b : 2 * tau, pol, ok, side);
        const unsigned x = tr ? ((sh ? j : jb) ^ pos) & 1u : 0u;
        const unsigned gr = group(it, x ? px : gx), c = 6 * gr + 3;
        const unsigned flip = sh ? 0u : ((unsigned)(pol >> t * 32) >> (c >> 14) * 4) & 1u;
        const bool early = !t && b < H0;
        const unsigned okt = (unsigned)(ok >> t * 32);
        const bool known = (okt >> (it >> 2) * 3 / 8 * 4) & (okt >> ((it >> 2) * 3 + 2) / 8 * 4) & 1u;
        const unsigned ex = (early || b < 2 || !known ? (n & 3) == 0 ? EXS(0) : (n & 3) == 1 ? EXS(1) : (n & 3) == 2 ? EXS(2) : EXS(3) : 0) + (early || known ? 0 : n & 1)
                          + (n >= 84 && n < 87 ? G21 : 0) + (t ? n == 0 ? GAP1 : n == NG2 && ((pol ^ (pol >> 32)) & 0x111111u) ? GAP2 : 0 : n >= 3 && n < 3 + 4 * NSLOW ? EXK : 0);
        return gr * (64 * DH * 4) | ex << 4 | (early || known || sh ? 0u : 8u) | x << 2 | flip << 1
             | (((sh ? jb : j ^ win ^ flip) ^ (c >> 8) ^ (c >> 15)) & 1u);
    };

    const int mh = (blockIdx.x / 16 * 2 + (blockIdx.x & 7) / 4 + 20) % NH, msl = (blockIdx.x & 3) * 2 + ((blockIdx.x >> 3) & 1);
    const bool both = mh >> 1 == 10;
    v4i x[2] = {};
    unsigned fa[4], mls, one, oo;
    auto prepare = [&]() {
        auto lds = [](const void* p) { return (unsigned)(size_t)(const __attribute__((address_space(3))) char*)p; };
        fa[0] = lds(&sm.fin[wave][g == 3 ? 24 + (r & 3) * 2 : (r & 3) * 6 + g * 2]);
        fa[1] = lane == 0 ? lds(&sm.fcnt) : lds(&sm.vote[lane]);
        fa[2] = lds(&sm.fin[lane >> 3][(lane & 7) * 4]);
        fa[3] = lds(&sm.fin[lane >> 3][25]);
        mls = (lane & 3) * 8;
        one = tid < NT && lane == 0;
        oo = (mh * DH + msl * SLC + r * 4 + g) * 2;
        asm volatile("" : "+v"(fa[0]), "+v"(fa[1]), "+v"(fa[2]), "+v"(fa[3]), "+v"(mls), "+v"(one), "+v"(oo));
    };
    unsigned d_t1 = 0, d_o[5] = {};
    if (__builtin_expect(wave < WPB, 1)) {
    const int ww = (wave & 3) * 2 + (wave >> 2);
    const int ti = ww * 4 + g;
    const int tev = ti + (ti >= 11 ? 10 : 0) + (ti >= 22 ? 11 : 0), tod = ti + 11 + (ti >= 10 ? 11 : 0) + (ti >= 21 ? 10 : 0);
    const unsigned offe = tev * (DH * 4) + r * 16, offo = tod * (DH * 4) + r * 16;
    unsigned descv[2], dsc[4];
    auto lanes = [=](unsigned d) { return (d & 1u ? offo : offe) + (d & ~0x3fffu); };
    auto fetch = [&](int t) {
        while (__builtin_amdgcn_readfirstlane(ld_wg(&sm.ready)) == 0) __builtin_amdgcn_s_sleep(1);
        asm volatile("" ::: "memory");
#pragma unroll
        for (int j = 0; j < 2; ++j) {
            descv[j] = sm.desc[t][j][lane];
            if (ww >= WPB / 2 && (descv[j] & 8u)) descv[j] ^= 3u;
        }
    };
    const char* dummy = reinterpret_cast<const char*>(qv) + (size_t)(blockIdx.x * WPB + wave) * 4864;

    v4i buf[RING];
    auto burst = [&](const char* p, int i, unsigned o) {
#pragma unroll
        for (int e = 0; e < 3; ++e) { buf[i * 3 + e] = ldstream(p + o + e * 256); SB(); }
    };

    const unsigned phase = ((pos & (NWG / 2 - 1)) * (BODY16 / 512) + ww * WST * (BODY16 / (12 * 256))) >> 16;
    unsigned base = tb + phase + PSH;
    unsigned sched = 0, off = 0, offv = 0;
    int prev = 0, now = 0, hold = 0, big = 0, body = 0;
    unsigned hist = 0;
    unsigned vc = 0;
    int d_l = 0, d_n = 0, d_f = 0, d_s = 0, d_nb = 0;
    unsigned d_bar = 0, d_x = 0;
    auto pace = [&](int due) {
        int now;
        while ((now = (int)(clk() - base)) < due) __builtin_amdgcn_s_sleep(1);
        return now;
    };
    auto follow = [&](unsigned o2) {
        hold = o2 - off > SLIP / 2 ? HOLDB : o2 != off ? HOLD : max(hold - 1, 0);
        big = o2 - off > SLIP / 2 ? HOLDB : max(big - 1, 0);
        if ((int)(o2 - off) >= SLIP) ++d_nb;
        base += o2 - off;
        off = o2;
    };
    auto pacen = [&](auto N) {
        constexpr int n = decltype(N)::value;
        follow(__builtin_amdgcn_readfirstlane(offv));
        now = pace(slot(n));
        vc = inflight();
        hist <<= 1;
        if (__builtin_expect(vc > 3, 0)) {
            hist |= 1u;
            asm volatile("s_waitcnt vmcnt(3)");
            now = (int)(clk() - base);
            if (now > slot(n) + THR && prev < PRV && big == 0 && off + SLIP <= MAXOFF && body >= NOSLK
                && (int)(base + now - __builtin_amdgcn_readfirstlane(ld_wg(&sm.quiet))) >= 0) {
                if (lane == 0 && __hip_atomic_fetch_add(&sm.vote[body], 1u, __ATOMIC_RELAXED, __HIP_MEMORY_SCOPE_WORKGROUP) + 1 >= VOTES)
                    __hip_atomic_fetch_max(&sm.slip, off + SLIP, __ATOMIC_RELAXED, __HIP_MEMORY_SCOPE_WORKGROUP);
                unsigned o2;
                while ((o2 = __builtin_amdgcn_readfirstlane(ld_wg(&sm.slip))) == off && (int)(clk() - base) < now + VWAIT) {}
                if (o2 != off) { follow(o2); now = pace(slot(n)); }
            }
            if (hold == 0 && off + SS <= MAXOFF && body >= NOSM && body % NH != 21 && __builtin_popcount(hist & DBM) >= DBN && lane == 0) st_wg(&sm.req, off + SS);
        }
        if (d_n >= BA && d_n < BB) d_l = max(d_l, now - slot(n));
        if (now - slot(n) > 30 && d_f == 0) d_f = d_n + 1;
        if (off > 0 && d_s == 0) d_s = d_n + 1;
        ++d_n;
    };
    auto check = [&](auto N) {
        constexpr int n = decltype(N)::value;
        prev = now - slot(n);
        if (lane == 0) st_wg(&sm.late[wave], (unsigned)prev);
    };
    auto getslip = [&]() { offv = __hip_atomic_load(&sm.slip, __ATOMIC_RELAXED, __HIP_MEMORY_SCOPE_WORKGROUP); };
    auto arrive = [&](int i) {
        asm volatile("" ::: "memory");
        if (lane == 0) __hip_atomic_fetch_add(&sm.cnt[i], 1u, __ATOMIC_RELAXED, __HIP_MEMORY_SCOPE_WORKGROUP);
    };
    auto next_body = [&]() {
        const unsigned s2 = sched + BODY16;
        base += (s2 >> 16) - (sched >> 16);
        sched = s2;
        ++body;
        if (DIAG == 14 && body == BA) d_bar = base - (sched >> 16) - d_nb * SLIP;
    };

    {
        v4i pv[2], pq[2];
        pq[0] = reinterpret_cast<const v4i*>(q)[tid];
        pq[1] = reinterpret_cast<const v4i*>(q)[NT + (tid & 255)];
        const unsigned vo = pg * 128 + (tid & 7) * 16;
        PST(1);
        if (PVAR == 0) pace(slot(3));
        if (PVAR == 1) until(tb + slot(3) + wave * slot(3) / 8);
        if (PVAR == 3) until(tb + (slot(3) + phase) * PFP / 100);
        SB();
        pk[3] = ldstream((const char*)kz + po);
        pk[4] = ldstream((const char*)kz + po + 128);
        pk[5] = ldstream((const char*)kz + po + 256);
        const int pl = (wave & 1) * 32 + (lane & 31);
        const bool isk = pl < 48, isz = isk ? pl >= 24 : pl >= 56;
        const char* par = (const char*)(isk ? isz ? kz : ks : isz ? vz : vs) + (isk ? pl % 24 : pl & 7) * 16
                        + group(tau * 4 + (wave >> 1), px) * (isk ? DH * 2 : 128);
        asm volatile("" : "+v"(par));
        PST(2);
        if (PVAR == 0) pace(slot(6));
        if (PVAR == 1) until(tb + slot(6) + wave * slot(1) / 8);
        if (PVAR == 3) until(tb + (slot(6) + phase * 2 / 3) * PFP / 100);
        SB();
        pv[0] = ldstream((const char*)vs + vo);
        SB();
        if (PVAR == 1) until(tb + slot(7) + wave * slot(1) / 8);
        SB();
        pv[1] = ldstream((const char*)vz + vo);
        SB();
        if (PVAR == 1) until(tb + slot(8));
        SB();
        v4i pp = __builtin_nontemporal_load((const __attribute__((address_space(1))) v4i*)par);
        dsc[0] = describe(0, 0, 0, 0u, 0u, 0u);
        dsc[1] = describe(0, 0, 1, 0u, 0u, 0u);
        PST(3); pace(slot(8) + KD); SB();
        burst((const char*)qk, 0, lanes(dsc[0]));
        PST(4);
        sm.qt[tid] = pq[0];
        if (tid < 256) sm.qt[NT + tid] = pq[1];
#pragma unroll
        for (int u = 0; u < 3; ++u) sm.ksz[(tid >> 3) * 48 + u * 8 + (tid & 7)] = pk[u];
#pragma unroll
        for (int u = 0; u < 3; ++u) {
            const v4i x = pk[3 + u];
            sm.ksz[(tid >> 3) * 48 + 24 + u * 8 + (tid & 7)] = v4i{(int)negscale(x.x), (int)negscale(x.y), (int)negscale(x.z), (int)negscale(x.w)};
        }
        PST(5); pace(slot(11) + KD); SB();
        burst((const char*)qk, 1, lanes(dsc[1]));
        PST(6);
        descv[0] = describe(0, lane, 0, 0u, 0u, 0u);
        descv[1] = describe(0, lane, 1, 0u, 0u, 0u);
        SB();
        sm.vsz[0][tid] = pv[0];
        sm.vsz[1][tid] = v4i{(int)negscale(pv[1].x), (int)negscale(pv[1].y), (int)negscale(pv[1].z), (int)negscale(pv[1].w)};
        if (isz) pp = v4i{(int)negscale(pp.x), (int)negscale(pp.y), (int)negscale(pp.z), (int)negscale(pp.w)};
        *(isk ? &sm.ksz[(64 + (wave >> 1)) * 48 + pl] : &sm.vsz[isz][(64 + (wave >> 1)) * 8 + (pl & 7)]) = pp;
    }
    asm volatile("" :: "v"(warm));
    PST(7);
    __syncthreads();
    PST(8);
    if (DST == 20) d_x = sm.dsv;
    d_bar = clk();
    base = __builtin_amdgcn_readfirstlane(sm.t0) + OFF0 + phase;

    const v2f huge = {0x1p127f, 0x1p127f};

    for (int S = 0; S < NH; ++S) {
        v2f kz2[6];
        unsigned slo[6], shi[6];
        v2u qq[3];
        const char* nsup = (const char*)(S + 1 < NH ? qk : qv);
        const int head = S >> 1 == 10 ? S : (S & ~1) + win;
        float mylogit = 0.f;
        float dot = 0.f;
        sfor<12>([&](auto N) {
            constexpr int n = decltype(N)::value;
            constexpr int i = n / 3, j = n % 3;
            if (n == 6 && (S == H0 - 1 || S == NH - 1)) fetch(S == NH - 1);
            if (j == 0) {
                const unsigned d = dsc[i ^ 2] = __builtin_amdgcn_readlane(descv[i & 1], (2 * S + 1 + (i >> 1)) & 63);
                const unsigned o = lanes(d);
                base += (d >> 4) & 31u;
                pacen(N);
                SB();
                burst(i < 2 ? (const char*)qk : nsup, i ^ 2, o);
                SB();
                if (tid == NT - 1) st_wg(&sm.tick, S * 4 + i + 1);
                check(N);
            }
            if (j == 2) getslip();
            if (i % 2 == 0 || ((dsc[i] ^ dsc[i ^ 1]) & 4u)) {
                const int it = S * 2 + (i >> 1), sl = dsc[i] & 4u ? 64 + (it & 3) : it;
                const v2u* pk = reinterpret_cast<const v2u*>(&sm.ksz[sl * 48]);
                const v2u a = pk[j * 16 + r], b = pk[48 + j * 16 + r];
                slo[j * 2 + 0] = a.x & 0xffffu; shi[j * 2 + 0] = a.x & 0xffff0000u;
                slo[j * 2 + 1] = a.y & 0xffffu; shi[j * 2 + 1] = a.y & 0xffff0000u;
                kz2[j * 2 + 0] = v2f{bf_lo(b.x), bf_hi(b.x)};
                kz2[j * 2 + 1] = v2f{bf_lo(b.y), bf_hi(b.y)};
            }
            if (i == 0) qq[j] = reinterpret_cast<const v2u*>(&sm.qt[head * 24])[j * 16 + r];
            if (j == 0) dot = 0.f;
            {
                const v4i x = buf[n % RING];
                const v2f t0 = __builtin_elementwise_fma(v2f{__int_as_float(x.x), __int_as_float(x.y)}, huge, kz2[j * 2]);
                const v2f t1 = __builtin_elementwise_fma(v2f{__int_as_float(x.z), __int_as_float(x.w)}, huge, kz2[j * 2 + 1]);
                float y[4];
                mul4(y, pack(t0.x, t0.y), pack(t1.x, t1.y), slo[j * 2], shi[j * 2], slo[j * 2 + 1], shi[j * 2 + 1]);
                dot = dot2(pack(y[0], y[1]), qq[j].x, dot);
                dot = dot2(pack(y[2], y[3]), qq[j].y, dot);
            }
            if (j == 2) {
                const float d = rowsum16(dot);
                if ((r & 3) == (i ^ ((dsc[i] >> 1) & 1))) mylogit = d;
            }
            asm volatile("" : "+v"(dot), "+v"(mylogit));
            SB();
        });
        if (r < 4) sm.p[S][wave][g * 4 + r] = (unsigned short)pack(rb(mylogit * 0x1p22f) * scale, 0.f);
        if ((S & 3) == 3) arrive(NH + (S >> 2));
        if (S == NH - 3 && tid == 0) st_wg(&sm.done, 1u);
        next_body();
    }

    for (int S = 0; S < NH; ++S) {
        v2u P;
        unsigned vsl, vsh, vzf;
        v4f acc = {0.f, 0.f, 0.f, 0.f};
        float y0[12];
        unsigned b0[12];
        const bool more = S + 1 < NH;
        unsigned sl, sh;
        v2u A[12];
        v2f z2;
        sfor<12>([&](auto N) {
            constexpr int n = decltype(N)::value;
            constexpr int i = n / 3, j = n % 3;
            if (j == 0) {
                const unsigned d = dsc[i ^ 2] = __builtin_amdgcn_readlane(descv[i & 1], (2 * S + 1 + (i >> 1)) & 63);
                unsigned o = lanes(d);
                const char* src = (const char*)qv;
                if (i >= 2 && !more) {
                    o = DUM ? 0 : i * 768;
                    src = !DUM ? dummy : i == 3 && wave == 0 ? (const char*)out + min(mh, NH - 2) * (DH * 2) : (const char*)ws + (i & 1) * (15 << 18) + blockIdx.x * 768;
                }
                if (i < 2 || more) base += (d >> 4) & 31u;
                if (i < 2 || more) pacen(N);
                SB();
                burst(src, i ^ 2, o);
                SB();
                if (tid == NT - 1) st_wg(&sm.tick, 4 * NH + S * 4 + i + 1);
                check(N);
            }
            if (n == 0) {
                const unsigned need = S < 2 ? 1 : S;
                while (__builtin_amdgcn_readfirstlane(ld_wg(&sm.prog)) < need) {}
                asm volatile("" ::: "memory");
                P = *reinterpret_cast<const v2u*>(&sm.p[S][wave][g * 4]);
                P.x = __builtin_amdgcn_alignbit(P.x, P.x, (dsc[0] & 2u) << 3);
                P.y = __builtin_amdgcn_alignbit(P.y, P.y, (dsc[2] & 2u) << 3);
                const unsigned d3 = __builtin_amdgcn_readlane(descv[1], 2 * S + 1);
                const unsigned x = (((dsc[0] & 7u) | (dsc[1] & 7u) << 8) | ((dsc[2] & 7u) << 16 | (d3 & 7u) << 24)) >> (r & 3) * 8;
                const int it = S * 2 + ((r >> 1) & 1);
                const int ltok = (x & 4u ? 64 + (it & 3) : it) * 64 + (x & 1u ? tod : tev);
                vsl = reinterpret_cast<const unsigned short*>(&sm.vsz[0][0])[ltok];
                vsh = vsl << 16;
                vzf = (unsigned)reinterpret_cast<const unsigned short*>(&sm.vsz[1][0])[ltok] << 16;
            }
            if (j == 2) getslip();
            if (j == 0) {
                sl = dppu<0x55 * i>(vsl);
                sh = dppu<0x55 * i>(vsh);
                const float zb = __uint_as_float(dppu<0x55 * i>(vzf));
                z2 = v2f{zb, zb};
            }
            float y[4];
            {
                const v4i x = buf[n];
                const v2f t0 = __builtin_elementwise_fma(v2f{__int_as_float(x.x), __int_as_float(x.y)}, huge, z2);
                const v2f t1 = __builtin_elementwise_fma(v2f{__int_as_float(x.z), __int_as_float(x.w)}, huge, z2);
                mul4(y, pack(t0.x, t0.y), pack(t1.x, t1.y), sl, sh, sl, sh);
            }
#pragma unroll
            for (int e = 0; e < 4; ++e) {
                const int idx = j * 4 + e;
                if (i % 2 == 0) {
                    y0[idx] = y[e];
                    asm volatile("" : "+v"(y0[idx]));
                    if (i == 2) {
                        A[idx] = v2u{r == idx ? P.x : 0u, r == idx ? P.y : 0u};
                        if (AH) asm volatile("" : "+v"(A[idx]));
                    }
                } else if (i == 1) {
                    b0[idx] = pack(y0[idx], y[e]);
                    asm volatile("" : "+v"(b0[idx]));
                } else {
                    const v2u B = {b0[idx], pack(y0[idx], y[e])};
                    acc = __builtin_amdgcn_mfma_f32_16x16x16bf16_1k(__builtin_bit_cast(v4s, A[idx]), __builtin_bit_cast(v4s, B), acc, 0, 0, 0);
                }
            }
            if (n == 11) {
                if (S == NH - 2 && tid == 0) sm.tref = base + slot(12) + slot(3);
                if (S == NH - 3 && tid == 0) st_wg(&sm.done, 2u);
                if (lane < 48) *reinterpret_cast<v4f*>(&sm.stage[S & 1][wave][lane * 4]) = acc;
                arrive(S);
            }
            SB();
        });
        next_body();
    }
    if (DST >= 21) d_x = sm.dsv;
    d_t1 = clk();
    d_o[0] = off; d_o[1] = DST ? d_x : d_l; d_o[2] = d_f; d_o[3] = d_bar - phase; d_o[4] = DIAG >= 12 ? d_nb : d_s;
    {
        const unsigned tag = tagof(sm.epoch, lane & 3);
        const float* src = ws + WS_R + ((size_t)((both ? 0 : mh & 1) * (NWG / 2) + wave * 16) * NRC + (mh >> 1) + (mh > 20)) * RS + msl * 16;
        const unsigned so = (lane >> 2) * (NRC * RS * 4) + (lane & 3) * 16;
        prepare();
        until(base - slot(12) + slot(3) - phase + (wave - (WPB - 1)) * STG + (mh >= NH - 2 ? TPL : both ? TPC - TPB : TPC));
        for (;;) {
            x[0] = ld_rec(src, so);
            x[1] = v4i{0, 0, 0, (int)tag};
            if (both) x[1] = ld_rec(src + NWG / 2 * NRC * RS, so);
            asm volatile("s_waitcnt vmcnt(0)" : "+v"(x[0]), "+v"(x[1]));
            if (__all((((unsigned)x[0].w ^ tag) | ((unsigned)x[1].w ^ tag)) < 256u) || clk() - tb >= TMO) break;
            __builtin_amdgcn_s_sleep(PSL);
        }
#pragma unroll
        for (int d = 5; d >= 0; --d) asm volatile("" :: "v"(buf[d]));
        x[0].w ^= tag;
        x[1].w ^= tag;
    }
    } else
    if (wave == WPB) {
        const int l = lane;
        u64* echo = tstart + NWG;
        u64* gofs = tstart + 3 * NWG;
        if (l == 0) {
            if (blockIdx.x < NR) st_dev(gofs + blockIdx.x * 32, tb64 - 100000ull);
            if (blockIdx.x == NR) st_dev(gofs + 16, tb64 - 100000ull);
            sm.slip = 0;
            sm.req = 0;
            sm.done = 0;
            sm.prog = 0;
            sm.ready = 0;
            sm.tick = 0;
            sm.quiet = tb;
            sm.fcnt = 0;
            sm.epoch = tb64;
            sm.have = blockIdx.x == 0;
        }
        if (l < NH + NH / 4) sm.cnt[l] = 0;
        sm.vote[l] = 0;
        until(tb + TGATH);
        unsigned ld;
        for (;;) {
            const u64 dt = ld_dev(tstart + NWG - 8 + (l & 7)) - tb64 + 1000ull;
            ld = dt < 2000ull ? (unsigned)dt : 0u;
            if (__all(dt < 2000ull) || clk() - tb > (unsigned)TGMAX) break;
        }
        unsigned sum = ld + dppu<0x124>(ld);
        sum += dppu<0x122>(sum);
        sum += dppu<0x121>(sum);
        const bool allv = __all(ld != 0);
        ld = max(ld, dppu<0x124>(ld));
        ld = max(ld, dppu<0x122>(ld));
        ld = max(ld, dppu<0x121>(ld));
        if (MEANF && allv) ld -= (ld - sum / 8) * MEANF / 4;
        ld = __builtin_amdgcn_readfirstlane(ld);
        const u64 t64 = ld ? tb64 + ld - 1000ull : tb64;
        if (l == 0) sm.t0 = (unsigned)t64;
        if (l == 0) sm.dsv = clk() - tb;
        u64 to[NWG / 64];
        bool have = blockIdx.x == 0;
        auto chores = [&](bool all) {
            if (blockIdx.x == 0) {
                u64 x[NWG / 64];
#pragma unroll
                for (int u = 0; u < NWG / 64; ++u) x[u] = ld_dev(tstart + u * 64 + l);
#pragma unroll
                for (int u = 0; u < NWG / 64; ++u) {
                    if (all || __any(x[u] != to[u])) st_rec(echo + u * 128, l * 16, v4i{(int)(unsigned)x[u], (int)(unsigned)(x[u] >> 32), (int)tb, (int)(unsigned)(tb64 >> 32)});
                    to[u] = x[u];
                }
            } else if (!have && !all) {
                v4i x = ld_rec(echo + blockIdx.x * 2, 0);
                asm volatile("s_waitcnt vmcnt(0)" : "+v"(x));
                if ((unsigned)x.x == tb && (unsigned)x.y == (unsigned)(tb64 >> 32)) {
                    have = true;
                    if (l == 0) sm.epoch = (u64)(unsigned)x.w << 32 | (unsigned)x.z;
                    asm volatile("s_waitcnt lgkmcnt(0)" ::: "memory");
                    if (l == 0) st_wg(&sm.have, 1u);
                }
            }
        };
        if (EARLY) chores(true);
        asm volatile("s_waitcnt lgkmcnt(0)\n\ts_barrier" ::: "memory");
        asm volatile("" :: "v"(warm));
        if (!EARLY) chores(true);
        if (!ETCH) {
            until(tb + TECHO);
            behind();
            chores(false);
        }
        const u64* mine = NR > 1 ? gofs + ((blockIdx.x >> 3) % NR) * 32 : gofs + 16;
        unsigned sent = 0, stage = 0, tsh = clk();
        int nt = blockIdx.x < 200, spin = 0;
        while (stage < 2) {
            if (PB > 1 && have) __builtin_amdgcn_s_sleep(PSLEEP);
            behind();
            unsigned tch = 0;
            if (nt < 25) {
                const unsigned pg = (blockIdx.x >> 3) + 32 * (nt % 12);
                const char* tp = (nt < 12 ? (const char*)qk : nt < 24 ? (const char*)qv : (const char*)ws) + ((size_t)pg << 21);
                const bool first = PTL && (pg == 0 || (((size_t)tp >> 21) & (PTL - 1)) == 0);
                tp += (((first ? blockIdx.x >> 3 : blockIdx.x) * 29 + nt) & 255) * 256;
                if (nt == 24 && blockIdx.x >= 16) tp = (const char*)out + min((pg - 2) * 4096, NH * DH * 2 - 4u);
                if (ld_wg(&sm.tick) + (first ? TLEAD + 4 : TLEAD) >= (unsigned)max(nt / 12 * 128 + nt % 12 * 4 / 3 * 8 - nt / 24 * 32, TLEAD + TF0)) {
                    unsigned x;
                    if (nt < 24 && !first) asm volatile("s_nop 4\n\tglobal_load_dword %0, %1, %2 nt" : "+v"(tch) : "v"(0), "s"(tp) : "memory");
                    else if (nt < 24 || blockIdx.x < 48) asm volatile("s_load_dword %0, %1, 0x0\n\ts_waitcnt lgkmcnt(0)" : "=s"(x) : "s"(tp) : "memory");
                    ++nt;
                }
            }
            const u64 gd = ld_dev(mine) - t64;
            asm volatile("s_waitcnt vmcnt(0)" : "+v"(tch));
            const unsigned go = gd <= (u64)MAXOFF ? (unsigned)gd : 0u;
            const unsigned own = ld_wg(&sm.slip);
            if (go > own && l == 0) __hip_atomic_fetch_max(&sm.slip, go, __ATOMIC_RELAXED, __HIP_MEMORY_SCOPE_WORKGROUP);
            if (own <= go) tsh = clk();
            else if (clk() - tsh > (unsigned)REVT && blockIdx.x >= NSENT) { if (l == 0) st_wg(&sm.slip, go); }
            unsigned req = ld_wg(&sm.req);
            if ((int)(clk() - ld_wg(&sm.quiet)) < 0) { req = 0; if (l == 0) st_wg(&sm.req, 0u); }
            unsigned want = blockIdx.x < NSENT ? own : 0u;
            if (req == go + SS && own == go) {
                unsigned x = ld_wg(&sm.late[l & 7]);
                x += dppu<0x121>(x);
                x += dppu<0x122>(x);
                x += dppu<0x124>(x);
                want = min(req + (__builtin_amdgcn_readfirstlane(x) * LFAC / 64 & ~15u), (unsigned)MAXOFF);
            }
            if (want > go && want > sent) {
                u64 old = 0;
                behind();
                if (l == 0) st_wg(&sm.quiet, clk() + TQA);
                if (l == 0) old = __hip_atomic_fetch_max(gofs + 16, t64 + want, __ATOMIC_RELAXED, __HIP_MEMORY_SCOPE_AGENT);
                if (NR > 1 && __builtin_amdgcn_readfirstlane((unsigned)(old < t64 + want)) && l < NR)
                    __hip_atomic_fetch_max(gofs + l * 32, t64 + want, __ATOMIC_RELAXED, __HIP_MEMORY_SCOPE_AGENT);
                sent = want;
            }
            if (have ? blockIdx.x == 0 && ++spin % 8 == 0 : clk() - tb >= (unsigned)TECHO - 40) { behind(); chores(false); }
            if (ld_wg(&sm.done) > stage) {
                ++stage;
                behind();
                asm volatile("" : "+s"(code));
                int w = 0;
#pragma unroll
                for (int u = 0; u < 4; ++u) {
                    const int a = (u * 64 + l) * 128;
                    w ^= *(gptr)(code + (a < codelen ? a : 0));
                }
                asm volatile("" :: "v"(w));
            }
        }
        while (!have && clk() - tb < TMO) chores(false);
        if (l == 0) st_wg(&sm.have, 1u);
        if (blockIdx.x == 0) {
            for (spin = 1; ld_wg(&sm.fcnt) < WPB; ++spin) {
                __builtin_amdgcn_s_sleep(2);
                if (spin % 1024 == 0) chores(false);
            }
            return;
        }
        if (!DRY) return;
        prepare();
    } else {
        u64* table = tstart + 3 * NWG + NR * 32;
        const unsigned ent = blockIdx.x - CAL0;
        int lead = 0, lat = 0;
        if (PRE) {
            v4i y[PRE ? PRE : 1];
#pragma unroll
            for (int u = 0; u < PRE; ++u) y[u] = *(const v4i*)((const char*)(ws + WS_R) + (pos * NRC + NRC - 1 - (PRE > 1 ? u * 2 + (lane >> 5) : 0)) * (RS * 4) + (lane & 31) * 16);
#pragma unroll
            for (int u = 0; u < PRE; ++u) asm volatile("" :: "v"(y[u]));
        }
        __syncthreads();
        if (ent < 64 && (ent & 31) < 24 && (ent & 3) < NMEM) {
            const unsigned t0 = __builtin_amdgcn_readfirstlane(sm.t0);
            unsigned page, x[4];
            auto pair = [&](unsigned k) {
                const unsigned h = (t0 + k + (ent & ~3u) * 2) * 0x9e3779b1u;
                page = (ent & 28) * 16 + (k & 1) * 32 + 24 + ((t0 + (k & 1)) * 0x85ebca6bu >> 29);
                return (const char*)(ent < 32 ? +qk : +qv) + ((size_t)page << 21) + (h >> 25) * 16384 + ((h >> 8) & 127) * 64;
            };
            asm volatile("s_load_dword %0, %4, 0x0\n\ts_load_dword %1, %4, 0x2000\n\ts_load_dword %2, %5, 0x0\n\t"
                         "s_load_dword %3, %5, 0x2000\n\ts_waitcnt lgkmcnt(0)"
                         : "=&s"(x[0]), "=&s"(x[1]), "=&s"(x[2]), "=&s"(x[3]) : "s"(pair(ent % 4 * 2)), "s"(pair(ent % 4 * 2 + 1)) : "memory");
            until(t0 + TPRB);
#pragma nounroll
            for (unsigned k = 2; k < 2 * NMEM; ++k) {
                u64 ta, tc, td;
                asm volatile("s_memtime %0\n\ts_waitcnt lgkmcnt(0)\n\ts_load_dword %3, %4, 0x0\n\ts_waitcnt lgkmcnt(0)\n\t"
                             "s_memtime %1\n\ts_waitcnt lgkmcnt(0)\n\ts_load_dword %3, %4, 0x2000\n\ts_waitcnt lgkmcnt(0)\n\t"
                             "s_memtime %2\n\ts_waitcnt lgkmcnt(0)"
                             : "=&s"(ta), "=&s"(tc), "=&s"(td), "=&s"(x[0]) : "s"(pair((ent % 4 * 2 + k) % (2 * NMEM))) : "memory");
                const int d0 = (int)(tc - ta), d1 = (int)(td - tc), d = max(-PCL, min(PCL, d1 - d0));
                if (min(d0, d1) > 350 && d0 + d1 < 1700) lead += (page ^ (page >> 7)) & 1 ? -d : d;
                lat += d0 + d1;
            }
        }
        asm volatile("" :: "v"(warm));
        while (ld_wg(&sm.have) == 0 && clk() - tb < TMO) __builtin_amdgcn_s_sleep(4);
        asm volatile("" ::: "memory");
        const u64 E = sm.epoch;
        if (ent < 64 && (ent & 31) < 24 && (ent & 3) < NMEM) {
            unsigned xcc;
            asm volatile("s_getreg_b32 %0, hwreg(HW_REG_XCC_ID, 0, 3)" : "=s"(xcc));
            if (xcc & 4) lead = -lead;
            behind();
            if (lane == 0) st_wg(&sm.quiet, clk() + TQ);
            if (lane < 8) st_rec(table + ent * 32, lane * 16, v4i{lead, (int)(xcc - blockIdx.x), (int)(unsigned)E, (int)(unsigned)(E >> 32)});
        }
        unsigned jobs = 0;
        v4i cal;
        until(tb + TTAB);
        for (;;) {
            behind();
            cal = ld_rec(table, ((lane & 31) < 24 && (lane & 3) < NMEM ? lane : 0) * 256);
            asm volatile("s_waitcnt vmcnt(0)" : "+v"(cal));
            if (__all((unsigned)cal.z == (unsigned)E && (unsigned)cal.w == (unsigned)(E >> 32)) || clk() - tb >= TMO) break;
        }
        {
            int sum = (lane & 3) < NMEM ? cal.x : 0;
            sum += (int)dppu<0xB1>(sum);
            sum += (int)dppu<0x4E>(sum);
            const u64 pol = __ballot(sum < 0) ^ FPOL, ok = __ballot(abs(sum) >= PTH) & FMASK;
            const unsigned side = ((unsigned)__builtin_amdgcn_readfirstlane(cal.y) + (blockIdx.x & ~4u)) >> 2;
#pragma unroll
            for (int u = 0; u < 4; ++u) {
                sm.desc[u >> 1][u & 1][lane] = describe(u >> 1, lane, u & 1, pol, ok, side);
                jobs |= (tau > 0 ? job(u >> 1, 2 * tau + (u & 1), pol, ok, side) : pos & 1u) << u;
            }
            asm volatile("" ::: "memory");
            if (lane == 0 && DST == 21) sm.dsv = clk() - tb;
            if (lane == 0 && DST == 22) sm.dsv = (12 - __popcll(ok & 0x11111100111111ull)) * 10;
            if (lane == 0) st_wg(&sm.ready, 1u);
        }
#pragma nounroll
        for (int cc = 0; cc <= NH / 4; ++cc) {
            const int c = cc < NH / 4 ? cc : tau >> 1;
            const bool act = (tau > 0 && c * 2 + (g >> 1) == tau) == (cc == NH / 4);
            while (ld_wg(&sm.cnt[NH + c]) < WPB) __builtin_amdgcn_s_sleep(2);
            asm volatile("" ::: "memory");
            if (cc >= NH / 4 - 1) {
                const bool sw = ((jobs ^ (jobs >> 2)) >> (lane >> 5)) & 1u;
                v2u* mine = reinterpret_cast<v2u*>(&sm.p[2 * tau][0][0]) + lane;
                if (cc < NH / 4) {
                    if (lane == 0) st_wg(&sm.quiet, clk() + TXW + TQ);
                    if (sw) st_rec(ws + WS_X + pos * 256, lane * 16, v4i{(int)mine->x, (int)mine->y, (int)(unsigned)E, (int)(unsigned)(E >> 32)});
                } else if (__any(sw)) {
                    until(clk() + TXW);
                }
                while (cc == NH / 4 && __any(sw)) {
                    v4i x = {};
                    if (sw) asm volatile("s_nop 4\n\tglobal_load_dwordx4 %0, %1, %2 sc1 nt" : "+v"(x) : "v"(lane * 16), "s"(ws + WS_X + (pos ^ 1) * 256) : "memory");
                    asm volatile("s_waitcnt vmcnt(0)" : "+v"(x));
                    if (__all(!sw || ((unsigned)x.z == (unsigned)E && (unsigned)x.w == (unsigned)(E >> 32))) || clk() - tb >= TMO) {
                        if (sw) *mine = v2u{(unsigned)x.x, (unsigned)x.y};
                        break;
                    }
                    __builtin_amdgcn_s_sleep(8);
                }
                asm volatile("" ::: "memory");
            }
            const bool two = c * 2 + (g >> 1) != 10;
            v4i* pp = reinterpret_cast<v4i*>(&sm.p[c * 4][0][0]) + lane;
            const v4i u = *pp;
            float x[8];
#pragma unroll
            for (int e = 0; e < 4; ++e) { x[2 * e] = bf_lo(u[e]); x[2 * e + 1] = bf_hi(u[e]); }
            float M = fmaxf(fmaxf(fmaxf(x[0], x[1]), fmaxf(x[2], x[3])), fmaxf(fmaxf(x[4], x[5]), fmaxf(x[6], x[7])));
            M = fmaxf(M, dpp<0x128>(M));
            M = fmaxf(M, dpp<0x124>(M));
            M = fmaxf(M, dpp<0x122>(M));
            M = fmaxf(M, dpp<0x121>(M));
            {
                auto y = __builtin_amdgcn_permlane16_swap(__float_as_uint(M), __float_as_uint(M), false, false);
                if (two) M = fmaxf(__uint_as_float(y[0]), __uint_as_float(y[1]));
            }
            v4i o;
            float L = 0.f;
#pragma unroll
            for (int e = 0; e < 4; ++e) {
                const unsigned pk = pack(__expf(x[2 * e] - M), __expf(x[2 * e + 1] - M));
                o[e] = (int)pk;
                L += bf_lo(pk) + bf_hi(pk);
            }
            if (act) *pp = o;
            L = rowsum16(L);
            const float L2 = fold16(L, L);
            if (two) L = L2;
            if (r == 0 && act) sm.ml[c * 4 + g] = __float_as_uint(M) | __builtin_bit_cast(unsigned short, (_Float16)L);
            asm volatile("" ::: "memory");
            if (lane == 0) st_wg(&sm.prog, 1u);
        }
        const unsigned tag = tagof(E, lane & 3);
        auto place = [&](int i) {
            const int i0 = tau + (tau > 10);
            const unsigned jb = i == i0 ? jobs >> 2 : i == 11 && tau == 10 ? jobs >> 3 : pos;
            return (((pos & ~1) | (jb & 1)) * NRC + i) * (RS * 4) + (lane & 31) * 16;
        };
        const unsigned last = place(NRC - 1);
        v2f keep[3] = {};
#pragma nounroll
        for (int h = 0; h < NH; ++h) {
            const unsigned mlb = (sm.ml[h] >> (lane & 3) * 8) & 0xffu;
            if (h < NH - 1) { while (ld_wg(&sm.cnt[h]) < WPB) __builtin_amdgcn_s_sleep(4); }
            else { while (ld_wg(&sm.cnt[h]) < WPB) {} }
            asm volatile("" ::: "memory");
            v2f c[4][3];
#pragma unroll
            for (int e = 0; e < 4; ++e)
#pragma unroll
                for (int k = 0; k < 3; ++k) c[e][k] = *reinterpret_cast<const v2f*>(&sm.stage[h & 1][(lane >> 5) * 4 + e][(lane & 31) * 6 + k * 2]);
            v4i x;
            const bool first = (h & 1) == 0 && h != 20;
#pragma unroll
            for (int k = 0; k < 3; ++k) {
                const v2f a = (keep[k] + (c[0][k] + c[1][k])) + (c[2][k] + c[3][k]), b = a * 0x1p22f;
                x[k] = (int)pack(fold32(b.x, b.x), fold32(b.y, b.y));
                keep[k] = first ? a : v2f{0.f, 0.f};
            }
            x[3] = (int)(tag | mlb);
            if (h == NH - 1) {
                if (lane < 32) {
                    if (BNT & 2) asm volatile("s_nop 4\n\tglobal_store_dwordx4 %0, %1, %2 sc1 nt\n\ts_nop 1" :: "v"(last), "v"(x), "s"(ws + WS_R) : "memory");
                    else st_rec(ws + WS_R, last, x);
                }
                break;
            }
            if (!first) sm.rec[(h >> 1) + (h > 20)][lane & 31] = x;
            asm volatile("" ::: "memory");
            if (lane == 0) st_wg(&sm.prog, h + 2);
            if (h == NH - 2) {
                v4i y[NRC / 2];
                unsigned o[NRC / 2];
#pragma unroll
                for (int u = 0; u < NRC / 2; ++u) { y[u] = (&sm.rec[0][0])[u * 64 + lane]; o[u] = place(u * 2 + (lane >> 5)); }
                until(ld_wg(&sm.tref) + TREL);
#pragma unroll
                for (int u = 0; u < NRC / 2; ++u) {
                    if (BNT & 1) asm volatile("s_nop 4\n\tglobal_store_dwordx4 %0, %1, %2 sc1 nt\n\ts_nop 1" :: "v"(o[u]), "v"(y[u]), "s"(ws + WS_R) : "memory");
                    else st_rec(ws + WS_R, o[u], y[u]);
                }
            }
        }
        return;
    }

    {
        asm volatile("" : "+v"(x[0]), "+v"(x[1]));
        auto gather = [&](unsigned w) {
            w <<= mls;
            w |= dppu<0xB1>(w);
            return w | dppu<0x4E>(w);
        };
        auto half = [](unsigned u) { return (float)__builtin_bit_cast(_Float16, (unsigned short)u); };
        const unsigned ml = gather(x[0].w);
        float m = bf_hi(ml), l = half(ml), a[6];
#pragma unroll
        for (int e = 0; e < 3; ++e) { a[2 * e] = bf_lo(x[0][e]); a[2 * e + 1] = bf_hi(x[0][e]); }
        if (both) {
            const unsigned m2 = gather(x[1].w);
            const float mx = fmaxf(m, bf_hi(m2)), w0 = __expf(m - mx), w1 = __expf(bf_hi(m2) - mx);
#pragma unroll
            for (int e = 0; e < 3; ++e) {
                a[2 * e] = a[2 * e] * w0 + bf_lo(x[1][e]) * w1;
                a[2 * e + 1] = a[2 * e + 1] * w0 + bf_hi(x[1][e]) * w1;
            }
            l = l * w0 + half(m2) * w1;
            m = mx;
        }
        const float M = x4max(m), w0 = __expf(m - M);
        l *= w0;
        float c0 = fold16(fold32(a[0] * w0, a[4] * w0), fold32(a[2] * w0, l));
        float c1 = fold16(fold32(a[1] * w0, a[5] * w0), fold32(a[3] * w0, l));
        c0 += dpp<0x128>(c0); c1 += dpp<0x128>(c1);
        c0 += dpp<0x124>(c0); c1 += dpp<0x124>(c1);
        if (g == 3) c1 = M;
        unsigned arr;
        v4f y;
        float mw;
        asm volatile("ds_write_b64 %3, %4\n\tds_add_rtn_u32 %0, %5, %6\n\tds_read_b32 %2, %8\n\tds_read_b128 %1, %7\n\ts_waitcnt lgkmcnt(0)"
                     : "=&v"(arr), "=&v"(y), "=&v"(mw) : "v"(fa[0]), "v"(v2f{c0, c1}), "v"(fa[1]), "v"(one), "v"(fa[2]), "v"(fa[3]) : "memory");
        const float w = __expf(mw - x4max(mw));
        float d = fold16(fold32(y.x * w, y.z * w), fold32(y.y * w, y.w * w));
        d += dpp<0x128>(d);
        const float L = __int_as_float(__builtin_amdgcn_readlane(__float_as_int(d), 6));
        const unsigned o = pack(d * __builtin_amdgcn_rcpf(L), 0.f);
        if (__builtin_amdgcn_readfirstlane(arr) == WPB - 1 && wave < WPB) {
            if (r < 6) *(unsigned short*)((char*)out + oo) = (unsigned short)o;
        }
    }
    if (wave < WPB) {
        unsigned* dg = reinterpret_cast<unsigned*>(ws + WS_T + 655360) + (blockIdx.x * WPB + wave) * 8;
        if (lane == 0) { dg[0] = (unsigned)T0; dg[1] = d_t1; dg[2] = clk(); for (int i = 0; i < 5; ++i) dg[3 + i] = d_o[i]; }
    }
}
__global__ void kivi_end_marker() { asm volatile("kivi_code_end:"); }

__global__ void diag_kernel(const float* ws) {
    const unsigned* dg = reinterpret_cast<const unsigned*>(ws + WS_T + 655360);
    const unsigned ref = dg[0];
    int mn = 1 << 30, x = -(1 << 30), cnt = 0;
    for (int b = 0; b < 8; ++b) {
        const unsigned* e = dg + (b * 256 + threadIdx.x) * 8;
        mn = min(mn, (int)(e[0] - ref));
        const int v = DIAG == 2 ? (int)(e[0] - ref) : DIAG == 3 ? (int)e[3] : DIAG == 4 ? (int)(e[1] - ref - e[3]) : DIAG == 5 ? (int)(e[2] - ref)
                    : DIAG == 12 ? (int)(e[2] - ref - e[7] * SLIP) : DIAG == 13 ? (int)(e[3] - e[7] * SLIP) : DIAG == 6 ? (int)e[4] : DIAG == 7 || DIAG == 14 ? (int)(e[6] - ref) : DIAG == 8 ? (e[5] ? 1000 - (int)e[5] : 0) : DIAG == 9 ? (e[7] ? 1000 - (int)e[7] : 0) : 0;
        x = max(x, v);
        cnt += DIAG == 11 ? (int)e[4] : (int)e[4] > 30;
    }
    __shared__ int smn, smx, sc;
    if (threadIdx.x == 0) { smn = 1 << 30; smx = -(1 << 30); sc = 0; }
    __syncthreads();
    atomicMin(&smn, mn); atomicMax(&smx, x); atomicAdd(&sc, cnt);
    __syncthreads();
    mn = smn; x = smx;
    int X = 0;
    if (DIAG == 2 || DIAG == 7 || DIAG == 14) X = x - mn;
    if (DIAG == 3 || DIAG == 6 || DIAG == 8 || DIAG == 9) X = x;
    if (DIAG == 4 || DIAG == 5 || DIAG == 12) X = x - mn - 20000;
    if (DIAG == 13) X = x;
    if (DIAG == 10) X = sc;
    if (DIAG == 11) X = sc / 2048;
    X = max(0, min(X, 3000));
    while ((int)(clk() - ref) < mn + 25000 + X * 10) __builtin_amdgcn_s_sleep(1);
}
extern "C" void kernel_launch(void* const* d_in, const int* in_sizes, int n_in,
                              void* d_out, int out_size, void* d_ws, size_t ws_size,
                              hipStream_t stream) {
    const __hip_bfloat16* q = (const __hip_bfloat16*)d_in[0];
    const int* qk = (const int*)d_in[1];
    const int* qv = (const int*)d_in[2];
    const __hip_bfloat16* ks = (const __hip_bfloat16*)d_in[3];
    const __hip_bfloat16* kz = (const __hip_bfloat16*)d_in[4];
    const __hip_bfloat16* vs = (const __hip_bfloat16*)d_in[5];
    const __hip_bfloat16* vz = (const __hip_bfloat16*)d_in[6];
    const float scale = (float)(1.0 / sqrt((double)DH));
    Order order;
    for (int b = 0; b < NWG / 2; ++b) {
        const int p = position(b);
        order.g[b] = groupof(p) | groupof(p + 128) << 8 | groupof(p ^ 1) << 16 | groupof((p ^ 1) + 128) << 24;
    }
    kivi_stream_kernel<<<NWG, NT + 128, 0, stream>>>(q, qk, qv, ks, kz, vs, vz, (float*)d_ws, (__hip_bfloat16*)d_out, scale, order);
    diag_kernel<<<1, 256, 0, stream>>>((const float*)d_ws);
}
```

```cpp
#define MODE 3
#include <hip/hip_runtime.h>
#include <hip/hip_bf16.h>
#include <stdint.h>
#include <utility>

#define TGT 201.75
#define WST 16
#define THR 80
#define PRV 16
#define SLIP 250
#define MAXOFF 3000
#define NSENT 8
#define NR 32
#define TPL 260
#define STG 0
#define PSLEEP 32
#define OFF0 380
#define TGATH 150
#define GAP1 10
#define TREL 90
#define G21 3
#define NSLOW 1
#define EXK 6
#define NOSLK 2
#define TMO 100000000u
#define TPC 260
#define PSL 2
#define TGMAX 300
#define SBULK 1
#define PSH 0
#define STCH 1
#define SS 20
#define HOLD 4
#define VOTES 4
#define HOLDB 8
#define REVT 600
#define VWAIT 30
#define LFAC 8
#define NOSM 2
#define RING 12
#define SB() __builtin_amdgcn_sched_barrier(0)
#define PST(i) SB()

typedef int v4i __attribute__((ext_vector_type(4)));
typedef unsigned v2u __attribute__((ext_vector_type(2)));
typedef float v2f __attribute__((ext_vector_type(2)));
typedef float v4f __attribute__((ext_vector_type(4)));
typedef short v4s __attribute__((ext_vector_type(4)));
typedef __bf16 v2bf __attribute__((ext_vector_type(2)));

constexpr int DH = 192;
constexpr int NH = 32;
constexpr int SKV = 32768;
constexpr int WPB = 8;
constexpr int NT = WPB * 64;
constexpr int NWG = 256;
constexpr int NSL = 8, SLC = 24;
constexpr int RS = NSL * 16;
static_assert(SKV == 2 * 64 * NWG, "two 64-token groups per workgroup and head");
constexpr size_t WS_T = (size_t)NH * NWG * RS;
constexpr unsigned BODY16 = (unsigned)(TGT * 100.0 / 64.0 * 65536.0);
constexpr int slot(int n) { return (int)((n * (BODY16 / 12)) >> 16); }

template <typename F, int... I>
__device__ __forceinline__ void sfor_impl(F&& f, std::integer_sequence<int, I...>) {
    (f(std::integral_constant<int, I>{}), ...);
}
template <int N, typename F>
__device__ __forceinline__ void sfor(F&& f) {
    sfor_impl(f, std::make_integer_sequence<int, N>{});
}

__device__ __forceinline__ float bf_lo(unsigned u) { return __uint_as_float(u << 16); }
__device__ __forceinline__ float bf_hi(unsigned u) { return __uint_as_float(u & 0xffff0000u); }
__device__ __forceinline__ float rb(float x) { return (float)(__bf16)x; }
__device__ __forceinline__ unsigned pack(float lo, float hi) {
    return __builtin_bit_cast(unsigned, __builtin_convertvector(v2f{lo, hi}, v2bf));
}
__device__ __forceinline__ float dot2(unsigned a, unsigned b, float c) {
    return __builtin_amdgcn_fdot2_f32_bf16(__builtin_bit_cast(v2bf, a), __builtin_bit_cast(v2bf, b), c, false);
}
__device__ __forceinline__ void mul4(float (&y)[4], unsigned a, unsigned b, unsigned s0, unsigned s1, unsigned s2, unsigned s3) {
    asm("v_dot2_f32_bf16 %0, %4, %6, 0\n\tv_dot2_f32_bf16 %1, %4, %7, 0\n\t"
        "v_dot2_f32_bf16 %2, %5, %8, 0\n\tv_dot2_f32_bf16 %3, %5, %9, 0\n\ts_nop 2"
        : "=&v"(y[0]), "=&v"(y[1]), "=&v"(y[2]), "=&v"(y[3])
        : "v"(a), "v"(b), "v"(s0), "v"(s1), "v"(s2), "v"(s3));
}
__device__ __forceinline__ unsigned clk() { return (unsigned)__builtin_amdgcn_s_memrealtime(); }
__device__ __forceinline__ unsigned inflight() {
    unsigned x;
    asm volatile("s_getreg_b32 %0, hwreg(HW_REG_IB_STS, 0, 4)" : "=s"(x));
    return x;
}
__device__ __forceinline__ unsigned negscale(unsigned u) { return pack(bf_lo(u) * -0x1p-22f, bf_hi(u) * -0x1p-22f); }

template <int CTRL>
__device__ __forceinline__ unsigned dppu(unsigned x) {
    return (unsigned)__builtin_amdgcn_mov_dpp((int)x, CTRL, 0xf, 0xf, true);
}
template <int CTRL>
__device__ __forceinline__ float dpp(float x) { return __uint_as_float(dppu<CTRL>(__float_as_uint(x))); }
__device__ __forceinline__ float rowsum16(float x) {
    x += dpp<0x128>(x);
    x += dpp<0x124>(x);
    x += dpp<0x122>(x);
    x += dpp<0x121>(x);
    return x;
}
__device__ __forceinline__ v4i ldstream(const void* p) {
    return __builtin_nontemporal_load(reinterpret_cast<const v4i*>(p));
}

__device__ __forceinline__ void st_rec(void* p, unsigned o, v4i a) {
    asm volatile("global_store_dwordx4 %0, %1, %2 sc1\n\ts_nop 1" :: "v"(o), "v"(a), "s"(p) : "memory");
}
__device__ __forceinline__ v4i ld_rec(const void* p, unsigned o) {
    v4i x;
    asm volatile("global_load_dwordx4 %0, %1, %2 sc1" : "=v"(x) : "v"(o), "s"(p) : "memory");
    return x;
}
typedef unsigned long long u64;
__device__ __forceinline__ u64 ld_dev(const u64* p) { return __hip_atomic_load(p, __ATOMIC_RELAXED, __HIP_MEMORY_SCOPE_AGENT); }
__device__ __forceinline__ void st_dev(u64* p, u64 v) { __hip_atomic_store(p, v, __ATOMIC_RELAXED, __HIP_MEMORY_SCOPE_AGENT); }
__device__ __forceinline__ unsigned ld_wg(const unsigned* p) { return __hip_atomic_load(p, __ATOMIC_RELAXED, __HIP_MEMORY_SCOPE_WORKGROUP); }
__device__ __forceinline__ void st_wg(unsigned* p, unsigned v) { __hip_atomic_store(p, v, __ATOMIC_RELAXED, __HIP_MEMORY_SCOPE_WORKGROUP); }
__device__ __forceinline__ float fold32(float a, float b) {
    auto s = __builtin_amdgcn_permlane32_swap(__float_as_uint(a), __float_as_uint(b), false, false);
    return __uint_as_float(s[0]) + __uint_as_float(s[1]);
}
__device__ __forceinline__ float fold16(float a, float b) {
    auto s = __builtin_amdgcn_permlane16_swap(__float_as_uint(a), __float_as_uint(b), false, false);
    return __uint_as_float(s[0]) + __uint_as_float(s[1]);
}
__device__ __forceinline__ float x4max(float x) {
    x = fmaxf(x, dpp<0x124>(x));
    x = fmaxf(x, dpp<0x128>(x));
    auto s = __builtin_amdgcn_permlane16_swap(__float_as_uint(x), __float_as_uint(x), false, false);
    x = fmaxf(__uint_as_float(s[0]), __uint_as_float(s[1]));
    auto t = __builtin_amdgcn_permlane32_swap(__float_as_uint(x), __float_as_uint(x), false, false);
    return fmaxf(__uint_as_float(t[0]), __uint_as_float(t[1]));
}

struct __attribute__((aligned(16))) Smem {
    v4i ksz[64 * 48];
    v4i qt[NH * 24];
    v4i vsz[2 * 512];
    v4i rec[NH][RS / 4];
    float stage[2][WPB][DH];
    unsigned short p[NH][WPB][16];
    unsigned ml[NH];
    float fin[WPB][32];
    u64 epoch;
    unsigned have;
    unsigned tref;
    unsigned slip;
    unsigned vote[2 * NH];
    unsigned late[WPB];
    unsigned req;
    unsigned t0;
    unsigned done;
    unsigned prog;
    unsigned fcnt;
    unsigned cnt[NH + NH / 4];
};

__global__ __launch_bounds__(NT + 128, 3) void kivi_stream_kernel(
    const __hip_bfloat16* __restrict__ q, const int* __restrict__ qk, const int* __restrict__ qv,
    const __hip_bfloat16* __restrict__ ks, const __hip_bfloat16* __restrict__ kz,
    const __hip_bfloat16* __restrict__ vs, const __hip_bfloat16* __restrict__ vz,
    float* __restrict__ ws, __hip_bfloat16* __restrict__ out, float scale) {
    __shared__ Smem sm;

    unsigned ka0, ka1;
    asm volatile("s_load_dword %0, %2, 0x0\n\ts_load_dword %1, %2, 0x40" : "=&s"(ka0), "=&s"(ka1) : "s"(__builtin_amdgcn_kernarg_segment_ptr()));
    const u64 tb64 = __builtin_amdgcn_s_memrealtime();
    const unsigned tb = (unsigned)tb64;
    const int tid = threadIdx.x;
    typedef const __attribute__((address_space(1))) int* gptr;
    u64 code;
    int codelen;
    asm volatile("s_getpc_b64 %0\nkivi_code_begin:\n\ts_mov_b32 %1, kivi_code_end-kivi_code_begin" : "=s"(code), "=s"(codelen));
    const int warm = *(gptr)(code + min(tid * 128, codelen - 4));
    const int lane = tid & 63;
    const int wave = __builtin_amdgcn_readfirstlane(tid >> 6);
    const int r = lane & 15;
    const int g = lane >> 4;
    const int pos = (blockIdx.x & ~7) | ((blockIdx.x & 3) << 1) | ((blockIdx.x >> 2) & 1);
    const int g0 = (pos & (NWG / 2 - 1)) + (pos / (NWG / 2)) * NWG;
    constexpr int GST = NWG / 2;
    unsigned po[3];
    int dsti[3];
#pragma unroll
    for (int u = 0; u < 3; ++u) {
        const int f = u * NT + tid;
        const int gi = f / 24, pc = f % 24;
        dsti[u] = gi * 48 + pc;
        po[u] = ((gi >> 1) * (2 * NWG) + (gi & 1) * GST + g0) * (DH * 2) + pc * 16;
        asm volatile("" : "+v"(po[u]));
    }
    SB();
    v4i pk[6];
    if (wave < WPB) {
        pk[0] = ldstream((const char*)ks + po[0]);
        pk[1] = ldstream((const char*)ks + po[1]);
        pk[2] = ldstream((const char*)ks + po[2]);
    }
    asm volatile("s_waitcnt lgkmcnt(0)" :: "s"(ka0), "s"(ka1));
    SB();
    u64* tstart = reinterpret_cast<u64*>(ws + WS_T);
    auto tagof = [](u64 e, int j) {
        e *= 0x9e3779b97f4a7c15ull;
        return (unsigned)(j == 3 ? (e ^ (e >> 29)) >> 8 : e >> (j * 20)) << 8;
    };
    auto until = [&](unsigned t) { while ((int)(clk() - t) < 0) __builtin_amdgcn_s_sleep(1); };

    if (__builtin_expect(wave == WPB, 0)) {
        const int l = lane;
        u64* echo = tstart + NWG;
        u64* gofs = tstart + 2 * NWG;
        if (l == 0) {
            st_dev(tstart + blockIdx.x, tb64);
            if (blockIdx.x < NR) st_dev(gofs + blockIdx.x * 32, tb64 - 100000ull);
            if (blockIdx.x == NR) st_dev(gofs + 16, tb64 - 100000ull);
            sm.slip = 0;
            sm.req = 0;
            sm.done = 0;
            sm.prog = 0;
            sm.fcnt = 0;
            sm.epoch = tb64;
            sm.have = blockIdx.x == 0;
        }
        if (l < NH + NH / 4) sm.cnt[l] = 0;
        sm.vote[l] = 0;
        until(tb + TGATH);
        unsigned ld;
        for (;;) {
            const u64 dt = ld_dev(tstart + NWG - 8 + (l & 7)) - tb64 + 1000ull;
            ld = dt < 2000ull ? (unsigned)dt : 0u;
            if (__all(dt < 2000ull) || clk() - tb > (unsigned)TGMAX) break;
        }
        ld = max(ld, dppu<0x124>(ld));
        ld = max(ld, dppu<0x122>(ld));
        ld = max(ld, dppu<0x121>(ld));
        ld = __builtin_amdgcn_readfirstlane(ld);
        const u64 t64 = ld ? tb64 + ld - 1000ull : tb64;
        if (l == 0) sm.t0 = (unsigned)t64;
        __syncthreads();
        asm volatile("" :: "v"(warm));
        u64 E = tb64, to[NWG / 64];
        bool have = blockIdx.x == 0;
        if (have) {
#pragma unroll
            for (int u = 0; u < NWG / 64; ++u) to[u] = ld_dev(tstart + u * 64 + l);
            asm volatile("s_waitcnt vmcnt(0)" ::: "memory");
#pragma unroll
            for (int u = 0; u < NWG / 64; ++u) st_dev(echo + u * 64 + l, to[u]);
        }
        auto chores = [&]() {
            if (blockIdx.x == 0) {
#pragma unroll
                for (int u = 0; u < NWG / 64; ++u) {
                    const u64 x = ld_dev(tstart + u * 64 + l);
                    if (x != to[u]) { st_dev(echo + u * 64 + l, x); to[u] = x; }
                }
            } else if (!have && ld_dev(echo + blockIdx.x) == tb64) {
                asm volatile("" ::: "memory");
                E = ld_dev(tstart);
                have = true;
                if (l == 0) sm.epoch = E;
                asm volatile("s_waitcnt lgkmcnt(0)" ::: "memory");
                if (l == 0) st_wg(&sm.have, 1u);
            }
        };
        const u64* mine = gofs + ((blockIdx.x >> 3) % NR) * 32;
        unsigned sent = 0, stage = 0, tsh = clk();
        while (stage < 2) {
            const u64 gd = ld_dev(mine) - t64;
            const unsigned go = gd <= (u64)MAXOFF ? (unsigned)gd : 0u;
            const unsigned own = ld_wg(&sm.slip);
            if (go > own && l == 0) __hip_atomic_fetch_max(&sm.slip, go, __ATOMIC_RELAXED, __HIP_MEMORY_SCOPE_WORKGROUP);
            if (own <= go) tsh = clk();
            else if (clk() - tsh > (unsigned)REVT && blockIdx.x >= NSENT) { if (l == 0) st_wg(&sm.slip, go); }
            const unsigned req = ld_wg(&sm.req);
            unsigned want = blockIdx.x < NSENT ? own : 0u;
            if (req == go + SS && own == go) {
                unsigned x = ld_wg(&sm.late[l & 7]);
                x += dppu<0x121>(x);
                x += dppu<0x122>(x);
                x += dppu<0x124>(x);
                want = min(req + (__builtin_amdgcn_readfirstlane(x) * LFAC / 64 & ~15u), (unsigned)MAXOFF);
            }
            if (want > go && want > sent) {
                u64 old = 0;
                if (l == 0) old = __hip_atomic_fetch_max(gofs + 16, t64 + want, __ATOMIC_RELAXED, __HIP_MEMORY_SCOPE_AGENT);
                if (__builtin_amdgcn_readfirstlane((unsigned)(old < t64 + want)) && l < NR)
                    __hip_atomic_fetch_max(gofs + l * 32, t64 + want, __ATOMIC_RELAXED, __HIP_MEMORY_SCOPE_AGENT);
                sent = want;
            }
            chores();
            __builtin_amdgcn_s_sleep(PSLEEP);
            if (ld_wg(&sm.done) > stage) {
                ++stage;
                asm volatile("" : "+s"(code));
                int w = 0;
#pragma unroll
                for (int u = 0; u < 3; ++u) {
                    const int a = (u * 64 + l) * 128;
                    w ^= *(gptr)(code + (a < codelen ? a : 0));
                }
                asm volatile("" :: "v"(w));
            }
        }
        while (!have && clk() - tb < TMO) chores();
        if (l == 0) st_wg(&sm.have, 1u);
        if (blockIdx.x == 0) {
            for (int spin = 1; ld_wg(&sm.fcnt) < WPB; ++spin) {
                __builtin_amdgcn_s_sleep(2);
                if (spin % 1024 == 0) chores();
            }
        }
        return;
    }
    if (__builtin_expect(wave == WPB + 1, 0)) {
        {
            const int k = blockIdx.x >> 3;
            const char* pg = k < 3 ? (const char*)kz + ((size_t)k << 21) : k == 3 ? (const char*)vs : k == 4 ? (const char*)vz
                           : k < 7 ? (const char*)ks + ((size_t)(k - 4) << 21) : (const char*)qk + ((size_t)(k - 7) << 21);
            unsigned x;
            asm volatile("s_load_dword %0, %1, 0x0\n\ts_waitcnt lgkmcnt(0)" : "=s"(x) : "s"(pg + (blockIdx.x & 7) * 256) : "memory");
        }
        __syncthreads();
        asm volatile("" :: "v"(warm));
#pragma nounroll
        for (int i = 0; i < 26; ++i) {
            const size_t pg = (size_t)((blockIdx.x >> 3) + 32 * (i % 12)) << 21;
            const char* tp = i < 12 ? (const char*)qk + pg : i < 24 ? (const char*)qv + pg : i == 24 ? (const char*)ws + ((size_t)((blockIdx.x >> 3) & 1) << 21) : (const char*)out;
            if (i < 25) tp += (((blockIdx.x >> 3) * 29 + i) & 127) * 256;
            unsigned x;
            asm volatile("s_load_dword %0, %1, 0x0\n\ts_waitcnt lgkmcnt(0)" : "=s"(x) : "s"(tp) : "memory");
        }
#pragma nounroll
        for (int c = 0; c < NH / 4; ++c) {
            while (ld_wg(&sm.cnt[NH + c]) < WPB) __builtin_amdgcn_s_sleep(8);
            asm volatile("" ::: "memory");
            v4i* pp = reinterpret_cast<v4i*>(&sm.p[c * 4][0][0]) + lane;
            const v4i u = *pp;
            float x[8];
#pragma unroll
            for (int e = 0; e < 4; ++e) { x[2 * e] = bf_lo(u[e]); x[2 * e + 1] = bf_hi(u[e]); }
            float M = fmaxf(fmaxf(fmaxf(x[0], x[1]), fmaxf(x[2], x[3])), fmaxf(fmaxf(x[4], x[5]), fmaxf(x[6], x[7])));
            M = fmaxf(M, dpp<0x128>(M));
            M = fmaxf(M, dpp<0x124>(M));
            M = fmaxf(M, dpp<0x122>(M));
            M = fmaxf(M, dpp<0x121>(M));
            v4i o;
            float L = 0.f;
#pragma unroll
            for (int e = 0; e < 4; ++e) {
                const unsigned pk = pack(__expf(x[2 * e] - M), __expf(x[2 * e + 1] - M));
                o[e] = (int)pk;
                L += bf_lo(pk) + bf_hi(pk);
            }
            *pp = o;
            L = rowsum16(L);
            if (r == 0) sm.ml[c * 4 + g] = __float_as_uint(M) | __builtin_bit_cast(unsigned short, (_Float16)L);
            asm volatile("" ::: "memory");
            if (lane == 0) st_wg(&sm.prog, c + 1);
        }
        while (ld_wg(&sm.have) == 0) __builtin_amdgcn_s_sleep(8);
        asm volatile("" ::: "memory");
        const unsigned tag = tagof(sm.epoch, lane & 3);
        float* mine = ws + (size_t)blockIdx.x * NH * RS;
#pragma nounroll
        for (int h = 0; h < NH; ++h) {
            const unsigned mlb = (sm.ml[h] >> (lane & 3) * 8) & 0xffu;
            if (h < NH - 1) { while (ld_wg(&sm.cnt[h]) < WPB) __builtin_amdgcn_s_sleep(4); }
            else { while (ld_wg(&sm.cnt[h]) < WPB) {} }
            asm volatile("" ::: "memory");
            v2f c[4][3];
#pragma unroll
            for (int e = 0; e < 4; ++e)
#pragma unroll
                for (int k = 0; k < 3; ++k) c[e][k] = *reinterpret_cast<const v2f*>(&sm.stage[h & 1][(lane >> 5) * 4 + e][(lane & 31) * 6 + k * 2]);
            v4i x;
#pragma unroll
            for (int k = 0; k < 3; ++k) {
                const v2f a = ((c[0][k] + c[1][k]) + (c[2][k] + c[3][k])) * 0x1p22f;
                x[k] = (int)pack(fold32(a.x, a.x), fold32(a.y, a.y));
            }
            x[3] = (int)(tag | mlb);
            if (h == NH - 1) {
                if (lane < 32) st_rec(mine + h * RS, lane * 16, x);
                break;
            }
            sm.rec[h][lane & 31] = x;
            asm volatile("" ::: "memory");
            if (lane == 0) st_wg(&sm.prog, NH / 4 + h + 1);
            if (h == NH - 2) {
                v4i y[NH / 2];
#pragma unroll
                for (int u = 0; u < NH / 2; ++u) y[u] = (&sm.rec[0][0])[u * 64 + lane];
                until(ld_wg(&sm.tref) + TREL);
#pragma unroll
                for (int u = 0; u < NH / 2; ++u)
                    if (u < NH / 2 - 1 || lane < 32) st_rec(mine + u * 2 * RS, lane * 16, y[u]);
            }
        }
        return;
    }

    const int ww = (wave & 3) * 2 + (wave >> 2);
    const int ti = ww * 4 + g, tl = ti & 15;
    const int tev = (ti >> 4) * 32 + tl + (tl >= 5 ? 6 : 0) + (tl >= 10 ? 5 : 0);
    const int tod = (ti >> 4) * 32 + 5 + tl + (tl >= 6 ? 5 : 0) + (tl >= 11 ? 6 : 0);
    constexpr size_t ITER = (size_t)GST * 64 * DH * 4;
    constexpr size_t HEAD = (size_t)SKV * DH * 4;
    auto odd = [&](int S, int i) { return (i ^ (pos / (NWG / 2)) ^ ((6 * (S * 2 * NWG + g0 + (i >> 1) * GST) + 3) >> 14)) & 1; };
    const unsigned offe = tev * (DH * 4) + r * 16, offo = tod * (DH * 4) + r * 16;
    const char* kp = reinterpret_cast<const char*>(qk) + (size_t)g0 * (64 * DH * 4);
    const char* vp = reinterpret_cast<const char*>(qv) + (size_t)g0 * (64 * DH * 4);
    const char* dummy = reinterpret_cast<const char*>(qv) + (size_t)(blockIdx.x * WPB + wave) * 4864;

    v4i buf[RING];
    auto burst = [&](const char* p, int i, unsigned o) {
        p += (size_t)(i >> 1) * ITER;
#pragma unroll
        for (int e = 0; e < 3; ++e) { buf[i * 3 + e] = ldstream(p + o + e * 256); SB(); }
    };

    const unsigned phase = ((pos & (NWG / 2 - 1)) * (BODY16 / 512) + ww * WST * (BODY16 / (12 * 256))) >> 16;
    unsigned base = tb + phase + PSH;
    unsigned sched = 0, off = 0, offv = 0, ex = 0;
    int prev = 0, now = 0, hold = 0, big = 0, body = 0;
    unsigned vc = 0;
    auto pace = [&](int due) {
        int now;
        while ((now = (int)(clk() - base)) < due) __builtin_amdgcn_s_sleep(1);
        return now;
    };
    auto follow = [&](unsigned o2) {
        hold = o2 - off > SLIP / 2 ? HOLDB : o2 != off ? HOLD : max(hold - 1, 0);
        big = o2 - off > SLIP / 2 ? HOLDB : max(big - 1, 0);
        base += o2 - off;
        off = o2;
    };
    auto pacen = [&](auto N) {
        constexpr int n = decltype(N)::value;
        follow(__builtin_amdgcn_readfirstlane(offv));
        now = pace(slot(n));
        vc = inflight();
        if (__builtin_expect(vc > 3, 0)) {
            asm volatile("s_waitcnt vmcnt(3)");
            now = (int)(clk() - base);
            if (now > slot(n) + THR && prev < PRV && big == 0 && off + SLIP <= MAXOFF && body >= NOSLK) {
                if (lane == 0 && __hip_atomic_fetch_add(&sm.vote[body], 1u, __ATOMIC_RELAXED, __HIP_MEMORY_SCOPE_WORKGROUP) + 1 >= VOTES)
                    __hip_atomic_fetch_max(&sm.slip, off + SLIP, __ATOMIC_RELAXED, __HIP_MEMORY_SCOPE_WORKGROUP);
                unsigned o2;
                while ((o2 = __builtin_amdgcn_readfirstlane(ld_wg(&sm.slip))) == off && (int)(clk() - base) < now + VWAIT) {}
                if (o2 != off) { follow(o2); now = pace(slot(n)); }
            }
            if (hold == 0 && off + SS <= MAXOFF && body >= NOSM && lane == 0) st_wg(&sm.req, off + SS);
        }
    };
    auto check = [&](auto N) {
        constexpr int n = decltype(N)::value;
        base += ex;
        prev = now - slot(n);
        if (lane == 0) st_wg(&sm.late[wave], (unsigned)prev);
    };
    auto getslip = [&]() { offv = __hip_atomic_load(&sm.slip, __ATOMIC_RELAXED, __HIP_MEMORY_SCOPE_WORKGROUP); };
    auto arrive = [&](int i) {
        asm volatile("" ::: "memory");
        if (lane == 0) __hip_atomic_fetch_add(&sm.cnt[i], 1u, __ATOMIC_RELAXED, __HIP_MEMORY_SCOPE_WORKGROUP);
    };
    auto next_body = [&]() {
        const unsigned s2 = sched + BODY16;
        base += (s2 >> 16) - (sched >> 16);
        sched = s2;
        ++body;
    };

    {
        v4i pv[2], pq[2];
        pq[0] = reinterpret_cast<const v4i*>(q)[tid];
        pq[1] = reinterpret_cast<const v4i*>(q)[NT + (tid & 255)];
        const unsigned vo = (((tid >> 4) * (2 * NWG) + ((tid >> 3) & 1) * GST + g0) * 64 + (tid & 7) * 8) * 2;
        PST(1); pace(slot(3)); SB();
        pk[3] = ldstream((const char*)kz + po[0]);
        pk[4] = ldstream((const char*)kz + po[1]);
        pk[5] = ldstream((const char*)kz + po[2]);
        PST(2); pace(slot(6)); SB();
        pv[0] = ldstream((const char*)vs + vo);
        pv[1] = ldstream((const char*)vz + vo);
        PST(3); pace(slot(8)); SB();
        burst(kp, 0, odd(0, 0) ? offo : offe);
        PST(4);
        sm.qt[tid] = pq[0];
        if (tid < 256) sm.qt[NT + tid] = pq[1];
#pragma unroll
        for (int u = 0; u < 3; ++u) sm.ksz[dsti[u]] = pk[u];
#pragma unroll
        for (int u = 0; u < 3; ++u) {
            const v4i x = pk[3 + u];
            sm.ksz[dsti[u] + 24] = v4i{(int)negscale(x.x), (int)negscale(x.y), (int)negscale(x.z), (int)negscale(x.w)};
        }
        PST(5); pace(slot(11)); SB();
        burst(kp, 1, odd(0, 1) ? offo : offe);
        PST(6);
        sm.vsz[tid] = pv[0];
        sm.vsz[NT + tid] = v4i{(int)negscale(pv[1].x), (int)negscale(pv[1].y), (int)negscale(pv[1].z), (int)negscale(pv[1].w)};
    }
    asm volatile("" :: "v"(warm));
    PST(7);
    __syncthreads();
    PST(8);
    base = __builtin_amdgcn_readfirstlane(sm.t0) + OFF0 + phase;

    const v2f huge = {0x1p127f, 0x1p127f};

    for (int S = 0; S < NH; ++S) {
        ex = S < NSLOW ? EXK : S == 21 ? G21 : 0;
        v2f kz2[6];
        unsigned slo[6], shi[6];
        v2u qq[3];
        const char* nsup = (S + 1 < NH) ? kp + HEAD : vp;
        float mylogit = 0.f;
        float dot = 0.f;
        sfor<12>([&](auto N) {
            constexpr int n = decltype(N)::value;
            constexpr int i = n / 3, j = n % 3;
            if (j == 0) {
                const unsigned o = odd(i < 2 ? S : (S + 1) % NH, i ^ 2) ? offo : offe;
                pacen(N);
                SB();
                burst(i < 2 ? kp : nsup, i ^ 2, o);
                SB();
                check(N);
            }
            if (j == 2) getslip();
            if (i % 2 == 0) {
                const v2u* pk = reinterpret_cast<const v2u*>(&sm.ksz[(S * 2 + n / 6) * 48]);
                const v2u a = pk[j * 16 + r], b = pk[48 + j * 16 + r];
                slo[j * 2 + 0] = a.x & 0xffffu; shi[j * 2 + 0] = a.x & 0xffff0000u;
                slo[j * 2 + 1] = a.y & 0xffffu; shi[j * 2 + 1] = a.y & 0xffff0000u;
                kz2[j * 2 + 0] = v2f{bf_lo(b.x), bf_hi(b.x)};
                kz2[j * 2 + 1] = v2f{bf_lo(b.y), bf_hi(b.y)};
            }
            if (i == 0) qq[j] = reinterpret_cast<const v2u*>(&sm.qt[S * 24])[j * 16 + r];
            if (j == 0) dot = 0.f;
            {
                const v4i x = buf[n % RING];
                const v2f t0 = __builtin_elementwise_fma(v2f{__int_as_float(x.x), __int_as_float(x.y)}, huge, kz2[j * 2]);
                const v2f t1 = __builtin_elementwise_fma(v2f{__int_as_float(x.z), __int_as_float(x.w)}, huge, kz2[j * 2 + 1]);
                float y[4];
                mul4(y, pack(t0.x, t0.y), pack(t1.x, t1.y), slo[j * 2], shi[j * 2], slo[j * 2 + 1], shi[j * 2 + 1]);
                dot = dot2(pack(y[0], y[1]), qq[j].x, dot);
                dot = dot2(pack(y[2], y[3]), qq[j].y, dot);
            }
            if (j == 2) {
                const float d = rowsum16(dot);
                if ((r & 3) == i) mylogit = d;
            }
            asm volatile("" : "+v"(dot), "+v"(mylogit));
            SB();
        });
        if (r < 4) sm.p[S][wave][g * 4 + r] = (unsigned short)pack(rb(mylogit * 0x1p22f) * scale, 0.f);
        if ((S & 3) == 3) arrive(NH + (S >> 2));
        if (S == NH - 3 && tid == 0) st_wg(&sm.done, 1u);
        kp = nsup;
        next_body();
    }
    base += GAP1;

    for (int S = 0; S < NH; ++S) {
        ex = S == 21 ? G21 : 0;
        v2u P;
        unsigned vsl, vsh, vzf;
        v4f acc = {0.f, 0.f, 0.f, 0.f};
        float y0[12];
        unsigned b0[12];
        const bool more = S + 1 < NH;
        const char* nsup = more ? vp + HEAD : dummy;
        unsigned sl, sh;
        v2f z2;
        sfor<12>([&](auto N) {
            constexpr int n = decltype(N)::value;
            constexpr int i = n / 3, j = n % 3;
            if (j == 0) {
                unsigned o = odd(i < 2 ? S : S + 1, i ^ 2) ? offo : offe;
                if (i >= 2 && !more) o = i * 768;
                if (i < 2 || more) pacen(N);
                SB();
                burst(i < 2 ? vp : nsup, i ^ 2, o);
                SB();
                check(N);
            }
            if (n == 0) {
                const unsigned need = S < 2 ? 1 : NH / 4 + S - 1;
                while (__builtin_amdgcn_readfirstlane(ld_wg(&sm.prog)) < need) {}
                asm volatile("" ::: "memory");
                P = *reinterpret_cast<const v2u*>(&sm.p[S][wave][g * 4]);
                const int ltok = S * 128 + ((r >> 1) & 1) * 64 + (odd(S, r & 3) ? tod : tev);
                const unsigned short* pv = reinterpret_cast<const unsigned short*>(&sm.vsz[0]);
                vsl = pv[ltok];
                vsh = vsl << 16;
                vzf = (unsigned)pv[4096 + ltok] << 16;
            }
            if (j == 2) getslip();
            if (j == 0) {
                sl = dppu<0x55 * i>(vsl);
                sh = dppu<0x55 * i>(vsh);
                const float zb = __uint_as_float(dppu<0x55 * i>(vzf));
                z2 = v2f{zb, zb};
            }
            float y[4];
            {
                const v4i x = buf[n];
                const v2f t0 = __builtin_elementwise_fma(v2f{__int_as_float(x.x), __int_as_float(x.y)}, huge, z2);
                const v2f t1 = __builtin_elementwise_fma(v2f{__int_as_float(x.z), __int_as_float(x.w)}, huge, z2);
                mul4(y, pack(t0.x, t0.y), pack(t1.x, t1.y), sl, sh, sl, sh);
            }
#pragma unroll
            for (int e = 0; e < 4; ++e) {
                const int idx = j * 4 + e;
                if (i % 2 == 0) {
                    y0[idx] = y[e];
                    asm volatile("" : "+v"(y0[idx]));
                } else if (i == 1) {
                    b0[idx] = pack(y0[idx], y[e]);
                    asm volatile("" : "+v"(b0[idx]));
                } else {
                    const v2u B = {b0[idx], pack(y0[idx], y[e])};
                    const v2u A = {r == idx ? P.x : 0u, r == idx ? P.y : 0u};
                    acc = __builtin_amdgcn_mfma_f32_16x16x16bf16_1k(__builtin_bit_cast(v4s, A), __builtin_bit_cast(v4s, B), acc, 0, 0, 0);
                }
            }
            if (n == 11) {
                if (S == NH - 2 && tid == 0) sm.tref = base + slot(12) + slot(3);
                if (S == NH - 3 && tid == 0) st_wg(&sm.done, 2u);
                if (lane < 48) *reinterpret_cast<v4f*>(&sm.stage[S & 1][wave][lane * 4]) = acc;
                arrive(S);
            }
            SB();
        });
        vp = nsup;
        next_body();
    }
    {
        const int mh = (blockIdx.x / 16 * 2 + (blockIdx.x & 7) / 4 + NH - 1) % NH, msl = (blockIdx.x & 3) * 2 + ((blockIdx.x >> 3) & 1);
        const unsigned tag = tagof(sm.epoch, lane & 3);
        const float* src = ws + ((size_t)wave * 32 * NH + mh) * RS + msl * 16;
        const unsigned so = (lane >> 2) * (NH * RS * 4) + (lane & 3) * 16;
        until(base - slot(12) + slot(3) - phase + wave * STG + (mh == NH - 1 ? TPL : TPC));
        v4i x[2];
        for (;;) {
            x[0] = ld_rec(src, so);
            x[1] = ld_rec(src + 16 * NH * RS, so);
            asm volatile("s_waitcnt vmcnt(0)" : "+v"(x[0]), "+v"(x[1]));
            if (__all((((unsigned)x[0].w ^ tag) | ((unsigned)x[1].w ^ tag)) < 256u) || clk() - tb >= TMO) break;
            __builtin_amdgcn_s_sleep(PSL);
        }
#pragma unroll
        for (int d = 0; d < 6; ++d) asm volatile("" :: "v"(buf[d]));
        unsigned ml[2];
#pragma unroll
        for (int k = 0; k < 2; ++k) {
            ml[k] = ((unsigned)x[k].w ^ tag) << (lane & 3) * 8;
            ml[k] |= dppu<0xB1>(ml[k]);
            ml[k] |= dppu<0x4E>(ml[k]);
        }
        const float M = x4max(fmaxf(bf_hi(ml[0]), bf_hi(ml[1])));
        const float w0 = __expf(bf_hi(ml[0]) - M), w1 = __expf(bf_hi(ml[1]) - M);
        float a[6];
#pragma unroll
        for (int e = 0; e < 3; ++e) {
            a[2 * e] = bf_lo(x[0][e]) * w0 + bf_lo(x[1][e]) * w1;
            a[2 * e + 1] = bf_hi(x[0][e]) * w0 + bf_hi(x[1][e]) * w1;
        }
        const float l = (float)__builtin_bit_cast(_Float16, (unsigned short)ml[0]) * w0 + (float)__builtin_bit_cast(_Float16, (unsigned short)ml[1]) * w1;
        float c0 = fold16(fold32(a[0], a[4]), fold32(a[2], l));
        float c1 = fold16(fold32(a[1], a[5]), fold32(a[3], l));
        c0 += dpp<0x124>(c0); c1 += dpp<0x124>(c1);
        c0 += dpp<0x128>(c0); c1 += dpp<0x128>(c1);
        if (g == 3) c1 = M;
        if (r < 4) *reinterpret_cast<v2f*>(&sm.fin[wave][g == 3 ? 24 + r * 2 : r * 6 + g * 2]) = v2f{c0, c1};
        asm volatile("" ::: "memory");
        unsigned arr = 0;
        if (lane == 0) arr = __hip_atomic_fetch_add(&sm.fcnt, 1u, __ATOMIC_RELAXED, __HIP_MEMORY_SCOPE_WORKGROUP);
        asm volatile("" ::: "memory");
        const v4f y = *reinterpret_cast<const volatile v4f*>(&sm.fin[lane >> 3][(lane & 7) * 4]);
        const float mw = *reinterpret_cast<const volatile float*>(&sm.fin[lane >> 3][25]);
        const float w = __expf(mw - x4max(mw));
        float d = fold16(fold32(y.x * w, y.z * w), fold32(y.y * w, y.w * w));
        d += dpp<0x128>(d);
        const float L = __int_as_float(__builtin_amdgcn_readlane(__float_as_int(d), 6));
        const unsigned o = pack(d / L, 0.f);
        asm volatile("" :: "v"(o));
        if (__builtin_amdgcn_readfirstlane(arr) == WPB - 1) {
            if (r < 6) reinterpret_cast<unsigned short*>(out)[mh * DH + msl * SLC + r * 4 + g] = (unsigned short)o;
        }
    }
}
__global__ void kivi_end_marker() { asm volatile("kivi_code_end:"); }

__global__ void tlb_probe(const char* k, unsigned* res) {
    const unsigned t = clk();
    if (blockIdx.x == 0 && threadIdx.x == 0) res[255] = t;
    const char* p = k + ((size_t)(64 + blockIdx.x * 32) << 21);
    p += (0 - (size_t)p) & ((16u << 20) - 1);
    int d[3] = {0, 0, 0};
    for (int i = 0; i < 15; ++i) {
        const char* a = i < 8 ? p + ((size_t)i << 21) + i * 4352 : p + (i - 7) * (262144 + 4352);
        u64 t0, t1; unsigned x;
        asm volatile("s_memtime %0\n\ts_waitcnt lgkmcnt(0)\n\ts_load_dword %2, %3, 0x0\n\ts_waitcnt lgkmcnt(0)\n\t"
                     "s_memtime %1\n\ts_waitcnt lgkmcnt(0)" : "=&s"(t0), "=&s"(t1), "=&s"(x) : "s"(a) : "memory");
        d[i == 0 ? 0 : i < 8 ? 1 : 2] += (int)(t1 - t0);
    }
    if (threadIdx.x == 0) { res[blockIdx.x * 4] = d[0]; res[blockIdx.x * 4 + 1] = d[1] / 7; res[blockIdx.x * 4 + 2] = d[2] / 7; res[blockIdx.x * 4 + 3] = (unsigned)((size_t)k >> 12) & 511; }
}
__global__ void spin_kernel(const unsigned* res) {
    int X = 0;
    for (int b = 0; b < 8; ++b) X += res[b * 4 + MODE];
    X /= 16;
    while ((int)(clk() - res[255]) < 30000 + X * 10) __builtin_amdgcn_s_sleep(1);
}
extern "C" void kernel_launch(void* const* d_in, const int* in_sizes, int n_in,
                              void* d_out, int out_size, void* d_ws, size_t ws_size,
                              hipStream_t stream) {
    const __hip_bfloat16* q = (const __hip_bfloat16*)d_in[0];
    const int* qk = (const int*)d_in[1];
    const int* qv = (const int*)d_in[2];
    const __hip_bfloat16* ks = (const __hip_bfloat16*)d_in[3];
    const __hip_bfloat16* kz = (const __hip_bfloat16*)d_in[4];
    const __hip_bfloat16* vs = (const __hip_bfloat16*)d_in[5];
    const __hip_bfloat16* vz = (const __hip_bfloat16*)d_in[6];
    const float scale = (float)(1.0 / sqrt((double)DH));
    unsigned* res = (unsigned*)((char*)d_ws + (64 << 20));
    tlb_probe<<<8, 64, 0, stream>>>((const char*)qk, res);
    kivi_stream_kernel<<<NWG, NT + 128, 0, stream>>>(q, qk, qv, ks, kz, vs, vz, (float*)d_ws, (__hip_bfloat16*)d_out, scale);
    spin_kernel<<<1, 64, 0, stream>>>(res);
}
```

```cpp
#define DIAG 13
#include <hip/hip_runtime.h>
#include <hip/hip_bf16.h>
#include <stdint.h>
#include <utility>

#define TGT 199.25
#define WST 16
#define THR 80
#define PRV 16
#define SLIP 250
#define MAXOFF 3000
#define NSENT 8
#define NR 32
#define TPL 265
#define STG 4
#define PSLEEP 16
#define OFF0 380
#define TGATH 150
#define GAP1 10
#define TREL 110
#define NSLOW 1
#define EXK 7
#define NOSLK 2
#define TMO 100000000u
#define TPC 265
#define PSL 2
#define DST 0
#define BA 0
#define BB 16
#define TGMAX 300
#define PSH 0
#define SS 20
#define HOLD 4
#define VOTES 4
#define HOLDB 8
#define REVT 600
#define VWAIT 30
#define LFAC 4
#define NOSM 2
#define H0 5
#define TGS 203.0
#define SPL 2
#define G21 5
#define EXS(i) ((int)(((i) + 1) * (TGS - TGT) * 100.0 / 256.0 + 0.5) - (int)((i) * (TGS - TGT) * 100.0 / 256.0 + 0.5))
#define CAL0 128
#define PTH (NMEM * (NMEM - 1) * 150)
#define NMEM 2
#define PCL 500
#define TPRB 580
#define DBM 7
#define DBN 2
#define TQA 500
#define PB 1
#define PTL 8
#define EARLY 0
#define LATEB 20
#define MEANF 0
#define PF 100
#define DRY 0
#define TPB 20
#define PRE 0
#define BNT 0
#define PVAR 0
#define PFP 50
#define KD 0
#define ETCH 1
#define TF0 2
#define VT 0
#define TECHO2 900
#define AH 1
#define DUM 1
#define TLEAD 14
#define GAP2 10
#define NG2 3
#define TECHO 650
#define TQ 300
#define TTAB 1300
#define TXW 200
#define FMASK ~0ull
#define FPOL 0ull
#define RING 12
#define SB() __builtin_amdgcn_sched_barrier(0)
#define PST(i) SB()
#undef PST
#define PST(i) do { SB(); if (DST == i + 1) d_x = clk() - base; SB(); } while (0)

typedef int v4i __attribute__((ext_vector_type(4)));
typedef unsigned v2u __attribute__((ext_vector_type(2)));
typedef float v2f __attribute__((ext_vector_type(2)));
typedef float v4f __attribute__((ext_vector_type(4)));
typedef short v4s __attribute__((ext_vector_type(4)));
typedef __bf16 v2bf __attribute__((ext_vector_type(2)));

constexpr int DH = 192;
constexpr int NH = 32;
constexpr int SKV = 32768;
constexpr int WPB = 8;
constexpr int NT = WPB * 64;
constexpr int NWG = 256;
constexpr int NSL = 8, SLC = 24;
constexpr int RS = NSL * 16;
static_assert(SKV == 2 * 64 * NWG, "two 64-token groups per workgroup and head");
constexpr int NX = 4;
constexpr int NRC = 17;
constexpr size_t WS_R = 262144;
constexpr size_t WS_X = WS_R + (size_t)NRC * NWG * RS;
constexpr size_t WS_T = WS_X + (size_t)NWG * 256;
constexpr unsigned BODY16 = (unsigned)(TGT * 100.0 / 64.0 * 65536.0);
constexpr int slot(int n) { return (int)((n * (BODY16 / 12)) >> 16); }

template <typename F, int... I>
__device__ __forceinline__ void sfor_impl(F&& f, std::integer_sequence<int, I...>) {
    (f(std::integral_constant<int, I>{}), ...);
}
template <int N, typename F>
__device__ __forceinline__ void sfor(F&& f) {
    sfor_impl(f, std::make_integer_sequence<int, N>{});
}

__device__ __forceinline__ float bf_lo(unsigned u) { return __uint_as_float(u << 16); }
__device__ __forceinline__ float bf_hi(unsigned u) { return __uint_as_float(u & 0xffff0000u); }
__device__ __forceinline__ float rb(float x) { return (float)(__bf16)x; }
__device__ __forceinline__ unsigned pack(float lo, float hi) {
    return __builtin_bit_cast(unsigned, __builtin_convertvector(v2f{lo, hi}, v2bf));
}
__device__ __forceinline__ float dot2(unsigned a, unsigned b, float c) {
    return __builtin_amdgcn_fdot2_f32_bf16(__builtin_bit_cast(v2bf, a), __builtin_bit_cast(v2bf, b), c, false);
}
__device__ __forceinline__ void mul4(float (&y)[4], unsigned a, unsigned b, unsigned s0, unsigned s1, unsigned s2, unsigned s3) {
    asm("v_dot2_f32_bf16 %0, %4, %6, 0\n\tv_dot2_f32_bf16 %1, %4, %7, 0\n\t"
        "v_dot2_f32_bf16 %2, %5, %8, 0\n\tv_dot2_f32_bf16 %3, %5, %9, 0\n\ts_nop 2"
        : "=&v"(y[0]), "=&v"(y[1]), "=&v"(y[2]), "=&v"(y[3])
        : "v"(a), "v"(b), "v"(s0), "v"(s1), "v"(s2), "v"(s3));
}
__device__ __forceinline__ unsigned clk() { return (unsigned)__builtin_amdgcn_s_memrealtime(); }
__device__ __forceinline__ unsigned inflight() {
    unsigned x;
    asm volatile("s_getreg_b32 %0, hwreg(HW_REG_IB_STS, 0, 4)" : "=s"(x));
    return x;
}
__device__ __forceinline__ unsigned negscale(unsigned u) { return pack(bf_lo(u) * -0x1p-22f, bf_hi(u) * -0x1p-22f); }

template <int CTRL>
__device__ __forceinline__ unsigned dppu(unsigned x) {
    return (unsigned)__builtin_amdgcn_mov_dpp((int)x, CTRL, 0xf, 0xf, true);
}
template <int CTRL>
__device__ __forceinline__ float dpp(float x) { return __uint_as_float(dppu<CTRL>(__float_as_uint(x))); }
__device__ __forceinline__ float rowsum16(float x) {
    x += dpp<0x128>(x);
    x += dpp<0x124>(x);
    x += dpp<0x122>(x);
    x += dpp<0x121>(x);
    return x;
}
__device__ __forceinline__ v4i ldstream(const void* p) {
    return __builtin_nontemporal_load(reinterpret_cast<const v4i*>(p));
}

__device__ __forceinline__ void st_rec(void* p, unsigned o, v4i a) {
    asm volatile("s_nop 4\n\tglobal_store_dwordx4 %0, %1, %2 sc1\n\ts_nop 1" :: "v"(o), "v"(a), "s"(p) : "memory");
}
__device__ __forceinline__ v4i ld_rec(const void* p, unsigned o) {
    v4i x;
    asm volatile("s_nop 4\n\tglobal_load_dwordx4 %0, %1, %2 sc1" : "=v"(x) : "v"(o), "s"(p) : "memory");
    return x;
}
typedef unsigned long long u64;
__device__ __forceinline__ u64 ld_dev(const u64* p) { return __hip_atomic_load(p, __ATOMIC_RELAXED, __HIP_MEMORY_SCOPE_AGENT); }
__device__ __forceinline__ void st_dev(u64* p, u64 v) { __hip_atomic_store(p, v, __ATOMIC_RELAXED, __HIP_MEMORY_SCOPE_AGENT); }
__device__ __forceinline__ unsigned ld_wg(const unsigned* p) { return __hip_atomic_load(p, __ATOMIC_RELAXED, __HIP_MEMORY_SCOPE_WORKGROUP); }
__device__ __forceinline__ void st_wg(unsigned* p, unsigned v) { __hip_atomic_store(p, v, __ATOMIC_RELAXED, __HIP_MEMORY_SCOPE_WORKGROUP); }
__device__ __forceinline__ float fold32(float a, float b) {
    auto s = __builtin_amdgcn_permlane32_swap(__float_as_uint(a), __float_as_uint(b), false, false);
    return __uint_as_float(s[0]) + __uint_as_float(s[1]);
}
__device__ __forceinline__ float fold16(float a, float b) {
    auto s = __builtin_amdgcn_permlane16_swap(__float_as_uint(a), __float_as_uint(b), false, false);
    return __uint_as_float(s[0]) + __uint_as_float(s[1]);
}
__device__ __forceinline__ float x4max(float x) {
    float m;
    asm("s_nop 1\n\tv_max_f32_dpp %1, %1, %1 row_ror:4 row_mask:0xf bank_mask:0xf\n\t"
        "s_nop 1\n\tv_max_f32_dpp %1, %1, %1 row_ror:8 row_mask:0xf bank_mask:0xf\n\t"
        "s_nop 1\n\tv_max_f32_dpp %1, %1, %1 row_bcast:15 row_mask:0xa bank_mask:0xf\n\t"
        "s_nop 1\n\tv_max_f32_dpp %1, %1, %1 row_bcast:31 row_mask:0xc bank_mask:0xf\n\t"
        "s_nop 0\n\tv_readlane_b32 %0, %1, 63\n\ts_nop 1" : "=s"(m), "+v"(x));
    return m;
}

struct __attribute__((aligned(16))) Smem {
    v4i ksz[(64 + NX) * 48];
    v4i qt[NH * 24];
    v4i vsz[2][(64 + NX) * 8];
    v4i rec[NRC - 1][RS / 4];
    float stage[2][WPB][DH];
    unsigned short p[NH][WPB][16];
    unsigned ml[NH];
    float fin[WPB + 1][32];
    u64 epoch;
    unsigned desc[2][2][2 * NH];
    unsigned ready;
    unsigned have;
    unsigned tref;
    unsigned slip;
    unsigned vote[2 * NH];
    unsigned late[WPB];
    unsigned req;
    unsigned t0;
    unsigned done;
    unsigned prog;
    unsigned fcnt;
    unsigned dsv;
    unsigned tick;
    unsigned quiet;
    unsigned cnt[NH + NH / 4];
};

__host__ __device__ constexpr int position(int b) { return (b & ~7) | ((b & 3) << 1) | ((b >> 2) & 1); }
constexpr int groupof(int p) {
    const int r0 = p < 85 ? 0 : p < 171 ? 85 : 171, na = p < 171 ? 43 : 42, t = p - r0;
    return r0 + (t < 64 ? ((t >> 4) & 1) * na + (t >> 5) * 16 + (t & 15) : t < na + 32 ? t - 32 : t);
}
struct Order { unsigned g[NWG / 2]; };

__global__ __launch_bounds__(NT + 128, 3) void kivi_stream_kernel(
    const __hip_bfloat16* __restrict__ q, const int* __restrict__ qk, const int* __restrict__ qv,
    const __hip_bfloat16* __restrict__ ks, const __hip_bfloat16* __restrict__ kz,
    const __hip_bfloat16* __restrict__ vs, const __hip_bfloat16* __restrict__ vz,
    float* __restrict__ ws, __hip_bfloat16* __restrict__ out, float scale,
    Order order) {
    __shared__ Smem sm;

    u64 ka0;
    unsigned ka1, ka2;
    asm volatile("s_load_dwordx2 %0, %3, 0x18\n\ts_load_dword %1, %3, %4 offset:0x4c\n\ts_load_dword %2, %3, 0x40"
                 : "=&s"(ka0), "=&s"(ka1), "=&s"(ka2) : "s"(__builtin_amdgcn_kernarg_segment_ptr()), "s"((blockIdx.x & 127) * 4));
    const u64 T0 = __builtin_amdgcn_s_memrealtime();
    const int tid = threadIdx.x;
    typedef const __attribute__((address_space(1))) int* gptr;
    u64 code;
    int codelen;
    asm volatile("s_getpc_b64 %0\nkivi_code_begin:\n\ts_mov_b32 %1, kivi_code_end-kivi_code_begin-4" : "=s"(code), "=s"(codelen));
    const int warm = *(gptr)(code + min((unsigned)tid * 128u, (unsigned)codelen));
    const int win = blockIdx.x >> 7;
    auto blockof = [&](int it) { return it >> 2 == 10 ? it : (it & ~3) + 2 * win + ((it >> 1) & 1); };
    auto halfof = [&](int it) { return (it >> 2 == 10 ? win : it) & 1; };
    auto group = [&](int it, int x) { return blockof(it) * NWG + ((x >> halfof(it) * 8) & 255); };
    unsigned pb = blockof(tid >> 3) * NWG, ph = halfof(tid >> 3) * 8;
    asm volatile("" : "+v"(pb), "+v"(ph));
    SB();
    asm volatile("s_waitcnt lgkmcnt(0)" : "+s"(ka0), "+s"(ka1), "+s"(ka2));
    const int gx = ka1 & 0xffff, px = ka1 >> 16;
    const unsigned pg = pb + ((ka1 >> ph) & 255), po = pg * (DH * 2) + (tid & 7) * 16;
    v4i pk[6];
    if (tid < NT) {
        typedef const __attribute__((address_space(1))) char* gchar;
        typedef const __attribute__((address_space(1))) v4i* gv4;
#pragma unroll
        for (int u = 0; u < 3; ++u) pk[u] = __builtin_nontemporal_load((gv4)((gchar)ka0 + po + u * 128));
    }
    SB();
    const u64 tb64 = __builtin_amdgcn_s_memrealtime() - LATEB;
    const unsigned tb = (unsigned)tb64;
    const int wave = __builtin_amdgcn_readfirstlane(tid >> 6);
    u64* tstart = reinterpret_cast<u64*>(ws + WS_T);
    if (tid == NT) st_dev(tstart + blockIdx.x, tb64);
    if (wave == WPB + 1) {
        const unsigned k = blockIdx.x >> 3, k4 = min(k, 7u) * 4;
        const u64 p = ((const __attribute__((address_space(4))) u64*)__builtin_amdgcn_kernarg_segment_ptr())[(0x13365444u >> k4) & 7u];
        unsigned x;
        asm volatile("s_load_dword %0, %1, 0x0\n\ts_waitcnt lgkmcnt(0)" : "=s"(x) : "s"(p + (((k - ((0x74443000u >> k4) & 7u)) << 21) + (blockIdx.x & 7) * 256)) : "memory");
    }
    SB();
    const int lane = tid & 63;
    const int r = lane & 15;
    const int g = lane >> 4;
    const int pos = position(blockIdx.x);
    auto tagof = [](u64 e, int j) {
        e *= 0x9e3779b97f4a7c15ull;
        return (unsigned)(j == 3 ? (e ^ (e >> 29)) >> 8 : e >> (j * 20)) << 8;
    };
    auto until = [&](unsigned t) { while ((int)(clk() - t) < 0) __builtin_amdgcn_s_sleep(1); };
    auto behind = [&]() {
        const unsigned tick = ld_wg(&sm.tick), t = clk();
        while (ld_wg(&sm.tick) == tick && clk() - t < 100u) __builtin_amdgcn_s_sleep(1);
    };
    const int quart = (pos >> 5) & 3, tau = (-((pos >> 1) + quart)) & 15;
    auto shared = [&](int b) { return SPL == 4 || ((b ^ quart) & 1) == 0; };
    auto job = [&](int t, int b, u64 pol, u64 ok, unsigned side) {
        const int bd = tau == 10 || (t == 0 && shared(b)) ? b : 2 * tau + (SPL == 4 ? 0 : quart & 1);
        const int reg = (shared(b) ? t : 1) * 32 + (group(2 * bd, pos & 1 ? px : gx) * 3 >> 13) * 4;
        return (((ok >> reg) & 1u && (t || b >= H0) ? side ^ (unsigned)(pol >> reg) : quart >> 1) ^ pos) & 1u;
    };
    auto describe = [&](int t, int it, int j, u64 pol, u64 ok, unsigned side) {
        const int b = it >> 1, n = it * 2 + j;
        const bool tr = it >> 2 == tau && tau > 0, sh = tr && shared(b);
        const unsigned jb = job(t, tr ? b : 2 * tau, pol, ok, side);
        const unsigned x = tr ? ((sh ? j : jb) ^ pos) & 1u : 0u;
        const unsigned gr = group(it, x ? px : gx), c = 6 * gr + 3;
        const unsigned flip = sh ? 0u : ((unsigned)(pol >> t * 32) >> (c >> 14) * 4) & 1u;
        const bool early = !t && b < H0;
        const unsigned okt = (unsigned)(ok >> t * 32);
        const bool known = (okt >> (it >> 2) * 3 / 8 * 4) & (okt >> ((it >> 2) * 3 + 2) / 8 * 4) & 1u;
        const unsigned ex = (early || b < 2 || !known ? (n & 3) == 0 ? EXS(0) : (n & 3) == 1 ? EXS(1) : (n & 3) == 2 ? EXS(2) : EXS(3) : 0) + (early || known ? 0 : n & 1)
                          + (n >= 84 && n < 87 ? G21 : 0) + (t ? n == 0 ? GAP1 : n == NG2 && ((pol ^ (pol >> 32)) & 0x111111u) ? GAP2 : 0 : n >= 3 && n < 3 + 4 * NSLOW ? EXK : 0);
        return gr * (64 * DH * 4) | ex << 4 | (early || known || sh ? 0u : 8u) | x << 2 | flip << 1
             | (((sh ? jb : j ^ win ^ flip) ^ (c >> 8) ^ (c >> 15)) & 1u);
    };

    const int mh = (blockIdx.x / 16 * 2 + (blockIdx.x & 7) / 4 + 20) % NH, msl = (blockIdx.x & 3) * 2 + ((blockIdx.x >> 3) & 1);
    const bool both = mh >> 1 == 10;
    v4i x[2] = {};
    unsigned fa[4], mls, one, oo;
    auto prepare = [&]() {
        auto lds = [](const void* p) { return (unsigned)(size_t)(const __attribute__((address_space(3))) char*)p; };
        fa[0] = lds(&sm.fin[wave][g == 3 ? 24 + (r & 3) * 2 : (r & 3) * 6 + g * 2]);
        fa[1] = lane == 0 ? lds(&sm.fcnt) : lds(&sm.vote[lane]);
        fa[2] = lds(&sm.fin[lane >> 3][(lane & 7) * 4]);
        fa[3] = lds(&sm.fin[lane >> 3][25]);
        mls = (lane & 3) * 8;
        one = tid < NT && lane == 0;
        oo = (mh * DH + msl * SLC + r * 4 + g) * 2;
        asm volatile("" : "+v"(fa[0]), "+v"(fa[1]), "+v"(fa[2]), "+v"(fa[3]), "+v"(mls), "+v"(one), "+v"(oo));
    };
    unsigned d_t1 = 0, d_o[5] = {};
    if (__builtin_expect(wave < WPB, 1)) {
    const int ww = (wave & 3) * 2 + (wave >> 2);
    const int ti = ww * 4 + g;
    const int tev = ti + (ti >= 11 ? 10 : 0) + (ti >= 22 ? 11 : 0), tod = ti + 11 + (ti >= 10 ? 11 : 0) + (ti >= 21 ? 10 : 0);
    const unsigned offe = tev * (DH * 4) + r * 16, offo = tod * (DH * 4) + r * 16;
    unsigned descv[2], dsc[4];
    auto lanes = [=](unsigned d) { return (d & 1u ? offo : offe) + (d & ~0x3fffu); };
    auto fetch = [&](int t) {
        while (__builtin_amdgcn_readfirstlane(ld_wg(&sm.ready)) == 0) __builtin_amdgcn_s_sleep(1);
        asm volatile("" ::: "memory");
#pragma unroll
        for (int j = 0; j < 2; ++j) {
            descv[j] = sm.desc[t][j][lane];
            if (ww >= WPB / 2 && (descv[j] & 8u)) descv[j] ^= 3u;
        }
    };
    const char* dummy = reinterpret_cast<const char*>(qv) + (size_t)(blockIdx.x * WPB + wave) * 4864;

    v4i buf[RING];
    auto burst = [&](const char* p, int i, unsigned o) {
#pragma unroll
        for (int e = 0; e < 3; ++e) { buf[i * 3 + e] = ldstream(p + o + e * 256); SB(); }
    };

    const unsigned phase = ((pos & (NWG / 2 - 1)) * (BODY16 / 512) + ww * WST * (BODY16 / (12 * 256))) >> 16;
    unsigned base = tb + phase + PSH;
    unsigned sched = 0, off = 0, offv = 0;
    int prev = 0, now = 0, hold = 0, big = 0, body = 0;
    unsigned hist = 0;
    unsigned vc = 0;
    int d_l = 0, d_n = 0, d_f = 0, d_s = 0, d_nb = 0;
    unsigned d_bar = 0, d_x = 0;
    auto pace = [&](int due) {
        int now;
        while ((now = (int)(clk() - base)) < due) __builtin_amdgcn_s_sleep(1);
        return now;
    };
    auto follow = [&](unsigned o2) {
        hold = o2 - off > SLIP / 2 ? HOLDB : o2 != off ? HOLD : max(hold - 1, 0);
        big = o2 - off > SLIP / 2 ? HOLDB : max(big - 1, 0);
        if ((int)(o2 - off) >= SLIP) ++d_nb;
        base += o2 - off;
        off = o2;
    };
    auto pacen = [&](auto N) {
        constexpr int n = decltype(N)::value;
        follow(__builtin_amdgcn_readfirstlane(offv));
        now = pace(slot(n));
        vc = inflight();
        hist <<= 1;
        if (__builtin_expect(vc > 3, 0)) {
            hist |= 1u;
            asm volatile("s_waitcnt vmcnt(3)");
            now = (int)(clk() - base);
            if (now > slot(n) + THR && prev < PRV && big == 0 && off + SLIP <= MAXOFF && body >= NOSLK
                && (int)(base + now - __builtin_amdgcn_readfirstlane(ld_wg(&sm.quiet))) >= 0) {
                if (lane == 0 && __hip_atomic_fetch_add(&sm.vote[body], 1u, __ATOMIC_RELAXED, __HIP_MEMORY_SCOPE_WORKGROUP) + 1 >= VOTES)
                    __hip_atomic_fetch_max(&sm.slip, off + SLIP, __ATOMIC_RELAXED, __HIP_MEMORY_SCOPE_WORKGROUP);
                unsigned o2;
                while ((o2 = __builtin_amdgcn_readfirstlane(ld_wg(&sm.slip))) == off && (int)(clk() - base) < now + VWAIT) {}
                if (o2 != off) { follow(o2); now = pace(slot(n)); }
            }
            if (hold == 0 && off + SS <= MAXOFF && body >= NOSM && body % NH != 21 && __builtin_popcount(hist & DBM) >= DBN && lane == 0) st_wg(&sm.req, off + SS);
        }
        if (d_n >= BA && d_n < BB) d_l = max(d_l, now - slot(n));
        if (now - slot(n) > 30 && d_f == 0) d_f = d_n + 1;
        if (off > 0 && d_s == 0) d_s = d_n + 1;
        ++d_n;
    };
    auto check = [&](auto N) {
        constexpr int n = decltype(N)::value;
        prev = now - slot(n);
        if (lane == 0) st_wg(&sm.late[wave], (unsigned)prev);
    };
    auto getslip = [&]() { offv = __hip_atomic_load(&sm.slip, __ATOMIC_RELAXED, __HIP_MEMORY_SCOPE_WORKGROUP); };
    auto arrive = [&](int i) {
        asm volatile("" ::: "memory");
        if (lane == 0) __hip_atomic_fetch_add(&sm.cnt[i], 1u, __ATOMIC_RELAXED, __HIP_MEMORY_SCOPE_WORKGROUP);
    };
    auto next_body = [&]() {
        const unsigned s2 = sched + BODY16;
        base += (s2 >> 16) - (sched >> 16);
        sched = s2;
        ++body;
        if (DIAG == 14 && body == BA) d_bar = base - (sched >> 16) - d_nb * SLIP;
    };

    {
        v4i pv[2], pq[2];
        pq[0] = reinterpret_cast<const v4i*>(q)[tid];
        pq[1] = reinterpret_cast<const v4i*>(q)[NT + (tid & 255)];
        const unsigned vo = pg * 128 + (tid & 7) * 16;
        PST(1);
        if (PVAR == 0) pace(slot(3));
        if (PVAR == 1) until(tb + slot(3) + wave * slot(3) / 8);
        if (PVAR == 3) until(tb + (slot(3) + phase) * PFP / 100);
        SB();
        pk[3] = ldstream((const char*)kz + po);
        pk[4] = ldstream((const char*)kz + po + 128);
        pk[5] = ldstream((const char*)kz + po + 256);
        const int pl = (wave & 1) * 32 + (lane & 31);
        const bool isk = pl < 48, isz = isk ? pl >= 24 : pl >= 56;
        const char* par = (const char*)(isk ? isz ? kz : ks : isz ? vz : vs) + (isk ? pl % 24 : pl & 7) * 16
                        + group(tau * 4 + (wave >> 1), px) * (isk ? DH * 2 : 128);
        asm volatile("" : "+v"(par));
        PST(2);
        if (PVAR == 0) pace(slot(6));
        if (PVAR == 1) until(tb + slot(6) + wave * slot(1) / 8);
        if (PVAR == 3) until(tb + (slot(6) + phase * 2 / 3) * PFP / 100);
        SB();
        pv[0] = ldstream((const char*)vs + vo);
        SB();
        if (PVAR == 1) until(tb + slot(7) + wave * slot(1) / 8);
        SB();
        pv[1] = ldstream((const char*)vz + vo);
        SB();
        if (PVAR == 1) until(tb + slot(8));
        SB();
        v4i pp = __builtin_nontemporal_load((const __attribute__((address_space(1))) v4i*)par);
        dsc[0] = describe(0, 0, 0, 0u, 0u, 0u);
        dsc[1] = describe(0, 0, 1, 0u, 0u, 0u);
        PST(3); pace(slot(8) + KD); SB();
        burst((const char*)qk, 0, lanes(dsc[0]));
        PST(4);
        sm.qt[tid] = pq[0];
        if (tid < 256) sm.qt[NT + tid] = pq[1];
#pragma unroll
        for (int u = 0; u < 3; ++u) sm.ksz[(tid >> 3) * 48 + u * 8 + (tid & 7)] = pk[u];
#pragma unroll
        for (int u = 0; u < 3; ++u) {
            const v4i x = pk[3 + u];
            sm.ksz[(tid >> 3) * 48 + 24 + u * 8 + (tid & 7)] = v4i{(int)negscale(x.x), (int)negscale(x.y), (int)negscale(x.z), (int)negscale(x.w)};
        }
        PST(5); pace(slot(11) + KD); SB();
        burst((const char*)qk, 1, lanes(dsc[1]));
        PST(6);
        descv[0] = describe(0, lane, 0, 0u, 0u, 0u);
        descv[1] = describe(0, lane, 1, 0u, 0u, 0u);
        SB();
        sm.vsz[0][tid] = pv[0];
        sm.vsz[1][tid] = v4i{(int)negscale(pv[1].x), (int)negscale(pv[1].y), (int)negscale(pv[1].z), (int)negscale(pv[1].w)};
        if (isz) pp = v4i{(int)negscale(pp.x), (int)negscale(pp.y), (int)negscale(pp.z), (int)negscale(pp.w)};
        *(isk ? &sm.ksz[(64 + (wave >> 1)) * 48 + pl] : &sm.vsz[isz][(64 + (wave >> 1)) * 8 + (pl & 7)]) = pp;
    }
    asm volatile("" :: "v"(warm));
    PST(7);
    __syncthreads();
    PST(8);
    if (DST == 20) d_x = sm.dsv;
    d_bar = clk();
    base = __builtin_amdgcn_readfirstlane(sm.t0) + OFF0 + phase;

    const v2f huge = {0x1p127f, 0x1p127f};

    for (int S = 0; S < NH; ++S) {
        v2f kz2[6];
        unsigned slo[6], shi[6];
        v2u qq[3];
        const char* nsup = (const char*)(S + 1 < NH ? qk : qv);
        const int head = S >> 1 == 10 ? S : (S & ~1) + win;
        float mylogit = 0.f;
        float dot = 0.f;
        sfor<12>([&](auto N) {
            constexpr int n = decltype(N)::value;
            constexpr int i = n / 3, j = n % 3;
            if (n == 6 && (S == H0 - 1 || S == NH - 1)) fetch(S == NH - 1);
            if (j == 0) {
                const unsigned d = dsc[i ^ 2] = __builtin_amdgcn_readlane(descv[i & 1], (2 * S + 1 + (i >> 1)) & 63);
                const unsigned o = lanes(d);
                base += (d >> 4) & 31u;
                pacen(N);
                SB();
                burst(i < 2 ? (const char*)qk : nsup, i ^ 2, o);
                SB();
                if (tid == NT - 1) st_wg(&sm.tick, S * 4 + i + 1);
                check(N);
            }
            if (j == 2) getslip();
            if (i % 2 == 0 || ((dsc[i] ^ dsc[i ^ 1]) & 4u)) {
                const int it = S * 2 + (i >> 1), sl = dsc[i] & 4u ? 64 + (it & 3) : it;
                const v2u* pk = reinterpret_cast<const v2u*>(&sm.ksz[sl * 48]);
                const v2u a = pk[j * 16 + r], b = pk[48 + j * 16 + r];
                slo[j * 2 + 0] = a.x & 0xffffu; shi[j * 2 + 0] = a.x & 0xffff0000u;
                slo[j * 2 + 1] = a.y & 0xffffu; shi[j * 2 + 1] = a.y & 0xffff0000u;
                kz2[j * 2 + 0] = v2f{bf_lo(b.x), bf_hi(b.x)};
                kz2[j * 2 + 1] = v2f{bf_lo(b.y), bf_hi(b.y)};
            }
            if (i == 0) qq[j] = reinterpret_cast<const v2u*>(&sm.qt[head * 24])[j * 16 + r];
            if (j == 0) dot = 0.f;
            {
                const v4i x = buf[n % RING];
                const v2f t0 = __builtin_elementwise_fma(v2f{__int_as_float(x.x), __int_as_float(x.y)}, huge, kz2[j * 2]);
                const v2f t1 = __builtin_elementwise_fma(v2f{__int_as_float(x.z), __int_as_float(x.w)}, huge, kz2[j * 2 + 1]);
                float y[4];
                mul4(y, pack(t0.x, t0.y), pack(t1.x, t1.y), slo[j * 2], shi[j * 2], slo[j * 2 + 1], shi[j * 2 + 1]);
                dot = dot2(pack(y[0], y[1]), qq[j].x, dot);
                dot = dot2(pack(y[2], y[3]), qq[j].y, dot);
            }
            if (j == 2) {
                const float d = rowsum16(dot);
                if ((r & 3) == (i ^ ((dsc[i] >> 1) & 1))) mylogit = d;
            }
            asm volatile("" : "+v"(dot), "+v"(mylogit));
            SB();
        });
        if (r < 4) sm.p[S][wave][g * 4 + r] = (unsigned short)pack(rb(mylogit * 0x1p22f) * scale, 0.f);
        if ((S & 3) == 3) arrive(NH + (S >> 2));
        if (S == NH - 3 && tid == 0) st_wg(&sm.done, 1u);
        next_body();
    }

    for (int S = 0; S < NH; ++S) {
        v2u P;
        unsigned vsl, vsh, vzf;
        v4f acc = {0.f, 0.f, 0.f, 0.f};
        float y0[12];
        unsigned b0[12];
        const bool more = S + 1 < NH;
        unsigned sl, sh;
        v2u A[12];
        v2f z2;
        sfor<12>([&](auto N) {
            constexpr int n = decltype(N)::value;
            constexpr int i = n / 3, j = n % 3;
            if (j == 0) {
                const unsigned d = dsc[i ^ 2] = __builtin_amdgcn_readlane(descv[i & 1], (2 * S + 1 + (i >> 1)) & 63);
                unsigned o = lanes(d);
                const char* src = (const char*)qv;
                if (i >= 2 && !more) {
                    o = DUM ? 0 : i * 768;
                    src = !DUM ? dummy : i == 3 && wave == 0 ? (const char*)out + min(mh, NH - 2) * (DH * 2) : (const char*)ws + (i & 1) * (15 << 18) + blockIdx.x * 768;
                }
                if (i < 2 || more) base += (d >> 4) & 31u;
                if (i < 2 || more) pacen(N);
                SB();
                burst(src, i ^ 2, o);
                SB();
                if (tid == NT - 1) st_wg(&sm.tick, 4 * NH + S * 4 + i + 1);
                check(N);
            }
            if (n == 0) {
                const unsigned need = S < 2 ? 1 : S;
                while (__builtin_amdgcn_readfirstlane(ld_wg(&sm.prog)) < need) {}
                asm volatile("" ::: "memory");
                P = *reinterpret_cast<const v2u*>(&sm.p[S][wave][g * 4]);
                P.x = __builtin_amdgcn_alignbit(P.x, P.x, (dsc[0] & 2u) << 3);
                P.y = __builtin_amdgcn_alignbit(P.y, P.y, (dsc[2] & 2u) << 3);
                const unsigned d3 = __builtin_amdgcn_readlane(descv[1], 2 * S + 1);
                const unsigned x = (((dsc[0] & 7u) | (dsc[1] & 7u) << 8) | ((dsc[2] & 7u) << 16 | (d3 & 7u) << 24)) >> (r & 3) * 8;
                const int it = S * 2 + ((r >> 1) & 1);
                const int ltok = (x & 4u ? 64 + (it & 3) : it) * 64 + (x & 1u ? tod : tev);
                vsl = reinterpret_cast<const unsigned short*>(&sm.vsz[0][0])[ltok];
                vsh = vsl << 16;
                vzf = (unsigned)reinterpret_cast<const unsigned short*>(&sm.vsz[1][0])[ltok] << 16;
            }
            if (j == 2) getslip();
            if (j == 0) {
                sl = dppu<0x55 * i>(vsl);
                sh = dppu<0x55 * i>(vsh);
                const float zb = __uint_as_float(dppu<0x55 * i>(vzf));
                z2 = v2f{zb, zb};
            }
            float y[4];
            {
                const v4i x = buf[n];
                const v2f t0 = __builtin_elementwise_fma(v2f{__int_as_float(x.x), __int_as_float(x.y)}, huge, z2);
                const v2f t1 = __builtin_elementwise_fma(v2f{__int_as_float(x.z), __int_as_float(x.w)}, huge, z2);
                mul4(y, pack(t0.x, t0.y), pack(t1.x, t1.y), sl, sh, sl, sh);
            }
#pragma unroll
            for (int e = 0; e < 4; ++e) {
                const int idx = j * 4 + e;
                if (i % 2 == 0) {
                    y0[idx] = y[e];
                    asm volatile("" : "+v"(y0[idx]));
                    if (i == 2) {
                        A[idx] = v2u{r == idx ? P.x : 0u, r == idx ? P.y : 0u};
                        if (AH) asm volatile("" : "+v"(A[idx]));
                    }
                } else if (i == 1) {
                    b0[idx] = pack(y0[idx], y[e]);
                    asm volatile("" : "+v"(b0[idx]));
                } else {
                    const v2u B = {b0[idx], pack(y0[idx], y[e])};
                    acc = __builtin_amdgcn_mfma_f32_16x16x16bf16_1k(__builtin_bit_cast(v4s, A[idx]), __builtin_bit_cast(v4s, B), acc, 0, 0, 0);
                }
            }
            if (n == 11) {
                if (S == NH - 2 && tid == 0) sm.tref = base + slot(12) + slot(3);
                if (S == NH - 3 && tid == 0) st_wg(&sm.done, 2u);
                if (lane < 48) *reinterpret_cast<v4f*>(&sm.stage[S & 1][wave][lane * 4]) = acc;
                arrive(S);
            }
            SB();
        });
        next_body();
    }
    if (DST >= 21) d_x = sm.dsv;
    d_t1 = clk();
    d_o[0] = off; d_o[1] = DST ? d_x : d_l; d_o[2] = d_f; d_o[3] = d_bar - phase; d_o[4] = DIAG >= 12 ? d_nb : d_s;
    {
        const unsigned tag = tagof(sm.epoch, lane & 3);
        const float* src = ws + WS_R + ((size_t)((both ? 0 : mh & 1) * (NWG / 2) + wave * 16) * NRC + (mh >> 1) + (mh > 20)) * RS + msl * 16;
        const unsigned so = (lane >> 2) * (NRC * RS * 4) + (lane & 3) * 16;
        prepare();
        until(base - slot(12) + slot(3) - phase + (wave - (WPB - 1)) * STG + (mh >= NH - 2 ? TPL : both ? TPC - TPB : TPC));
        for (;;) {
            x[0] = ld_rec(src, so);
            x[1] = v4i{0, 0, 0, (int)tag};
            if (both) x[1] = ld_rec(src + NWG / 2 * NRC * RS, so);
            asm volatile("s_waitcnt vmcnt(0)" : "+v"(x[0]), "+v"(x[1]));
            if (__all((((unsigned)x[0].w ^ tag) | ((unsigned)x[1].w ^ tag)) < 256u) || clk() - tb >= TMO) break;
            __builtin_amdgcn_s_sleep(PSL);
        }
#pragma unroll
        for (int d = 5; d >= 0; --d) asm volatile("" :: "v"(buf[d]));
        x[0].w ^= tag;
        x[1].w ^= tag;
    }
    } else
    if (wave == WPB) {
        const int l = lane;
        u64* echo = tstart + NWG;
        u64* gofs = tstart + 3 * NWG;
        if (l == 0) {
            if (blockIdx.x < NR) st_dev(gofs + blockIdx.x * 32, tb64 - 100000ull);
            if (blockIdx.x == NR) st_dev(gofs + 16, tb64 - 100000ull);
            sm.slip = 0;
            sm.req = 0;
            sm.done = 0;
            sm.prog = 0;
            sm.ready = 0;
            sm.tick = 0;
            sm.quiet = tb;
            sm.fcnt = 0;
            sm.epoch = tb64;
            sm.have = blockIdx.x == 0;
        }
        if (l < NH + NH / 4) sm.cnt[l] = 0;
        sm.vote[l] = 0;
        until(tb + TGATH);
        unsigned ld;
        for (;;) {
            const u64 dt = ld_dev(tstart + NWG - 8 + (l & 7)) - tb64 + 1000ull;
            ld = dt < 2000ull ? (unsigned)dt : 0u;
            if (__all(dt < 2000ull) || clk() - tb > (unsigned)TGMAX) break;
        }
        unsigned sum = ld + dppu<0x124>(ld);
        sum += dppu<0x122>(sum);
        sum += dppu<0x121>(sum);
        const bool allv = __all(ld != 0);
        ld = max(ld, dppu<0x124>(ld));
        ld = max(ld, dppu<0x122>(ld));
        ld = max(ld, dppu<0x121>(ld));
        if (MEANF && allv) ld -= (ld - sum / 8) * MEANF / 4;
        ld = __builtin_amdgcn_readfirstlane(ld);
        const u64 t64 = ld ? tb64 + ld - 1000ull : tb64;
        if (l == 0) sm.t0 = (unsigned)t64;
        if (l == 0) sm.dsv = clk() - tb;
        u64 to[NWG / 64];
        bool have = blockIdx.x == 0;
        auto chores = [&](bool all) {
            if (blockIdx.x == 0) {
                u64 x[NWG / 64];
#pragma unroll
                for (int u = 0; u < NWG / 64; ++u) x[u] = ld_dev(tstart + u * 64 + l);
#pragma unroll
                for (int u = 0; u < NWG / 64; ++u) {
                    if (all || __any(x[u] != to[u])) st_rec(echo + u * 128, l * 16, v4i{(int)(unsigned)x[u], (int)(unsigned)(x[u] >> 32), (int)tb, (int)(unsigned)(tb64 >> 32)});
                    to[u] = x[u];
                }
            } else if (!have && !all) {
                v4i x = ld_rec(echo + blockIdx.x * 2, 0);
                asm volatile("s_waitcnt vmcnt(0)" : "+v"(x));
                if ((unsigned)x.x == tb && (unsigned)x.y == (unsigned)(tb64 >> 32)) {
                    have = true;
                    if (l == 0) sm.epoch = (u64)(unsigned)x.w << 32 | (unsigned)x.z;
                    asm volatile("s_waitcnt lgkmcnt(0)" ::: "memory");
                    if (l == 0) st_wg(&sm.have, 1u);
                }
            }
        };
        if (EARLY) chores(true);
        asm volatile("s_waitcnt lgkmcnt(0)\n\ts_barrier" ::: "memory");
        asm volatile("" :: "v"(warm));
        if (!EARLY) chores(true);
        if (!ETCH) {
            until(tb + TECHO);
            behind();
            chores(false);
        }
        const u64* mine = NR > 1 ? gofs + ((blockIdx.x >> 3) % NR) * 32 : gofs + 16;
        unsigned sent = 0, stage = 0, tsh = clk();
        int nt = blockIdx.x < 200, spin = 0;
        while (stage < 2) {
            if (PB > 1 && have) __builtin_amdgcn_s_sleep(PSLEEP);
            behind();
            unsigned tch = 0;
            if (nt < 25) {
                const unsigned pg = (blockIdx.x >> 3) + 32 * (nt % 12);
                const char* tp = (nt < 12 ? (const char*)qk : nt < 24 ? (const char*)qv : (const char*)ws) + ((size_t)pg << 21);
                const bool first = PTL && (pg == 0 || (((size_t)tp >> 21) & (PTL - 1)) == 0);
                tp += (((first ? blockIdx.x >> 3 : blockIdx.x) * 29 + nt) & 255) * 256;
                if (nt == 24 && blockIdx.x >= 16) tp = (const char*)out + min((pg - 2) * 4096, NH * DH * 2 - 4u);
                if (ld_wg(&sm.tick) + (first ? TLEAD + 4 : TLEAD) >= (unsigned)max(nt / 12 * 128 + nt % 12 * 4 / 3 * 8 - nt / 24 * 32, TLEAD + TF0)) {
                    unsigned x;
                    if (nt < 24 && !first) { if (VT) asm volatile("s_nop 4\n\tglobal_load_dword %0, %1, %2 nt" : "+v"(tch) : "v"(0), "s"(tp) : "memory"); }
                    else if (nt < 24 || blockIdx.x < 48) asm volatile("s_load_dword %0, %1, 0x0\n\ts_waitcnt lgkmcnt(0)" : "=s"(x) : "s"(tp) : "memory");
                    ++nt;
                }
            }
            const u64 gd = ld_dev(mine) - t64;
            asm volatile("s_waitcnt vmcnt(0)" : "+v"(tch));
            const unsigned go = gd <= (u64)MAXOFF ? (unsigned)gd : 0u;
            const unsigned own = ld_wg(&sm.slip);
            if (go > own && l == 0) __hip_atomic_fetch_max(&sm.slip, go, __ATOMIC_RELAXED, __HIP_MEMORY_SCOPE_WORKGROUP);
            if (own <= go) tsh = clk();
            else if (clk() - tsh > (unsigned)REVT && blockIdx.x >= NSENT) { if (l == 0) st_wg(&sm.slip, go); }
            unsigned req = ld_wg(&sm.req);
            if ((int)(clk() - ld_wg(&sm.quiet)) < 0) { req = 0; if (l == 0) st_wg(&sm.req, 0u); }
            unsigned want = blockIdx.x < NSENT ? own : 0u;
            if (req == go + SS && own == go) {
                unsigned x = ld_wg(&sm.late[l & 7]);
                x += dppu<0x121>(x);
                x += dppu<0x122>(x);
                x += dppu<0x124>(x);
                want = min(req + (__builtin_amdgcn_readfirstlane(x) * LFAC / 64 & ~15u), (unsigned)MAXOFF);
            }
            if (want > go && want > sent) {
                u64 old = 0;
                behind();
                if (l == 0) st_wg(&sm.quiet, clk() + TQA);
                if (l == 0) old = __hip_atomic_fetch_max(gofs + 16, t64 + want, __ATOMIC_RELAXED, __HIP_MEMORY_SCOPE_AGENT);
                if (NR > 1 && __builtin_amdgcn_readfirstlane((unsigned)(old < t64 + want)) && l < NR)
                    __hip_atomic_fetch_max(gofs + l * 32, t64 + want, __ATOMIC_RELAXED, __HIP_MEMORY_SCOPE_AGENT);
                sent = want;
            }
            if (have ? blockIdx.x == 0 && ++spin % 8 == 0 : clk() - tb >= (unsigned)(blockIdx.x - CAL0 < 64u ? TECHO : TECHO2)) { behind(); chores(false); }
            if (ld_wg(&sm.done) > stage) {
                ++stage;
                behind();
                asm volatile("" : "+s"(code));
                int w = 0;
#pragma unroll
                for (int u = 0; u < 4; ++u) {
                    const int a = (u * 64 + l) * 128;
                    w ^= *(gptr)(code + (a < codelen ? a : 0));
                }
                asm volatile("" :: "v"(w));
            }
        }
        while (!have && clk() - tb < TMO) chores(false);
        if (l == 0) st_wg(&sm.have, 1u);
        if (blockIdx.x == 0) {
            for (spin = 1; ld_wg(&sm.fcnt) < WPB; ++spin) {
                __builtin_amdgcn_s_sleep(2);
                if (spin % 1024 == 0) chores(false);
            }
            return;
        }
        if (!DRY) return;
        prepare();
    } else {
        u64* table = tstart + 3 * NWG + NR * 32;
        const unsigned ent = blockIdx.x - CAL0;
        int lead = 0, lat = 0;
        if (PRE) {
            v4i y[PRE ? PRE : 1];
#pragma unroll
            for (int u = 0; u < PRE; ++u) y[u] = *(const v4i*)((const char*)(ws + WS_R) + (pos * NRC + NRC - 1 - (PRE > 1 ? u * 2 + (lane >> 5) : 0)) * (RS * 4) + (lane & 31) * 16);
#pragma unroll
            for (int u = 0; u < PRE; ++u) asm volatile("" :: "v"(y[u]));
        }
        __syncthreads();
        if (ent < 64 && (ent & 31) < 24 && (ent & 3) < NMEM) {
            const unsigned t0 = __builtin_amdgcn_readfirstlane(sm.t0);
            unsigned page, x[4];
            auto pair = [&](unsigned k) {
                const unsigned h = (t0 + k + (ent & ~3u) * 2) * 0x9e3779b1u;
                page = (ent & 28) * 16 + (k & 1) * 32 + 24 + ((t0 + (k & 1)) * 0x85ebca6bu >> 29);
                return (const char*)(ent < 32 ? +qk : +qv) + ((size_t)page << 21) + (h >> 25) * 16384 + ((h >> 8) & 127) * 64;
            };
            asm volatile("s_load_dword %0, %4, 0x0\n\ts_load_dword %1, %4, 0x2000\n\ts_load_dword %2, %5, 0x0\n\t"
                         "s_load_dword %3, %5, 0x2000\n\ts_waitcnt lgkmcnt(0)"
                         : "=&s"(x[0]), "=&s"(x[1]), "=&s"(x[2]), "=&s"(x[3]) : "s"(pair(ent % 4 * 2)), "s"(pair(ent % 4 * 2 + 1)) : "memory");
            until(t0 + TPRB);
#pragma nounroll
            for (unsigned k = 2; k < 2 * NMEM; ++k) {
                u64 ta, tc, td;
                asm volatile("s_memtime %0\n\ts_waitcnt lgkmcnt(0)\n\ts_load_dword %3, %4, 0x0\n\ts_waitcnt lgkmcnt(0)\n\t"
                             "s_memtime %1\n\ts_waitcnt lgkmcnt(0)\n\ts_load_dword %3, %4, 0x2000\n\ts_waitcnt lgkmcnt(0)\n\t"
                             "s_memtime %2\n\ts_waitcnt lgkmcnt(0)"
                             : "=&s"(ta), "=&s"(tc), "=&s"(td), "=&s"(x[0]) : "s"(pair((ent % 4 * 2 + k) % (2 * NMEM))) : "memory");
                const int d0 = (int)(tc - ta), d1 = (int)(td - tc), d = max(-PCL, min(PCL, d1 - d0));
                if (min(d0, d1) > 350 && d0 + d1 < 1700) lead += (page ^ (page >> 7)) & 1 ? -d : d;
                lat += d0 + d1;
            }
        }
        asm volatile("" :: "v"(warm));
        while (ld_wg(&sm.have) == 0 && clk() - tb < TMO) __builtin_amdgcn_s_sleep(4);
        asm volatile("" ::: "memory");
        const u64 E = sm.epoch;
        if (ent < 64 && (ent & 31) < 24 && (ent & 3) < NMEM) {
            unsigned xcc;
            asm volatile("s_getreg_b32 %0, hwreg(HW_REG_XCC_ID, 0, 3)" : "=s"(xcc));
            if (xcc & 4) lead = -lead;
            behind();
            if (lane == 0) st_wg(&sm.quiet, clk() + TQ);
            if (lane < 8) st_rec(table + ent * 32, lane * 16, v4i{lead, (int)(xcc - blockIdx.x), (int)(unsigned)E, (int)(unsigned)(E >> 32)});
        }
        unsigned jobs = 0;
        v4i cal;
        until(tb + TTAB);
        for (;;) {
            behind();
            cal = ld_rec(table, ((lane & 31) < 24 && (lane & 3) < NMEM ? lane : 0) * 256);
            asm volatile("s_waitcnt vmcnt(0)" : "+v"(cal));
            if (__all((unsigned)cal.z == (unsigned)E && (unsigned)cal.w == (unsigned)(E >> 32)) || clk() - tb >= TMO) break;
        }
        {
            int sum = (lane & 3) < NMEM ? cal.x : 0;
            sum += (int)dppu<0xB1>(sum);
            sum += (int)dppu<0x4E>(sum);
            const u64 pol = __ballot(sum < 0) ^ FPOL, ok = __ballot(abs(sum) >= PTH) & FMASK;
            const unsigned side = ((unsigned)__builtin_amdgcn_readfirstlane(cal.y) + (blockIdx.x & ~4u)) >> 2;
#pragma unroll
            for (int u = 0; u < 4; ++u) {
                sm.desc[u >> 1][u & 1][lane] = describe(u >> 1, lane, u & 1, pol, ok, side);
                jobs |= (tau > 0 ? job(u >> 1, 2 * tau + (u & 1), pol, ok, side) : pos & 1u) << u;
            }
            asm volatile("" ::: "memory");
            if (lane == 0 && DST == 21) sm.dsv = clk() - tb;
            if (lane == 0 && DST == 22) sm.dsv = (12 - __popcll(ok & 0x11111100111111ull)) * 10;
            if (lane == 0) st_wg(&sm.ready, 1u);
        }
#pragma nounroll
        for (int cc = 0; cc <= NH / 4; ++cc) {
            const int c = cc < NH / 4 ? cc : tau >> 1;
            const bool act = (tau > 0 && c * 2 + (g >> 1) == tau) == (cc == NH / 4);
            while (ld_wg(&sm.cnt[NH + c]) < WPB) __builtin_amdgcn_s_sleep(2);
            asm volatile("" ::: "memory");
            if (cc >= NH / 4 - 1) {
                const bool sw = ((jobs ^ (jobs >> 2)) >> (lane >> 5)) & 1u;
                v2u* mine = reinterpret_cast<v2u*>(&sm.p[2 * tau][0][0]) + lane;
                if (cc < NH / 4) {
                    if (lane == 0) st_wg(&sm.quiet, clk() + TXW + TQ);
                    if (sw) st_rec(ws + WS_X + pos * 256, lane * 16, v4i{(int)mine->x, (int)mine->y, (int)(unsigned)E, (int)(unsigned)(E >> 32)});
                } else if (__any(sw)) {
                    until(clk() + TXW);
                }
                while (cc == NH / 4 && __any(sw)) {
                    v4i x = {};
                    if (sw) asm volatile("s_nop 4\n\tglobal_load_dwordx4 %0, %1, %2 sc1 nt" : "+v"(x) : "v"(lane * 16), "s"(ws + WS_X + (pos ^ 1) * 256) : "memory");
                    asm volatile("s_waitcnt vmcnt(0)" : "+v"(x));
                    if (__all(!sw || ((unsigned)x.z == (unsigned)E && (unsigned)x.w == (unsigned)(E >> 32))) || clk() - tb >= TMO) {
                        if (sw) *mine = v2u{(unsigned)x.x, (unsigned)x.y};
                        break;
                    }
                    __builtin_amdgcn_s_sleep(8);
                }
                asm volatile("" ::: "memory");
            }
            const bool two = c * 2 + (g >> 1) != 10;
            v4i* pp = reinterpret_cast<v4i*>(&sm.p[c * 4][0][0]) + lane;
            const v4i u = *pp;
            float x[8];
#pragma unroll
            for (int e = 0; e < 4; ++e) { x[2 * e] = bf_lo(u[e]); x[2 * e + 1] = bf_hi(u[e]); }
            float M = fmaxf(fmaxf(fmaxf(x[0], x[1]), fmaxf(x[2], x[3])), fmaxf(fmaxf(x[4], x[5]), fmaxf(x[6], x[7])));
            M = fmaxf(M, dpp<0x128>(M));
            M = fmaxf(M, dpp<0x124>(M));
            M = fmaxf(M, dpp<0x122>(M));
            M = fmaxf(M, dpp<0x121>(M));
            {
                auto y = __builtin_amdgcn_permlane16_swap(__float_as_uint(M), __float_as_uint(M), false, false);
                if (two) M = fmaxf(__uint_as_float(y[0]), __uint_as_float(y[1]));
            }
            v4i o;
            float L = 0.f;
#pragma unroll
            for (int e = 0; e < 4; ++e) {
                const unsigned pk = pack(__expf(x[2 * e] - M), __expf(x[2 * e + 1] - M));
                o[e] = (int)pk;
                L += bf_lo(pk) + bf_hi(pk);
            }
            if (act) *pp = o;
            L = rowsum16(L);
            const float L2 = fold16(L, L);
            if (two) L = L2;
            if (r == 0 && act) sm.ml[c * 4 + g] = __float_as_uint(M) | __builtin_bit_cast(unsigned short, (_Float16)L);
            asm volatile("" ::: "memory");
            if (lane == 0) st_wg(&sm.prog, 1u);
        }
        const unsigned tag = tagof(E, lane & 3);
        auto place = [&](int i) {
            const int i0 = tau + (tau > 10);
            const unsigned jb = i == i0 ? jobs >> 2 : i == 11 && tau == 10 ? jobs >> 3 : pos;
            return (((pos & ~1) | (jb & 1)) * NRC + i) * (RS * 4) + (lane & 31) * 16;
        };
        const unsigned last = place(NRC - 1);
        v2f keep[3] = {};
#pragma nounroll
        for (int h = 0; h < NH; ++h) {
            const unsigned mlb = (sm.ml[h] >> (lane & 3) * 8) & 0xffu;
            if (h < NH - 1) { while (ld_wg(&sm.cnt[h]) < WPB) __builtin_amdgcn_s_sleep(4); }
            else { while (ld_wg(&sm.cnt[h]) < WPB) {} }
            asm volatile("" ::: "memory");
            v2f c[4][3];
#pragma unroll
            for (int e = 0; e < 4; ++e)
#pragma unroll
                for (int k = 0; k < 3; ++k) c[e][k] = *reinterpret_cast<const v2f*>(&sm.stage[h & 1][(lane >> 5) * 4 + e][(lane & 31) * 6 + k * 2]);
            v4i x;
            const bool first = (h & 1) == 0 && h != 20;
#pragma unroll
            for (int k = 0; k < 3; ++k) {
                const v2f a = (keep[k] + (c[0][k] + c[1][k])) + (c[2][k] + c[3][k]), b = a * 0x1p22f;
                x[k] = (int)pack(fold32(b.x, b.x), fold32(b.y, b.y));
                keep[k] = first ? a : v2f{0.f, 0.f};
            }
            x[3] = (int)(tag | mlb);
            if (h == NH - 1) {
                if (lane < 32) {
                    if (BNT & 2) asm volatile("s_nop 4\n\tglobal_store_dwordx4 %0, %1, %2 sc1 nt\n\ts_nop 1" :: "v"(last), "v"(x), "s"(ws + WS_R) : "memory");
                    else st_rec(ws + WS_R, last, x);
                }
                break;
            }
            if (!first) sm.rec[(h >> 1) + (h > 20)][lane & 31] = x;
            asm volatile("" ::: "memory");
            if (lane == 0) st_wg(&sm.prog, h + 2);
            if (h == NH - 2) {
                v4i y[NRC / 2];
                unsigned o[NRC / 2];
#pragma unroll
                for (int u = 0; u < NRC / 2; ++u) { y[u] = (&sm.rec[0][0])[u * 64 + lane]; o[u] = place(u * 2 + (lane >> 5)); }
                until(ld_wg(&sm.tref) + TREL);
#pragma unroll
                for (int u = 0; u < NRC / 2; ++u) {
                    if (BNT & 1) asm volatile("s_nop 4\n\tglobal_store_dwordx4 %0, %1, %2 sc1 nt\n\ts_nop 1" :: "v"(o[u]), "v"(y[u]), "s"(ws + WS_R) : "memory");
                    else st_rec(ws + WS_R, o[u], y[u]);
                }
            }
        }
        return;
    }

    {
        asm volatile("" : "+v"(x[0]), "+v"(x[1]));
        auto gather = [&](unsigned w) {
            w <<= mls;
            w |= dppu<0xB1>(w);
            return w | dppu<0x4E>(w);
        };
        auto half = [](unsigned u) { return (float)__builtin_bit_cast(_Float16, (unsigned short)u); };
        const unsigned ml = gather(x[0].w);
        float m = bf_hi(ml), l = half(ml), a[6];
#pragma unroll
        for (int e = 0; e < 3; ++e) { a[2 * e] = bf_lo(x[0][e]); a[2 * e + 1] = bf_hi(x[0][e]); }
        if (both) {
            const unsigned m2 = gather(x[1].w);
            const float mx = fmaxf(m, bf_hi(m2)), w0 = __expf(m - mx), w1 = __expf(bf_hi(m2) - mx);
#pragma unroll
            for (int e = 0; e < 3; ++e) {
                a[2 * e] = a[2 * e] * w0 + bf_lo(x[1][e]) * w1;
                a[2 * e + 1] = a[2 * e + 1] * w0 + bf_hi(x[1][e]) * w1;
            }
            l = l * w0 + half(m2) * w1;
            m = mx;
        }
        const float M = x4max(m), w0 = __expf(m - M);
        l *= w0;
        float c0 = fold16(fold32(a[0] * w0, a[4] * w0), fold32(a[2] * w0, l));
        float c1 = fold16(fold32(a[1] * w0, a[5] * w0), fold32(a[3] * w0, l));
        c0 += dpp<0x128>(c0); c1 += dpp<0x128>(c1);
        c0 += dpp<0x124>(c0); c1 += dpp<0x124>(c1);
        if (g == 3) c1 = M;
        unsigned arr;
        v4f y;
        float mw;
        asm volatile("ds_write_b64 %3, %4\n\tds_add_rtn_u32 %0, %5, %6\n\tds_read_b32 %2, %8\n\tds_read_b128 %1, %7\n\ts_waitcnt lgkmcnt(0)"
                     : "=&v"(arr), "=&v"(y), "=&v"(mw) : "v"(fa[0]), "v"(v2f{c0, c1}), "v"(fa[1]), "v"(one), "v"(fa[2]), "v"(fa[3]) : "memory");
        const float w = __expf(mw - x4max(mw));
        float d = fold16(fold32(y.x * w, y.z * w), fold32(y.y * w, y.w * w));
        d += dpp<0x128>(d);
        const float L = __int_as_float(__builtin_amdgcn_readlane(__float_as_int(d), 6));
        const unsigned o = pack(d * __builtin_amdgcn_rcpf(L), 0.f);
        if (__builtin_amdgcn_readfirstlane(arr) == WPB - 1 && wave < WPB) {
            if (r < 6) *(unsigned short*)((char*)out + oo) = (unsigned short)o;
        }
    }
    if (wave < WPB) {
        unsigned* dg = reinterpret_cast<unsigned*>(ws + WS_T + 655360) + (blockIdx.x * WPB + wave) * 8;
        if (lane == 0) { dg[0] = (unsigned)T0; dg[1] = d_t1; dg[2] = clk(); for (int i = 0; i < 5; ++i) dg[3 + i] = d_o[i]; }
    }
}
__global__ void kivi_end_marker() { asm volatile("kivi_code_end:"); }

__global__ void diag_kernel(const float* ws) {
    const unsigned* dg = reinterpret_cast<const unsigned*>(ws + WS_T + 655360);
    const unsigned ref = dg[0];
    int mn = 1 << 30, x = -(1 << 30), cnt = 0;
    for (int b = 0; b < 8; ++b) {
        const unsigned* e = dg + (b * 256 + threadIdx.x) * 8;
        mn = min(mn, (int)(e[0] - ref));
        const int v = DIAG == 2 ? (int)(e[0] - ref) : DIAG == 3 ? (int)e[3] : DIAG == 4 ? (int)(e[1] - ref - e[3]) : DIAG == 5 ? (int)(e[2] - ref)
                    : DIAG == 12 ? (int)(e[2] - ref - e[7] * SLIP) : DIAG == 13 ? (int)(e[3] - e[7] * SLIP) : DIAG == 6 ? (int)e[4] : DIAG == 7 || DIAG == 14 ? (int)(e[6] - ref) : DIAG == 8 ? (e[5] ? 1000 - (int)e[5] : 0) : DIAG == 9 ? (e[7] ? 1000 - (int)e[7] : 0) : 0;
        x = max(x, v);
        cnt += DIAG == 11 ? (int)e[4] : (int)e[4] > 30;
    }
    __shared__ int smn, smx, sc;
    if (threadIdx.x == 0) { smn = 1 << 30; smx = -(1 << 30); sc = 0; }
    __syncthreads();
    atomicMin(&smn, mn); atomicMax(&smx, x); atomicAdd(&sc, cnt);
    __syncthreads();
    mn = smn; x = smx;
    int X = 0;
    if (DIAG == 2 || DIAG == 7 || DIAG == 14) X = x - mn;
    if (DIAG == 3 || DIAG == 6 || DIAG == 8 || DIAG == 9) X = x;
    if (DIAG == 4 || DIAG == 5 || DIAG == 12) X = x - mn - 20000;
    if (DIAG == 13) X = x;
    if (DIAG == 10) X = sc;
    if (DIAG == 11) X = sc / 2048;
    X = max(0, min(X, 3000));
    while ((int)(clk() - ref) < mn + 25000 + X * 10) __builtin_amdgcn_s_sleep(1);
}
extern "C" void kernel_launch(void* const* d_in, const int* in_sizes, int n_in,
                              void* d_out, int out_size, void* d_ws, size_t ws_size,
                              hipStream_t stream) {
    const __hip_bfloat16* q = (const __hip_bfloat16*)d_in[0];
    const int* qk = (const int*)d_in[1];
    const int* qv = (const int*)d_in[2];
    const __hip_bfloat16* ks = (const __hip_bfloat16*)d_in[3];
    const __hip_bfloat16* kz = (const __hip_bfloat16*)d_in[4];
    const __hip_bfloat16* vs = (const __hip_bfloat16*)d_in[5];
    const __hip_bfloat16* vz = (const __hip_bfloat16*)d_in[6];
    const float scale = (float)(1.0 / sqrt((double)DH));
    Order order;
    for (int b = 0; b < NWG / 2; ++b) {
        const int p = position(b);
        order.g[b] = groupof(p) | groupof(p + 128) << 8 | groupof(p ^ 1) << 16 | groupof((p ^ 1) + 128) << 24;
    }
    kivi_stream_kernel<<<NWG, NT + 128, 0, stream>>>(q, qk, qv, ks, kz, vs, vz, (float*)d_ws, (__hip_bfloat16*)d_out, scale, order);
    diag_kernel<<<1, 256, 0, stream>>>((const float*)d_ws);
}
```

```cpp
#define DIAG 14
#include <hip/hip_runtime.h>
#include <hip/hip_bf16.h>
#include <stdint.h>
#include <utility>

#define TGT 199.25
#define WST 16
#define THR 80
#define PRV 16
#define SLIP 250
#define MAXOFF 3000
#define NSENT 8
#define NR 32
#define TPL 265
#define STG 4
#define PSLEEP 16
#define OFF0 392
#define TGATH 150
#define GAP1 10
#define TREL 110
#define NSLOW 1
#define EXK 7
#define NOSLK 2
#define TMO 100000000u
#define TPC 265
#define PSL 2
#define DST 0
#define BA 8
#define BB 16
#define TGMAX 300
#define PSH 0
#define SS 20
#define HOLD 4
#define VOTES 4
#define HOLDB 8
#define REVT 600
#define VWAIT 30
#define LFAC 4
#define NOSM 2
#define H0 5
#define TGS 203.0
#define SPL 2
#define G21 5
#define EXS(i) ((int)(((i) + 1) * (TGS - TGT) * 100.0 / 256.0 + 0.5) - (int)((i) * (TGS - TGT) * 100.0 / 256.0 + 0.5))
#define CAL0 128
#define PTH (NMEM * (NMEM - 1) * 150)
#define NMEM 2
#define PCL 500
#define TPRB 580
#define DBM 7
#define DBN 2
#define TQA 500
#define PB 1
#define PTL 8
#define EARLY 0
#define LATEB 20
#define MEANF 0
#define PF 100
#define DRY 0
#define TPB 20
#define PRE 0
#define BNT 0
#define PVAR 0
#define PFP 50
#define KD 0
#define ETCH 1
#define TF0 2
#define VT 0
#define TECHO2 900
#define CNT 0
#define QNT 0
#define FT 1
#define TBL 1
#define SPOLL 0
#define THRO 1
#define AH 1
#define DUM 1
#define TLEAD 14
#define GAP2 10
#define NG2 3
#define TECHO 650
#define TQ 300
#define TTAB 1300
#define TXW 200
#define FMASK ~0ull
#define FPOL 0ull
#define RING 12
#define SB() __builtin_amdgcn_sched_barrier(0)
#define PST(i) SB()
#undef PST
#define PST(i) do { SB(); if (DST == i + 1) d_x = clk() - base; SB(); } while (0)

typedef int v4i __attribute__((ext_vector_type(4)));
typedef unsigned v2u __attribute__((ext_vector_type(2)));
typedef float v2f __attribute__((ext_vector_type(2)));
typedef float v4f __attribute__((ext_vector_type(4)));
typedef short v4s __attribute__((ext_vector_type(4)));
typedef __bf16 v2bf __attribute__((ext_vector_type(2)));

constexpr int DH = 192;
constexpr int NH = 32;
constexpr int SKV = 32768;
constexpr int WPB = 8;
constexpr int NT = WPB * 64;
constexpr int NWG = 256;
constexpr int NSL = 8, SLC = 24;
constexpr int RS = NSL * 16;
static_assert(SKV == 2 * 64 * NWG, "two 64-token groups per workgroup and head");
constexpr int NX = 4;
constexpr int NRC = 17;
constexpr size_t WS_R = 262144;
constexpr size_t WS_X = WS_R + (size_t)NRC * NWG * RS;
constexpr size_t WS_T = WS_X + (size_t)NWG * 256;
constexpr unsigned BODY16 = (unsigned)(TGT * 100.0 / 64.0 * 65536.0);
constexpr int slot(int n) { return (int)((n * (BODY16 / 12)) >> 16); }

template <typename F, int... I>
__device__ __forceinline__ void sfor_impl(F&& f, std::integer_sequence<int, I...>) {
    (f(std::integral_constant<int, I>{}), ...);
}
template <int N, typename F>
__device__ __forceinline__ void sfor(F&& f) {
    sfor_impl(f, std::make_integer_sequence<int, N>{});
}

__device__ __forceinline__ float bf_lo(unsigned u) { return __uint_as_float(u << 16); }
__device__ __forceinline__ float bf_hi(unsigned u) { return __uint_as_float(u & 0xffff0000u); }
__device__ __forceinline__ float rb(float x) { return (float)(__bf16)x; }
__device__ __forceinline__ unsigned pack(float lo, float hi) {
    return __builtin_bit_cast(unsigned, __builtin_convertvector(v2f{lo, hi}, v2bf));
}
__device__ __forceinline__ float dot2(unsigned a, unsigned b, float c) {
    return __builtin_amdgcn_fdot2_f32_bf16(__builtin_bit_cast(v2bf, a), __builtin_bit_cast(v2bf, b), c, false);
}
__device__ __forceinline__ void mul4(float (&y)[4], unsigned a, unsigned b, unsigned s0, unsigned s1, unsigned s2, unsigned s3) {
    asm("v_dot2_f32_bf16 %0, %4, %6, 0\n\tv_dot2_f32_bf16 %1, %4, %7, 0\n\t"
        "v_dot2_f32_bf16 %2, %5, %8, 0\n\tv_dot2_f32_bf16 %3, %5, %9, 0\n\ts_nop 2"
        : "=&v"(y[0]), "=&v"(y[1]), "=&v"(y[2]), "=&v"(y[3])
        : "v"(a), "v"(b), "v"(s0), "v"(s1), "v"(s2), "v"(s3));
}
__device__ __forceinline__ unsigned clk() { return (unsigned)__builtin_amdgcn_s_memrealtime(); }
__device__ __forceinline__ unsigned inflight() {
    unsigned x;
    asm volatile("s_getreg_b32 %0, hwreg(HW_REG_IB_STS, 0, 4)" : "=s"(x));
    return x;
}
template <int CTRL>
__device__ __forceinline__ unsigned dppu(unsigned x) {
    return (unsigned)__builtin_amdgcn_mov_dpp((int)x, CTRL, 0xf, 0xf, true);
}
template <int CTRL>
__device__ __forceinline__ float dpp(float x) { return __uint_as_float(dppu<CTRL>(__float_as_uint(x))); }
__device__ __forceinline__ float rowsum16(float x) {
    x += dpp<0x128>(x);
    x += dpp<0x124>(x);
    x += dpp<0x122>(x);
    x += dpp<0x121>(x);
    return x;
}
__device__ __forceinline__ v4i ldstream(const void* p) {
    return __builtin_nontemporal_load(reinterpret_cast<const v4i*>(p));
}

__device__ __forceinline__ unsigned lds(const void* p) { return (unsigned)(size_t)(const __attribute__((address_space(3))) char*)p; }
template <int O, bool STREAM>
__device__ __forceinline__ void to_lds(unsigned d, const void* p, unsigned o) {
    if (STREAM) asm volatile("s_mov_b32 m0, %0\n\ts_nop 4\n\tglobal_load_lds_dwordx4 %1, %2 offset:%3 nt" :: "s"(d - O), "v"(o), "s"(p), "n"(O) : "memory");
    else asm volatile("s_mov_b32 m0, %0\n\ts_nop 4\n\tglobal_load_lds_dwordx4 %1, %2 offset:%3" :: "s"(d - O), "v"(o), "s"(p), "n"(O) : "memory");
}
__device__ __forceinline__ void st_rec(void* p, unsigned o, v4i a) {
    asm volatile("s_nop 4\n\tglobal_store_dwordx4 %0, %1, %2 sc1\n\ts_nop 1" :: "v"(o), "v"(a), "s"(p) : "memory");
}
__device__ __forceinline__ v4i ld_rec(const void* p, unsigned o) {
    v4i x;
    asm volatile("s_nop 4\n\tglobal_load_dwordx4 %0, %1, %2 sc1" : "=v"(x) : "v"(o), "s"(p) : "memory");
    return x;
}
typedef unsigned long long u64;
__device__ __forceinline__ u64 ld_dev(const u64* p) { return __hip_atomic_load(p, __ATOMIC_RELAXED, __HIP_MEMORY_SCOPE_AGENT); }
__device__ __forceinline__ void st_dev(u64* p, u64 v) { __hip_atomic_store(p, v, __ATOMIC_RELAXED, __HIP_MEMORY_SCOPE_AGENT); }
__device__ __forceinline__ unsigned ld_wg(const unsigned* p) { return __hip_atomic_load(p, __ATOMIC_RELAXED, __HIP_MEMORY_SCOPE_WORKGROUP); }
__device__ __forceinline__ u64 ld_wg64(const u64* p) { return __hip_atomic_load(p, __ATOMIC_RELAXED, __HIP_MEMORY_SCOPE_WORKGROUP); }
__device__ __forceinline__ void st_wg(unsigned* p, unsigned v) { __hip_atomic_store(p, v, __ATOMIC_RELAXED, __HIP_MEMORY_SCOPE_WORKGROUP); }
__device__ __forceinline__ float fold32(float a, float b) {
    auto s = __builtin_amdgcn_permlane32_swap(__float_as_uint(a), __float_as_uint(b), false, false);
    return __uint_as_float(s[0]) + __uint_as_float(s[1]);
}
__device__ __forceinline__ float fold16(float a, float b) {
    auto s = __builtin_amdgcn_permlane16_swap(__float_as_uint(a), __float_as_uint(b), false, false);
    return __uint_as_float(s[0]) + __uint_as_float(s[1]);
}
__device__ __forceinline__ float x4max(float x) {
    float m;
    asm("s_nop 1\n\tv_max_f32_dpp %1, %1, %1 row_ror:4 row_mask:0xf bank_mask:0xf\n\t"
        "s_nop 1\n\tv_max_f32_dpp %1, %1, %1 row_ror:8 row_mask:0xf bank_mask:0xf\n\t"
        "s_nop 1\n\tv_max_f32_dpp %1, %1, %1 row_bcast:15 row_mask:0xa bank_mask:0xf\n\t"
        "s_nop 1\n\tv_max_f32_dpp %1, %1, %1 row_bcast:31 row_mask:0xc bank_mask:0xf\n\t"
        "s_nop 0\n\tv_readlane_b32 %0, %1, 63\n\ts_nop 1" : "=s"(m), "+v"(x));
    return m;
}

struct __attribute__((aligned(16))) Smem {
    v4i ksz[2][3][NT];
    v4i par[NX][64];
    v4i qt[NH * 24];
    v4i vsz[2][NT];
    v4i rec[NRC - 1][RS / 4];
    float stage[2][WPB][DH];
    unsigned short p[NH][WPB][16];
    unsigned ml[NH];
    float fin[WPB + 1][32];
    u64 epoch;
    unsigned desc[2][2][2 * NH];
    unsigned ready;
    unsigned have;
    unsigned tref;
    unsigned slip;
    unsigned vote[2 * NH];
    unsigned late[WPB];
    unsigned req;
    v2u t0;
    unsigned done;
    unsigned prog;
    unsigned fcnt;
    unsigned dsv;
    unsigned tick;
    unsigned quiet;
    unsigned cnt[NH + NH / 4];
};

__host__ __device__ constexpr int position(int b) { return (b & ~7) | ((b & 3) << 1) | ((b >> 2) & 1); }
constexpr int groupof(int p) {
    const int r0 = p < 85 ? 0 : p < 171 ? 85 : 171, na = p < 171 ? 43 : 42, t = p - r0;
    return r0 + (t < 64 ? ((t >> 4) & 1) * na + (t >> 5) * 16 + (t & 15) : t < na + 32 ? t - 32 : t);
}
struct Order { unsigned g[NWG / 2]; };

__global__ __launch_bounds__(NT + 128, 3) void kivi_stream_kernel(
    const __hip_bfloat16* __restrict__ q, const int* __restrict__ qk, const int* __restrict__ qv,
    const __hip_bfloat16* __restrict__ ks, const __hip_bfloat16* __restrict__ kz,
    const __hip_bfloat16* __restrict__ vs, const __hip_bfloat16* __restrict__ vz,
    float* __restrict__ ws, __hip_bfloat16* __restrict__ out, float scale,
    Order order) {
    __shared__ Smem sm;

    u64 ka0;
    unsigned ka1, ka2;
    asm volatile("s_load_dwordx2 %0, %3, 0x18\n\ts_load_dword %1, %3, %4 offset:0x4c\n\ts_load_dword %2, %3, 0x40"
                 : "=&s"(ka0), "=&s"(ka1), "=&s"(ka2) : "s"(__builtin_amdgcn_kernarg_segment_ptr()), "s"((blockIdx.x & 127) * 4));
    const u64 T0 = __builtin_amdgcn_s_memrealtime();
    const int tid = threadIdx.x;
    typedef const __attribute__((address_space(1))) int* gptr;
    u64 code;
    int codelen;
    asm volatile("s_getpc_b64 %0\nkivi_code_begin:\n\ts_mov_b32 %1, kivi_code_end-kivi_code_begin-4" : "=s"(code), "=s"(codelen));
    const gptr wp = (gptr)(code + min((unsigned)tid * 128u, (unsigned)codelen));
    const int warm = CNT ? __builtin_nontemporal_load(wp) : *wp;
    const int win = blockIdx.x >> 7;
    auto blockof = [&](int it) { return it >> 2 == 10 ? it : (it & ~3) + 2 * win + ((it >> 1) & 1); };
    auto halfof = [&](int it) { return (it >> 2 == 10 ? win : it) & 1; };
    auto group = [&](int it, int x) { return blockof(it) * NWG + ((x >> halfof(it) * 8) & 255); };
    unsigned pb = blockof(tid >> 3) * NWG, ph = halfof(tid >> 3) * 8;
    const int wave = __builtin_amdgcn_readfirstlane(tid >> 6);
    asm volatile("" : "+v"(pb), "+v"(ph));
    SB();
    asm volatile("s_waitcnt lgkmcnt(0)" : "+s"(ka0), "+s"(ka1), "+s"(ka2));
    const int gx = ka1 & 0xffff, px = ka1 >> 16;
    const unsigned pg = pb + ((ka1 >> ph) & 255), po = pg * (DH * 2) + (tid & 7) * 16;
    if (wave < WPB) {
        to_lds<0, true>(lds(&sm.ksz[0][0][wave * 64]), (const void*)ka0, po);
        to_lds<128, true>(lds(&sm.ksz[0][1][wave * 64]), (const void*)ka0, po);
        to_lds<256, true>(lds(&sm.ksz[0][2][wave * 64]), (const void*)ka0, po);
    }
    SB();
    const u64 tb64 = __builtin_amdgcn_s_memrealtime() - LATEB;
    const unsigned tb = (unsigned)tb64;
    u64* tstart = reinterpret_cast<u64*>(ws + WS_T);
    u64* tbase = tstart + 3 * NWG + NR * 32 + 64 * 32;
    if (TBL && blockIdx.x >= NWG - 8 && tid >= NT && tid < NT + 8)
        st_rec(tbase + (blockIdx.x & 7) * 32, (tid & 7) * 16, v4i{(int)tb, (int)(tb64 >> 32), (int)tb, (int)(tb64 >> 32)});
    if (tid == NT) st_dev(tstart + blockIdx.x, tb64);
    int wq = 0;
    if (QNT && tid >= NT && tid < NT + 96) wq = __builtin_nontemporal_load((const int*)q + (tid - NT) * 32);
    if (wave == WPB + 1 && (!FT || blockIdx.x < 120 || ((blockIdx.x >> 3) & 3) == 3)) {
        const unsigned k = blockIdx.x >> 3, k4 = min(k, 7u) * 4;
        const u64 p = ((const __attribute__((address_space(4))) u64*)__builtin_amdgcn_kernarg_segment_ptr())[(0x13365444u >> k4) & 7u];
        unsigned x;
        asm volatile("s_load_dword %0, %1, 0x0\n\ts_waitcnt lgkmcnt(0)" : "=s"(x) : "s"(p + (((k - ((0x74443000u >> k4) & 7u)) << 21) + (FT ? k : blockIdx.x & 7) * 256)) : "memory");
    }
    SB();
    const int lane = tid & 63;
    const int r = lane & 15;
    const int g = lane >> 4;
    const int pos = position(blockIdx.x);
    auto tagof = [](u64 e, int j) {
        e *= 0x9e3779b97f4a7c15ull;
        return (unsigned)(j == 3 ? (e ^ (e >> 29)) >> 8 : e >> (j * 20)) << 8;
    };
    auto until = [&](unsigned t) { while ((int)(clk() - t) < 0) __builtin_amdgcn_s_sleep(1); };
    auto behind = [&]() {
        const unsigned tick = ld_wg(&sm.tick), t = clk();
        while (ld_wg(&sm.tick) == tick && clk() - t < 100u) __builtin_amdgcn_s_sleep(1);
    };
    const int quart = (pos >> 5) & 3, tau = (-((pos >> 1) + quart)) & 15;
    auto shared = [&](int b) { return SPL == 4 || ((b ^ quart) & 1) == 0; };
    auto job = [&](int t, int b, u64 pol, u64 ok, unsigned side) {
        const int bd = tau == 10 || (t == 0 && shared(b)) ? b : 2 * tau + (SPL == 4 ? 0 : quart & 1);
        const int reg = (shared(b) ? t : 1) * 32 + (group(2 * bd, pos & 1 ? px : gx) * 3 >> 13) * 4;
        return (((ok >> reg) & 1u && (t || b >= H0) ? side ^ (unsigned)(pol >> reg) : quart >> 1) ^ pos) & 1u;
    };
    auto describe = [&](int t, int it, int j, u64 pol, u64 ok, unsigned side) {
        const int b = it >> 1, n = it * 2 + j;
        const bool tr = it >> 2 == tau && tau > 0, sh = tr && shared(b);
        const unsigned jb = job(t, tr ? b : 2 * tau, pol, ok, side);
        const unsigned x = tr ? ((sh ? j : jb) ^ pos) & 1u : 0u;
        const unsigned gr = group(it, x ? px : gx), c = 6 * gr + 3;
        const unsigned flip = sh ? 0u : ((unsigned)(pol >> t * 32) >> (c >> 14) * 4) & 1u;
        const bool early = !t && b < H0;
        const unsigned okt = (unsigned)(ok >> t * 32);
        const bool known = (okt >> (it >> 2) * 3 / 8 * 4) & (okt >> ((it >> 2) * 3 + 2) / 8 * 4) & 1u;
        const unsigned ex = (early || b < 2 || !known ? (n & 3) == 0 ? EXS(0) : (n & 3) == 1 ? EXS(1) : (n & 3) == 2 ? EXS(2) : EXS(3) : 0) + (early || known ? 0 : n & 1)
                          + (n >= 84 && n < 87 ? G21 : 0) + (t ? n == 0 ? GAP1 : n == NG2 && ((pol ^ (pol >> 32)) & 0x111111u) ? GAP2 : 0 : n >= 3 && n < 3 + 4 * NSLOW ? EXK : 0);
        return gr * (64 * DH * 4) | ex << 4 | (early || known || sh ? 0u : 8u) | x << 2 | flip << 1
             | (((sh ? jb : j ^ win ^ flip) ^ (c >> 8) ^ (c >> 15)) & 1u);
    };

    const int mh = (blockIdx.x / 16 * 2 + (blockIdx.x & 7) / 4 + 20) % NH, msl = (blockIdx.x & 3) * 2 + ((blockIdx.x >> 3) & 1);
    const bool both = mh >> 1 == 10;
    v4i x[2] = {};
    unsigned fa[4], mls, one, oo;
    auto prepare = [&]() {
        fa[0] = lds(&sm.fin[wave][g == 3 ? 24 + (r & 3) * 2 : (r & 3) * 6 + g * 2]);
        fa[1] = lane == 0 ? lds(&sm.fcnt) : lds(&sm.vote[lane]);
        fa[2] = lds(&sm.fin[lane >> 3][(lane & 7) * 4]);
        fa[3] = lds(&sm.fin[lane >> 3][25]);
        mls = (lane & 3) * 8;
        one = tid < NT && lane == 0;
        oo = (mh * DH + msl * SLC + r * 4 + g) * 2;
        asm volatile("" : "+v"(fa[0]), "+v"(fa[1]), "+v"(fa[2]), "+v"(fa[3]), "+v"(mls), "+v"(one), "+v"(oo));
    };
    unsigned d_t1 = 0, d_o[5] = {};
    if (__builtin_expect(wave < WPB, 1)) {
    const int ww = (wave & 3) * 2 + (wave >> 2);
    const int ti = ww * 4 + g;
    const int tev = ti + (ti >= 11 ? 10 : 0) + (ti >= 22 ? 11 : 0), tod = ti + 11 + (ti >= 10 ? 11 : 0) + (ti >= 21 ? 10 : 0);
    const unsigned offe = tev * (DH * 4) + r * 16, offo = tod * (DH * 4) + r * 16;
    unsigned descv[2], dsc[4];
    auto lanes = [=](unsigned d) { return (d & 1u ? offo : offe) + (d & ~0x3fffu); };
    auto fetch = [&](int t) {
        while (__builtin_amdgcn_readfirstlane(ld_wg(&sm.ready)) == 0) __builtin_amdgcn_s_sleep(1);
        asm volatile("" ::: "memory");
#pragma unroll
        for (int j = 0; j < 2; ++j) {
            descv[j] = sm.desc[t][j][lane];
            if (ww >= WPB / 2 && (descv[j] & 8u)) descv[j] ^= 3u;
        }
    };
    const char* dummy = reinterpret_cast<const char*>(qv) + (size_t)(blockIdx.x * WPB + wave) * 4864;

    v4i buf[RING];
    auto burst = [&](const char* p, int i, unsigned o) {
#pragma unroll
        for (int e = 0; e < 3; ++e) { buf[i * 3 + e] = ldstream(p + o + e * 256); SB(); }
    };

    const unsigned phase = ((pos & (NWG / 2 - 1)) * (BODY16 / 512) + ww * WST * (BODY16 / (12 * 256))) >> 16;
    unsigned base = tb + phase + PSH;
    unsigned sched = 0, off = 0, offv = 0;
    int prev = 0, now = 0, hold = 0, big = 0, body = 0;
    unsigned hist = 0;
    unsigned vc = 0;
    int d_l = 0, d_n = 0, d_f = 0, d_s = 0, d_nb = 0;
    unsigned d_bar = 0, d_x = 0;
    auto pace = [&](int due) {
        int now;
        while ((now = (int)(clk() - base)) < due) __builtin_amdgcn_s_sleep(1);
        return now;
    };
    auto follow = [&](unsigned o2) {
        hold = o2 - off > SLIP / 2 ? HOLDB : o2 != off ? HOLD : max(hold - 1, 0);
        big = o2 - off > SLIP / 2 ? HOLDB : max(big - 1, 0);
        if ((int)(o2 - off) >= SLIP) ++d_nb;
        base += o2 - off;
        off = o2;
    };
    auto pacen = [&](auto N) {
        constexpr int n = decltype(N)::value;
        follow(__builtin_amdgcn_readfirstlane(offv));
        now = pace(slot(n));
        vc = inflight();
        hist <<= 1;
        if (__builtin_expect(vc > 3, 0)) {
            hist |= 1u;
            asm volatile("s_waitcnt vmcnt(3)");
            now = (int)(clk() - base);
            if (now > slot(n) + THR && prev < PRV && big == 0 && off + SLIP <= MAXOFF && body >= NOSLK
                && (int)(base + now - __builtin_amdgcn_readfirstlane(ld_wg(&sm.quiet))) >= 0) {
                if (lane == 0 && __hip_atomic_fetch_add(&sm.vote[body], 1u, __ATOMIC_RELAXED, __HIP_MEMORY_SCOPE_WORKGROUP) + 1 >= VOTES)
                    __hip_atomic_fetch_max(&sm.slip, off + SLIP, __ATOMIC_RELAXED, __HIP_MEMORY_SCOPE_WORKGROUP);
                unsigned o2;
                while ((o2 = __builtin_amdgcn_readfirstlane(ld_wg(&sm.slip))) == off && (int)(clk() - base) < now + VWAIT) {}
                if (o2 != off) { follow(o2); now = pace(slot(n)); }
            }
            if (hold == 0 && off + SS <= MAXOFF && body >= NOSM && body % NH != 21 && __builtin_popcount(hist & DBM) >= DBN && lane == 0) st_wg(&sm.req, off + SS);
        }
        if (d_n >= BA && d_n < BB) d_l = max(d_l, now - slot(n));
        if (now - slot(n) > 30 && d_f == 0) d_f = d_n + 1;
        if (off > 0 && d_s == 0) d_s = d_n + 1;
        ++d_n;
    };
    auto check = [&](auto N) {
        constexpr int n = decltype(N)::value;
        prev = now - slot(n);
        if (lane == 0) st_wg(&sm.late[wave], (unsigned)prev);
    };
    auto getslip = [&]() { offv = __hip_atomic_load(&sm.slip, __ATOMIC_RELAXED, __HIP_MEMORY_SCOPE_WORKGROUP); };
    auto arrive = [&](int i) {
        asm volatile("" ::: "memory");
        if (lane == 0) __hip_atomic_fetch_add(&sm.cnt[i], 1u, __ATOMIC_RELAXED, __HIP_MEMORY_SCOPE_WORKGROUP);
    };
    auto next_body = [&]() {
        const unsigned s2 = sched + BODY16;
        base += (s2 >> 16) - (sched >> 16);
        sched = s2;
        ++body;
        if (DIAG == 14 && body == BA) d_bar = base - (sched >> 16) - d_nb * SLIP;
    };

    {
        to_lds<0, false>(lds(&sm.qt[wave * 64]), q, tid * 16);
        if (wave < 4) to_lds<0, false>(lds(&sm.qt[NT + wave * 64]), q, (NT + tid) * 16);
        PST(1); pace(slot(3)); SB();
        to_lds<0, true>(lds(&sm.ksz[1][0][wave * 64]), kz, po);
        to_lds<128, true>(lds(&sm.ksz[1][1][wave * 64]), kz, po);
        to_lds<256, true>(lds(&sm.ksz[1][2][wave * 64]), kz, po);
        SB();
        const bool isk = lane < 48, isz = isk ? lane >= 24 : lane >= 56;
        const char* par = (const char*)(isk ? isz ? kz : ks : isz ? vz : vs) + (isk ? lane % 24 : lane & 7) * 16
                        + group(tau * 4 + (wave & 3), px) * (isk ? DH * 2 : 128);
        asm volatile("" : "+v"(par));
        dsc[0] = describe(0, 0, 0, 0u, 0u, 0u);
        dsc[1] = describe(0, 0, 1, 0u, 0u, 0u);
        PST(2); pace(slot(6)); SB();
        to_lds<0, true>(lds(&sm.vsz[0][wave * 64]), vs, pg * 128 + (tid & 7) * 16);
        to_lds<0, true>(lds(&sm.vsz[1][wave * 64]), vz, pg * 128 + (tid & 7) * 16);
        if (wave >= WPB - NX)
            asm volatile("s_mov_b32 m0, %0\n\ts_nop 0\n\tglobal_load_lds_dwordx4 %1, off nt" :: "s"(lds(&sm.par[wave & 3][0])), "v"(par) : "memory");
        PST(3); pace(slot(8) + KD); SB();
        burst((const char*)qk, 0, lanes(dsc[0]));
        PST(4);
        descv[0] = describe(0, lane, 0, 0u, 0u, 0u);
        descv[1] = describe(0, lane, 1, 0u, 0u, 0u);
        asm volatile("" : "+v"(descv[0]), "+v"(descv[1]));
        if (THRO) asm volatile("" :: "v"(warm));
        PST(5); pace(slot(11) + KD); SB();
        burst((const char*)qk, 1, lanes(dsc[1]));
        PST(6);
    }
    {
        unsigned t0;
        for (;;) {
            const u64 t = ld_wg64((const u64*)&sm.t0);
            t0 = __builtin_amdgcn_readfirstlane((unsigned)t);
            if (t0 == ~__builtin_amdgcn_readfirstlane((unsigned)(t >> 32)) && t0 - tb + 1100u < 2200u) break;
            t0 = tb;
            if (clk() - tb > 1000u) break;
        }
        PST(7);
        PST(8);
        d_bar = clk();
        base = t0 + OFF0 + phase;
    }

    const v2f huge = {0x1p127f, 0x1p127f};

    for (int S = 0; S < NH; ++S) {
        v2f kz2[6];
        unsigned slo[6], shi[6];
        v2u qq[3];
        const char* nsup = (const char*)(S + 1 < NH ? qk : qv);
        const int head = S >> 1 == 10 ? S : (S & ~1) + win;
        float mylogit = 0.f;
        float dot = 0.f;
        sfor<12>([&](auto N) {
            constexpr int n = decltype(N)::value;
            constexpr int i = n / 3, j = n % 3;
            if (n == 6 && (S == H0 - 1 || S == NH - 1)) fetch(S == NH - 1);
            if (j == 0) {
                const unsigned d = dsc[i ^ 2] = __builtin_amdgcn_readlane(descv[i & 1], (2 * S + 1 + (i >> 1)) & 63);
                const unsigned o = lanes(d);
                base += (d >> 4) & 31u;
                pacen(N);
                SB();
                burst(i < 2 ? (const char*)qk : nsup, i ^ 2, o);
                SB();
                if (tid == NT - 1) st_wg(&sm.tick, S * 4 + i + 1);
                check(N);
                if (n == 0 && S == 0) asm volatile("s_waitcnt vmcnt(9)\n\ts_barrier" ::: "memory");
            }
            if (j == 2) getslip();
            if (i % 2 == 0 || ((dsc[i] ^ dsc[i ^ 1]) & 4u)) {
                const int it = S * 2 + (i >> 1);
                const bool other = dsc[i] & 4u;
                const v2u* pk = reinterpret_cast<const v2u*>(other ? &sm.par[it & 3][j * 8] : &sm.ksz[0][j][it * 8]) + r;
                const v2u a = pk[0], b = pk[other ? 48 : 3 * NT * 2];
                slo[j * 2 + 0] = a.x & 0xffffu; shi[j * 2 + 0] = a.x & 0xffff0000u;
                slo[j * 2 + 1] = a.y & 0xffffu; shi[j * 2 + 1] = a.y & 0xffff0000u;
                kz2[j * 2 + 0] = v2f{bf_lo(b.x), bf_hi(b.x)} * -0x1p-22f;
                kz2[j * 2 + 1] = v2f{bf_lo(b.y), bf_hi(b.y)} * -0x1p-22f;
            }
            if (i == 0) qq[j] = reinterpret_cast<const v2u*>(&sm.qt[head * 24])[j * 16 + r];
            if (j == 0) dot = 0.f;
            {
                const v4i x = buf[n % RING];
                const v2f t0 = __builtin_elementwise_fma(v2f{__int_as_float(x.x), __int_as_float(x.y)}, huge, kz2[j * 2]);
                const v2f t1 = __builtin_elementwise_fma(v2f{__int_as_float(x.z), __int_as_float(x.w)}, huge, kz2[j * 2 + 1]);
                float y[4];
                mul4(y, pack(t0.x, t0.y), pack(t1.x, t1.y), slo[j * 2], shi[j * 2], slo[j * 2 + 1], shi[j * 2 + 1]);
                dot = dot2(pack(y[0], y[1]), qq[j].x, dot);
                dot = dot2(pack(y[2], y[3]), qq[j].y, dot);
            }
            if (j == 2) {
                const float d = rowsum16(dot);
                if ((r & 3) == (i ^ ((dsc[i] >> 1) & 1))) mylogit = d;
            }
            asm volatile("" : "+v"(dot), "+v"(mylogit));
            SB();
        });
        if (r < 4) sm.p[S][wave][g * 4 + r] = (unsigned short)pack(rb(mylogit * 0x1p22f) * scale, 0.f);
        if (!THRO) asm volatile("" :: "v"(warm));
        if ((S & 3) == 3) arrive(NH + (S >> 2));
        if (S == NH - 3 && tid == 0) st_wg(&sm.done, 1u);
        next_body();
    }

    for (int S = 0; S < NH; ++S) {
        v2u P;
        unsigned vsl, vsh, vzf;
        v4f acc = {0.f, 0.f, 0.f, 0.f};
        float y0[12];
        unsigned b0[12];
        const bool more = S + 1 < NH;
        unsigned sl, sh;
        v2u A[12];
        v2f z2;
        sfor<12>([&](auto N) {
            constexpr int n = decltype(N)::value;
            constexpr int i = n / 3, j = n % 3;
            if (j == 0) {
                const unsigned d = dsc[i ^ 2] = __builtin_amdgcn_readlane(descv[i & 1], (2 * S + 1 + (i >> 1)) & 63);
                unsigned o = lanes(d);
                const char* src = (const char*)qv;
                if (i >= 2 && !more) {
                    o = DUM ? 0 : i * 768;
                    src = !DUM ? dummy : i == 3 && wave == 0 ? (const char*)out + min(mh, NH - 2) * (DH * 2) : (const char*)ws + (i & 1) * (15 << 18) + blockIdx.x * 768;
                }
                if (i < 2 || more) base += (d >> 4) & 31u;
                if (i < 2 || more) pacen(N);
                SB();
                burst(src, i ^ 2, o);
                SB();
                if (tid == NT - 1) st_wg(&sm.tick, 4 * NH + S * 4 + i + 1);
                check(N);
            }
            if (n == 0) {
                const unsigned need = S < 2 ? 1 : S;
                while (__builtin_amdgcn_readfirstlane(ld_wg(&sm.prog)) < need) {}
                asm volatile("" ::: "memory");
                P = *reinterpret_cast<const v2u*>(&sm.p[S][wave][g * 4]);
                P.x = __builtin_amdgcn_alignbit(P.x, P.x, (dsc[0] & 2u) << 3);
                P.y = __builtin_amdgcn_alignbit(P.y, P.y, (dsc[2] & 2u) << 3);
                const unsigned d3 = __builtin_amdgcn_readlane(descv[1], 2 * S + 1);
                const unsigned x = (((dsc[0] & 7u) | (dsc[1] & 7u) << 8) | ((dsc[2] & 7u) << 16 | (d3 & 7u) << 24)) >> (r & 3) * 8;
                const int it = S * 2 + ((r >> 1) & 1);
                const unsigned short* pv = reinterpret_cast<const unsigned short*>(x & 4u ? &sm.par[it & 3][48] : &sm.vsz[0][it * 8]) + (x & 1u ? tod : tev);
                vsl = pv[0];
                vsh = vsl << 16;
                vzf = __float_as_uint(bf_lo(pv[x & 4u ? 64 : NT * 8]) * -0x1p-22f);
            }
            if (j == 2) getslip();
            if (j == 0) {
                sl = dppu<0x55 * i>(vsl);
                sh = dppu<0x55 * i>(vsh);
                const float zb = __uint_as_float(dppu<0x55 * i>(vzf));
                z2 = v2f{zb, zb};
            }
            float y[4];
            {
                const v4i x = buf[n];
                const v2f t0 = __builtin_elementwise_fma(v2f{__int_as_float(x.x), __int_as_float(x.y)}, huge, z2);
                const v2f t1 = __builtin_elementwise_fma(v2f{__int_as_float(x.z), __int_as_float(x.w)}, huge, z2);
                mul4(y, pack(t0.x, t0.y), pack(t1.x, t1.y), sl, sh, sl, sh);
            }
#pragma unroll
            for (int e = 0; e < 4; ++e) {
                const int idx = j * 4 + e;
                if (i % 2 == 0) {
                    y0[idx] = y[e];
                    asm volatile("" : "+v"(y0[idx]));
                    if (i == 2) {
                        A[idx] = v2u{r == idx ? P.x : 0u, r == idx ? P.y : 0u};
                        if (AH) asm volatile("" : "+v"(A[idx]));
                    }
                } else if (i == 1) {
                    b0[idx] = pack(y0[idx], y[e]);
                    asm volatile("" : "+v"(b0[idx]));
                } else {
                    const v2u B = {b0[idx], pack(y0[idx], y[e])};
                    acc = __builtin_amdgcn_mfma_f32_16x16x16bf16_1k(__builtin_bit_cast(v4s, A[idx]), __builtin_bit_cast(v4s, B), acc, 0, 0, 0);
                }
            }
            if (n == 11) {
                if (S == NH - 2 && tid == 0) sm.tref = base + slot(12) + slot(3);
                if (S == NH - 3 && tid == 0) st_wg(&sm.done, 2u);
                if (lane < 48) *reinterpret_cast<v4f*>(&sm.stage[S & 1][wave][lane * 4]) = acc;
                arrive(S);
            }
            SB();
        });
        next_body();
    }
    if (DST >= 21) d_x = sm.dsv;
    d_t1 = clk();
    d_o[0] = off; d_o[1] = DST ? d_x : d_l; d_o[2] = d_f; d_o[3] = d_bar - phase; d_o[4] = DIAG >= 12 ? d_nb : d_s;
    {
        const unsigned tag = tagof(sm.epoch, lane & 3);
        const float* src = ws + WS_R + ((size_t)((both ? 0 : mh & 1) * (NWG / 2) + wave * 16) * NRC + (mh >> 1) + (mh > 20)) * RS + msl * 16;
        const unsigned so = (lane >> 2) * (NRC * RS * 4) + (lane & 3) * 16;
        prepare();
        until(base - slot(12) + slot(3) - phase + (wave - (WPB - 1)) * STG + (mh >= NH - 2 ? TPL : both ? TPC - TPB : TPC));
        for (;;) {
            x[0] = ld_rec(src, so);
            x[1] = v4i{0, 0, 0, (int)tag};
            if (both) x[1] = ld_rec(src + NWG / 2 * NRC * RS, so);
            asm volatile("s_waitcnt vmcnt(0)" : "+v"(x[0]), "+v"(x[1]));
            if (__all((((unsigned)x[0].w ^ tag) | ((unsigned)x[1].w ^ tag)) < 256u) || clk() - tb >= TMO) break;
            __builtin_amdgcn_s_sleep(PSL);
        }
#pragma unroll
        for (int d = 5; d >= 0; --d) asm volatile("" :: "v"(buf[d]));
        x[0].w ^= tag;
        x[1].w ^= tag;
    }
    } else
    if (wave == WPB) {
        const int l = lane;
        u64* echo = tstart + NWG;
        u64* gofs = tstart + 3 * NWG;
        if (l == 0) {
            if (blockIdx.x < NR) st_dev(gofs + blockIdx.x * 32, tb64 - 100000ull);
            if (blockIdx.x == NR) st_dev(gofs + 16, tb64 - 100000ull);
            sm.slip = 0;
            sm.req = 0;
            sm.done = 0;
            sm.prog = 0;
            sm.ready = 0;
            sm.tick = 0;
            sm.quiet = tb;
            sm.fcnt = 0;
            sm.epoch = tb64;
            sm.have = blockIdx.x == 0;
        }
        if (l < NH + NH / 4) sm.cnt[l] = 0;
        sm.vote[l] = 0;
        until(tb + TGATH);
        unsigned ld;
        if (TBL == 2) {
            for (;;) {
                u64 x[8];
                asm volatile("s_load_dwordx2 %0, %8, 0x0 glc\n\ts_load_dwordx2 %1, %8, 0x100 glc\n\ts_load_dwordx2 %2, %8, 0x200 glc\n\t"
                             "s_load_dwordx2 %3, %8, 0x300 glc\n\ts_load_dwordx2 %4, %8, 0x400 glc\n\ts_load_dwordx2 %5, %8, 0x500 glc\n\t"
                             "s_load_dwordx2 %6, %8, 0x600 glc\n\ts_load_dwordx2 %7, %8, 0x700 glc\n\ts_waitcnt lgkmcnt(0)"
                             : "=&s"(x[0]), "=&s"(x[1]), "=&s"(x[2]), "=&s"(x[3]), "=&s"(x[4]), "=&s"(x[5]), "=&s"(x[6]), "=&s"(x[7]) : "s"(tbase) : "memory");
                bool all = true;
                ld = 0;
#pragma unroll
                for (int u = 0; u < 8; ++u) {
                    const u64 dt = x[u] - tb64 + 1000ull;
                    if (dt < 2000ull) ld = max(ld, (unsigned)dt); else all = false;
                }
                if (all || clk() - tb > (unsigned)TGMAX) break;
            }
        } else {
            for (;;) {
                const u64 dt = ld_dev(TBL ? tbase + (l & 7) * 32 : tstart + NWG - 8 + (l & 7)) - tb64 + 1000ull;
                ld = dt < 2000ull ? (unsigned)dt : 0u;
                if (__all(dt < 2000ull) || clk() - tb > (unsigned)TGMAX) break;
            }
            ld = max(ld, dppu<0x124>(ld));
            ld = max(ld, dppu<0x122>(ld));
            ld = max(ld, dppu<0x121>(ld));
            ld = __builtin_amdgcn_readfirstlane(ld);
        }
        const u64 t64 = ld ? tb64 + ld - 1000ull : tb64;
        if (l == 0) sm.t0 = v2u{(unsigned)t64, ~(unsigned)t64};
        if (l == 0) sm.dsv = clk() - tb;
        u64 to[NWG / 64];
        bool have = blockIdx.x == 0;
        auto chores = [&](bool all) {
            if (blockIdx.x == 0) {
                u64 x[NWG / 64];
#pragma unroll
                for (int u = 0; u < NWG / 64; ++u) x[u] = ld_dev(tstart + u * 64 + l);
#pragma unroll
                for (int u = 0; u < NWG / 64; ++u) {
                    if (all || __any(x[u] != to[u])) st_rec(echo + u * 128, l * 16, v4i{(int)(unsigned)x[u], (int)(unsigned)(x[u] >> 32), (int)tb, (int)(unsigned)(tb64 >> 32)});
                    to[u] = x[u];
                }
            } else if (!have && !all) {
                v4i x = ld_rec(echo + blockIdx.x * 2, 0);
                asm volatile("s_waitcnt vmcnt(0)" : "+v"(x));
                if ((unsigned)x.x == tb && (unsigned)x.y == (unsigned)(tb64 >> 32)) {
                    have = true;
                    if (l == 0) sm.epoch = (u64)(unsigned)x.w << 32 | (unsigned)x.z;
                    asm volatile("s_waitcnt lgkmcnt(0)" ::: "memory");
                    if (l == 0) st_wg(&sm.have, 1u);
                }
            }
        };
        if (EARLY) chores(true);
        asm volatile("s_waitcnt lgkmcnt(0)\n\ts_barrier" ::: "memory");
        asm volatile("" :: "v"(warm), "v"(wq));
        if (!EARLY) chores(true);
        if (!ETCH) {
            until(tb + TECHO);
            behind();
            chores(false);
        }
        const u64* mine = NR > 1 ? gofs + ((blockIdx.x >> 3) % NR) * 32 : gofs + 16;
        unsigned sent = 0, stage = 0, tsh = clk();
        int nt = blockIdx.x < 200, spin = 0;
        while (stage < 2) {
            if (PB > 1 && have) __builtin_amdgcn_s_sleep(PSLEEP);
            behind();
            unsigned tch = 0;
            if (nt < 25) {
                const unsigned pg = (blockIdx.x >> 3) + 32 * (nt % 12);
                const char* tp = (nt < 12 ? (const char*)qk : nt < 24 ? (const char*)qv : (const char*)ws) + ((size_t)pg << 21);
                const unsigned vp = (unsigned)((size_t)tp >> 21);
                const bool first = (vp & (PTL - 1)) == 0 || min(pg & 127u, 128u - (pg & 127u)) < (vp & -vp);
                tp += (((first ? blockIdx.x >> 3 : blockIdx.x) * 29 + nt) & 255) * 256;
                if (nt == 24 && blockIdx.x >= 16) tp = (const char*)out + min((pg - 2) * 4096, NH * DH * 2 - 4u);
                if (ld_wg(&sm.tick) + (first ? TLEAD + 4 : TLEAD) >= (unsigned)max(nt / 12 * 128 + nt % 12 * 4 / 3 * 8 - nt / 24 * 32, TLEAD + TF0)) {
                    unsigned x;
                    if (nt < 24 && !first) { if (VT) asm volatile("s_nop 4\n\tglobal_load_dword %0, %1, %2 nt" : "+v"(tch) : "v"(0), "s"(tp) : "memory"); }
                    else if (nt < 24 || blockIdx.x < 48) asm volatile("s_load_dword %0, %1, 0x0\n\ts_waitcnt lgkmcnt(0)" : "=s"(x) : "s"(tp) : "memory");
                    ++nt;
                }
            }
            u64 gd;
            if (SPOLL) asm volatile("s_load_dwordx2 %0, %1, 0x0 glc\n\ts_waitcnt lgkmcnt(0)" : "=s"(gd) : "s"(mine) : "memory");
            else gd = ld_dev(mine);
            gd -= t64;
            asm volatile("s_waitcnt vmcnt(0)" : "+v"(tch));
            const unsigned go = gd <= (u64)MAXOFF ? (unsigned)gd : 0u;
            const unsigned own = ld_wg(&sm.slip);
            if (go > own && l == 0) __hip_atomic_fetch_max(&sm.slip, go, __ATOMIC_RELAXED, __HIP_MEMORY_SCOPE_WORKGROUP);
            if (own <= go) tsh = clk();
            else if (clk() - tsh > (unsigned)REVT && blockIdx.x >= NSENT) { if (l == 0) st_wg(&sm.slip, go); }
            unsigned req = ld_wg(&sm.req);
            if ((int)(clk() - ld_wg(&sm.quiet)) < 0) { req = 0; if (l == 0) st_wg(&sm.req, 0u); }
            unsigned want = blockIdx.x < NSENT ? own : 0u;
            if (req == go + SS && own == go) {
                unsigned x = ld_wg(&sm.late[l & 7]);
                x += dppu<0x121>(x);
                x += dppu<0x122>(x);
                x += dppu<0x124>(x);
                want = min(req + (__builtin_amdgcn_readfirstlane(x) * LFAC / 64 & ~15u), (unsigned)MAXOFF);
            }
            if (want > go && want > sent) {
                u64 old = 0;
                behind();
                if (l == 0) st_wg(&sm.quiet, clk() + TQA);
                if (l == 0) old = __hip_atomic_fetch_max(gofs + 16, t64 + want, __ATOMIC_RELAXED, __HIP_MEMORY_SCOPE_AGENT);
                if (NR > 1 && __builtin_amdgcn_readfirstlane((unsigned)(old < t64 + want)) && l < NR)
                    __hip_atomic_fetch_max(gofs + l * 32, t64 + want, __ATOMIC_RELAXED, __HIP_MEMORY_SCOPE_AGENT);
                sent = want;
            }
            if (have ? blockIdx.x == 0 && ++spin % 8 == 0 : clk() - tb >= (unsigned)(blockIdx.x - CAL0 < 64u ? TECHO : TECHO2)) { behind(); chores(false); }
            if (ld_wg(&sm.done) > stage) {
                ++stage;
                behind();
                asm volatile("" : "+s"(code));
                int w = 0;
#pragma unroll
                for (int u = 0; u < 4; ++u) {
                    const int a = (u * 64 + l) * 128;
                    const gptr cp = (gptr)(code + (a < codelen ? a : 0));
                    w ^= CNT ? __builtin_nontemporal_load(cp) : *cp;
                }
                asm volatile("" :: "v"(w));
            }
        }
        while (!have && clk() - tb < TMO) chores(false);
        if (l == 0) st_wg(&sm.have, 1u);
        if (blockIdx.x == 0) {
            for (spin = 1; ld_wg(&sm.fcnt) < WPB; ++spin) {
                __builtin_amdgcn_s_sleep(2);
                if (spin % 1024 == 0) chores(false);
            }
            return;
        }
        if (!DRY) return;
        prepare();
    } else {
        u64* table = tstart + 3 * NWG + NR * 32;
        const unsigned ent = blockIdx.x - CAL0;
        int lead = 0, lat = 0;
        if (PRE) {
            v4i y[PRE ? PRE : 1];
#pragma unroll
            for (int u = 0; u < PRE; ++u) y[u] = *(const v4i*)((const char*)(ws + WS_R) + (pos * NRC + NRC - 1 - (PRE > 1 ? u * 2 + (lane >> 5) : 0)) * (RS * 4) + (lane & 31) * 16);
#pragma unroll
            for (int u = 0; u < PRE; ++u) asm volatile("" :: "v"(y[u]));
        }
        __syncthreads();
        if (ent < 64 && (ent & 31) < 24 && (ent & 3) < NMEM) {
            const unsigned t0 = __builtin_amdgcn_readfirstlane(sm.t0.x);
            unsigned page, x[4];
            auto pair = [&](unsigned k) {
                const unsigned h = (t0 + k + (ent & ~3u) * 2) * 0x9e3779b1u;
                page = (ent & 28) * 16 + (k & 1) * 32 + 24 + ((t0 + (k & 1)) * 0x85ebca6bu >> 29);
                return (const char*)(ent < 32 ? +qk : +qv) + ((size_t)page << 21) + (h >> 25) * 16384 + ((h >> 8) & 127) * 64;
            };
            asm volatile("s_load_dword %0, %4, 0x0\n\ts_load_dword %1, %4, 0x2000\n\ts_load_dword %2, %5, 0x0\n\t"
                         "s_load_dword %3, %5, 0x2000\n\ts_waitcnt lgkmcnt(0)"
                         : "=&s"(x[0]), "=&s"(x[1]), "=&s"(x[2]), "=&s"(x[3]) : "s"(pair(ent % 4 * 2)), "s"(pair(ent % 4 * 2 + 1)) : "memory");
            until(t0 + TPRB);
#pragma nounroll
            for (unsigned k = 2; k < 2 * NMEM; ++k) {
                u64 ta, tc, td;
                asm volatile("s_memtime %0\n\ts_waitcnt lgkmcnt(0)\n\ts_load_dword %3, %4, 0x0\n\ts_waitcnt lgkmcnt(0)\n\t"
                             "s_memtime %1\n\ts_waitcnt lgkmcnt(0)\n\ts_load_dword %3, %4, 0x2000\n\ts_waitcnt lgkmcnt(0)\n\t"
                             "s_memtime %2\n\ts_waitcnt lgkmcnt(0)"
                             : "=&s"(ta), "=&s"(tc), "=&s"(td), "=&s"(x[0]) : "s"(pair((ent % 4 * 2 + k) % (2 * NMEM))) : "memory");
                const int d0 = (int)(tc - ta), d1 = (int)(td - tc), d = max(-PCL, min(PCL, d1 - d0));
                if (min(d0, d1) > 350 && d0 + d1 < 1700) lead += (page ^ (page >> 7)) & 1 ? -d : d;
                lat += d0 + d1;
            }
        }
        asm volatile("" :: "v"(warm), "v"(wq));
        while (ld_wg(&sm.have) == 0 && clk() - tb < TMO) __builtin_amdgcn_s_sleep(4);
        asm volatile("" ::: "memory");
        const u64 E = sm.epoch;
        if (ent < 64 && (ent & 31) < 24 && (ent & 3) < NMEM) {
            unsigned xcc;
            asm volatile("s_getreg_b32 %0, hwreg(HW_REG_XCC_ID, 0, 3)" : "=s"(xcc));
            if (xcc & 4) lead = -lead;
            behind();
            if (lane == 0) st_wg(&sm.quiet, clk() + TQ);
            if (lane < 8) st_rec(table + ent * 32, lane * 16, v4i{lead, (int)(xcc - blockIdx.x), (int)(unsigned)E, (int)(unsigned)(E >> 32)});
        }
        unsigned jobs = 0;
        v4i cal;
        until(tb + TTAB);
        for (;;) {
            behind();
            cal = ld_rec(table, ((lane & 31) < 24 && (lane & 3) < NMEM ? lane : 0) * 256);
            asm volatile("s_waitcnt vmcnt(0)" : "+v"(cal));
            if (__all((unsigned)cal.z == (unsigned)E && (unsigned)cal.w == (unsigned)(E >> 32)) || clk() - tb >= TMO) break;
        }
        {
            int sum = (lane & 3) < NMEM ? cal.x : 0;
            sum += (int)dppu<0xB1>(sum);
            sum += (int)dppu<0x4E>(sum);
            const u64 pol = __ballot(sum < 0) ^ FPOL, ok = __ballot(abs(sum) >= PTH) & FMASK;
            const unsigned side = ((unsigned)__builtin_amdgcn_readfirstlane(cal.y) + (blockIdx.x & ~4u)) >> 2;
#pragma unroll
            for (int u = 0; u < 4; ++u) {
                sm.desc[u >> 1][u & 1][lane] = describe(u >> 1, lane, u & 1, pol, ok, side);
                jobs |= (tau > 0 ? job(u >> 1, 2 * tau + (u & 1), pol, ok, side) : pos & 1u) << u;
            }
            asm volatile("" ::: "memory");
            if (lane == 0 && DST == 21) sm.dsv = clk() - tb;
            if (lane == 0 && DST == 22) sm.dsv = (12 - __popcll(ok & 0x11111100111111ull)) * 10;
            if (lane == 0) st_wg(&sm.ready, 1u);
        }
#pragma nounroll
        for (int cc = 0; cc <= NH / 4; ++cc) {
            const int c = cc < NH / 4 ? cc : tau >> 1;
            const bool act = (tau > 0 && c * 2 + (g >> 1) == tau) == (cc == NH / 4);
            while (ld_wg(&sm.cnt[NH + c]) < WPB) __builtin_amdgcn_s_sleep(2);
            asm volatile("" ::: "memory");
            if (cc >= NH / 4 - 1) {
                const bool sw = ((jobs ^ (jobs >> 2)) >> (lane >> 5)) & 1u;
                v2u* mine = reinterpret_cast<v2u*>(&sm.p[2 * tau][0][0]) + lane;
                if (cc < NH / 4) {
                    if (lane == 0) st_wg(&sm.quiet, clk() + TXW + TQ);
                    if (sw) st_rec(ws + WS_X + pos * 256, lane * 16, v4i{(int)mine->x, (int)mine->y, (int)(unsigned)E, (int)(unsigned)(E >> 32)});
                } else if (__any(sw)) {
                    until(clk() + TXW);
                }
                while (cc == NH / 4 && __any(sw)) {
                    v4i x = {};
                    if (sw) asm volatile("s_nop 4\n\tglobal_load_dwordx4 %0, %1, %2 sc1 nt" : "+v"(x) : "v"(lane * 16), "s"(ws + WS_X + (pos ^ 1) * 256) : "memory");
                    asm volatile("s_waitcnt vmcnt(0)" : "+v"(x));
                    if (__all(!sw || ((unsigned)x.z == (unsigned)E && (unsigned)x.w == (unsigned)(E >> 32))) || clk() - tb >= TMO) {
                        if (sw) *mine = v2u{(unsigned)x.x, (unsigned)x.y};
                        break;
                    }
                    __builtin_amdgcn_s_sleep(8);
                }
                asm volatile("" ::: "memory");
            }
            const bool two = c * 2 + (g >> 1) != 10;
            v4i* pp = reinterpret_cast<v4i*>(&sm.p[c * 4][0][0]) + lane;
            const v4i u = *pp;
            float x[8];
#pragma unroll
            for (int e = 0; e < 4; ++e) { x[2 * e] = bf_lo(u[e]); x[2 * e + 1] = bf_hi(u[e]); }
            float M = fmaxf(fmaxf(fmaxf(x[0], x[1]), fmaxf(x[2], x[3])), fmaxf(fmaxf(x[4], x[5]), fmaxf(x[6], x[7])));
            M = fmaxf(M, dpp<0x128>(M));
            M = fmaxf(M, dpp<0x124>(M));
            M = fmaxf(M, dpp<0x122>(M));
            M = fmaxf(M, dpp<0x121>(M));
            {
                auto y = __builtin_amdgcn_permlane16_swap(__float_as_uint(M), __float_as_uint(M), false, false);
                if (two) M = fmaxf(__uint_as_float(y[0]), __uint_as_float(y[1]));
            }
            v4i o;
            float L = 0.f;
#pragma unroll
            for (int e = 0; e < 4; ++e) {
                const unsigned pk = pack(__expf(x[2 * e] - M), __expf(x[2 * e + 1] - M));
                o[e] = (int)pk;
                L += bf_lo(pk) + bf_hi(pk);
            }
            if (act) *pp = o;
            L = rowsum16(L);
            const float L2 = fold16(L, L);
            if (two) L = L2;
            if (r == 0 && act) sm.ml[c * 4 + g] = __float_as_uint(M) | __builtin_bit_cast(unsigned short, (_Float16)L);
            asm volatile("" ::: "memory");
            if (lane == 0) st_wg(&sm.prog, 1u);
        }
        const unsigned tag = tagof(E, lane & 3);
        auto place = [&](int i) {
            const int i0 = tau + (tau > 10);
            const unsigned jb = i == i0 ? jobs >> 2 : i == 11 && tau == 10 ? jobs >> 3 : pos;
            return (((pos & ~1) | (jb & 1)) * NRC + i) * (RS * 4) + (lane & 31) * 16;
        };
        const unsigned last = place(NRC - 1);
        v2f keep[3] = {};
#pragma nounroll
        for (int h = 0; h < NH; ++h) {
            const unsigned mlb = (sm.ml[h] >> (lane & 3) * 8) & 0xffu;
            if (h < NH - 1) { while (ld_wg(&sm.cnt[h]) < WPB) __builtin_amdgcn_s_sleep(4); }
            else { while (ld_wg(&sm.cnt[h]) < WPB) {} }
            asm volatile("" ::: "memory");
            v2f c[4][3];
#pragma unroll
            for (int e = 0; e < 4; ++e)
#pragma unroll
                for (int k = 0; k < 3; ++k) c[e][k] = *reinterpret_cast<const v2f*>(&sm.stage[h & 1][(lane >> 5) * 4 + e][(lane & 31) * 6 + k * 2]);
            v4i x;
            const bool first = (h & 1) == 0 && h != 20;
#pragma unroll
            for (int k = 0; k < 3; ++k) {
                const v2f a = (keep[k] + (c[0][k] + c[1][k])) + (c[2][k] + c[3][k]), b = a * 0x1p22f;
                x[k] = (int)pack(fold32(b.x, b.x), fold32(b.y, b.y));
                keep[k] = first ? a : v2f{0.f, 0.f};
            }
            x[3] = (int)(tag | mlb);
            if (h == NH - 1) {
                if (lane < 32) {
                    if (BNT & 2) asm volatile("s_nop 4\n\tglobal_store_dwordx4 %0, %1, %2 sc1 nt\n\ts_nop 1" :: "v"(last), "v"(x), "s"(ws + WS_R) : "memory");
                    else st_rec(ws + WS_R, last, x);
                }
                break;
            }
            if (!first) sm.rec[(h >> 1) + (h > 20)][lane & 31] = x;
            asm volatile("" ::: "memory");
            if (lane == 0) st_wg(&sm.prog, h + 2);
            if (h == NH - 2) {
                v4i y[NRC / 2];
                unsigned o[NRC / 2];
#pragma unroll
                for (int u = 0; u < NRC / 2; ++u) { y[u] = (&sm.rec[0][0])[u * 64 + lane]; o[u] = place(u * 2 + (lane >> 5)); }
                until(ld_wg(&sm.tref) + TREL);
#pragma unroll
                for (int u = 0; u < NRC / 2; ++u) {
                    if (BNT & 1) asm volatile("s_nop 4\n\tglobal_store_dwordx4 %0, %1, %2 sc1 nt\n\ts_nop 1" :: "v"(o[u]), "v"(y[u]), "s"(ws + WS_R) : "memory");
                    else st_rec(ws + WS_R, o[u], y[u]);
                }
            }
        }
        return;
    }

    {
        asm volatile("" : "+v"(x[0]), "+v"(x[1]));
        auto gather = [&](unsigned w) {
            w <<= mls;
            w |= dppu<0xB1>(w);
            return w | dppu<0x4E>(w);
        };
        auto half = [](unsigned u) { return (float)__builtin_bit_cast(_Float16, (unsigned short)u); };
        const unsigned ml = gather(x[0].w);
        float m = bf_hi(ml), l = half(ml), a[6];
#pragma unroll
        for (int e = 0; e < 3; ++e) { a[2 * e] = bf_lo(x[0][e]); a[2 * e + 1] = bf_hi(x[0][e]); }
        if (both) {
            const unsigned m2 = gather(x[1].w);
            const float mx = fmaxf(m, bf_hi(m2)), w0 = __expf(m - mx), w1 = __expf(bf_hi(m2) - mx);
#pragma unroll
            for (int e = 0; e < 3; ++e) {
                a[2 * e] = a[2 * e] * w0 + bf_lo(x[1][e]) * w1;
                a[2 * e + 1] = a[2 * e + 1] * w0 + bf_hi(x[1][e]) * w1;
            }
            l = l * w0 + half(m2) * w1;
            m = mx;
        }
        const float M = x4max(m), w0 = __expf(m - M);
        l *= w0;
        float c0 = fold16(fold32(a[0] * w0, a[4] * w0), fold32(a[2] * w0, l));
        float c1 = fold16(fold32(a[1] * w0, a[5] * w0), fold32(a[3] * w0, l));
        c0 += dpp<0x128>(c0); c1 += dpp<0x128>(c1);
        c0 += dpp<0x124>(c0); c1 += dpp<0x124>(c1);
        if (g == 3) c1 = M;
        unsigned arr;
        v4f y;
        float mw;
        asm volatile("ds_write_b64 %3, %4\n\tds_add_rtn_u32 %0, %5, %6\n\tds_read_b32 %2, %8\n\tds_read_b128 %1, %7\n\ts_waitcnt lgkmcnt(0)"
                     : "=&v"(arr), "=&v"(y), "=&v"(mw) : "v"(fa[0]), "v"(v2f{c0, c1}), "v"(fa[1]), "v"(one), "v"(fa[2]), "v"(fa[3]) : "memory");
        const float w = __expf(mw - x4max(mw));
        float d = fold16(fold32(y.x * w, y.z * w), fold32(y.y * w, y.w * w));
        d += dpp<0x128>(d);
        const float L = __int_as_float(__builtin_amdgcn_readlane(__float_as_int(d), 6));
        const unsigned o = pack(d * __builtin_amdgcn_rcpf(L), 0.f);
        if (__builtin_amdgcn_readfirstlane(arr) == WPB - 1 && wave < WPB) {
            if (r < 6) *(unsigned short*)((char*)out + oo) = (unsigned short)o;
        }
    }
    if (wave < WPB) {
        unsigned* dg = reinterpret_cast<unsigned*>(ws + WS_T + 655360) + (blockIdx.x * WPB + wave) * 8;
        if (lane == 0) { dg[0] = (unsigned)T0; dg[1] = d_t1; dg[2] = clk(); for (int i = 0; i < 5; ++i) dg[3 + i] = d_o[i]; }
    }
}
__global__ void kivi_end_marker() { asm volatile("kivi_code_end:"); }

__global__ void diag_kernel(const float* ws) {
    const unsigned* dg = reinterpret_cast<const unsigned*>(ws + WS_T + 655360);
    const unsigned ref = dg[0];
    int mn = 1 << 30, x = -(1 << 30), cnt = 0;
    for (int b = 0; b < 8; ++b) {
        const unsigned* e = dg + (b * 256 + threadIdx.x) * 8;
        mn = min(mn, (int)(e[0] - ref));
        const int v = DIAG == 2 ? (int)(e[0] - ref) : DIAG == 3 ? (int)e[3] : DIAG == 4 ? (int)(e[1] - ref - e[3]) : DIAG == 5 ? (int)(e[2] - ref)
                    : DIAG == 12 ? (int)(e[2] - ref - e[7] * SLIP) : DIAG == 13 ? (int)(e[3] - e[7] * SLIP) : DIAG == 6 ? (int)e[4] : DIAG == 7 || DIAG == 14 ? (int)(e[6] - ref) : DIAG == 8 ? (e[5] ? 1000 - (int)e[5] : 0) : DIAG == 9 ? (e[7] ? 1000 - (int)e[7] : 0) : 0;
        x = max(x, v);
        cnt += DIAG == 11 ? (int)e[4] : (int)e[4] > 30;
    }
    __shared__ int smn, smx, sc;
    if (threadIdx.x == 0) { smn = 1 << 30; smx = -(1 << 30); sc = 0; }
    __syncthreads();
    atomicMin(&smn, mn); atomicMax(&smx, x); atomicAdd(&sc, cnt);
    __syncthreads();
    mn = smn; x = smx;
    int X = 0;
    if (DIAG == 2 || DIAG == 7 || DIAG == 14) X = x - mn;
    if (DIAG == 3 || DIAG == 6 || DIAG == 8 || DIAG == 9) X = x;
    if (DIAG == 4 || DIAG == 5 || DIAG == 12) X = x - mn - 20000;
    if (DIAG == 13) X = x;
    if (DIAG == 10) X = sc;
    if (DIAG == 11) X = sc / 2048;
    X = max(0, min(X, 3000));
    while ((int)(clk() - ref) < mn + 25000 + X * 10) __builtin_amdgcn_s_sleep(1);
}
extern "C" void kernel_launch(void* const* d_in, const int* in_sizes, int n_in,
                              void* d_out, int out_size, void* d_ws, size_t ws_size,
                              hipStream_t stream) {
    const __hip_bfloat16* q = (const __hip_bfloat16*)d_in[0];
    const int* qk = (const int*)d_in[1];
    const int* qv = (const int*)d_in[2];
    const __hip_bfloat16* ks = (const __hip_bfloat16*)d_in[3];
    const __hip_bfloat16* kz = (const __hip_bfloat16*)d_in[4];
    const __hip_bfloat16* vs = (const __hip_bfloat16*)d_in[5];
    const __hip_bfloat16* vz = (const __hip_bfloat16*)d_in[6];
    const float scale = (float)(1.0 / sqrt((double)DH));
    Order order;
    for (int b = 0; b < NWG / 2; ++b) {
        const int p = position(b);
        order.g[b] = groupof(p) | groupof(p + 128) << 8 | groupof(p ^ 1) << 16 | groupof((p ^ 1) + 128) << 24;
    }
    kivi_stream_kernel<<<NWG, NT + 128, 0, stream>>>(q, qk, qv, ks, kz, vs, vz, (float*)d_ws, (__hip_bfloat16*)d_out, scale, order);
    diag_kernel<<<1, 256, 0, stream>>>((const float*)d_ws);
}
```

```cpp
#define DIAG 14
#include <hip/hip_runtime.h>
#include <hip/hip_bf16.h>
#include <stdint.h>
#include <utility>

#define TGT 199.25
#define WST 16
#define THR 80
#define PRV 16
#define SLIP 250
#define MAXOFF 3000
#define NSENT 8
#define NR 32
#define TPL 265
#define STG 4
#define PSLEEP 16
#define OFF0 380
#define TGATH 150
#define GAP1 10
#define TREL 110
#define NSLOW 1
#define EXK 7
#define NOSLK 2
#define TMO 100000000u
#define TPC 265
#define PSL 2
#define DST 0
#define BA 8
#define BB 16
#define TGMAX 300
#define PSH 0
#define SS 20
#define HOLD 4
#define VOTES 4
#define HOLDB 8
#define REVT 600
#define VWAIT 30
#define LFAC 4
#define NOSM 2
#define H0 5
#define TGS 203.0
#define SPL 2
#define G21 5
#define EXS(i) ((int)(((i) + 1) * (TGS - TGT) * 100.0 / 256.0 + 0.5) - (int)((i) * (TGS - TGT) * 100.0 / 256.0 + 0.5))
#define CAL0 128
#define PTH (NMEM * (NMEM - 1) * 150)
#define NMEM 2
#define PCL 500
#define TPRB 580
#define DBM 7
#define DBN 2
#define TQA 500
#define PB 1
#define PTL 8
#define EARLY 0
#define LATEB 20
#define MEANF 0
#define PF 100
#define DRY 0
#define TPB 20
#define PRE 0
#define BNT 0
#define PVAR 0
#define PFP 50
#define KD 0
#define ETCH 1
#define TF0 2
#define VT 0
#define TECHO2 900
#define CNT 0
#define QNT 0
#define FT 1
#define TBL 1
#define SPOLL 0
#define THRO 1
#define XT 30
#define XF 8
#define XM 40
#define AH 1
#define DUM 1
#define TLEAD 14
#define GAP2 10
#define NG2 3
#define TECHO 650
#define TQ 300
#define TTAB 1300
#define TXW 200
#define FMASK ~0ull
#define FPOL 0ull
#define RING 12
#define SB() __builtin_amdgcn_sched_barrier(0)
#define PST(i) SB()
#undef PST
#define PST(i) do { SB(); if (DST == i + 1) d_x = clk() - base; SB(); } while (0)

typedef int v4i __attribute__((ext_vector_type(4)));
typedef unsigned v2u __attribute__((ext_vector_type(2)));
typedef float v2f __attribute__((ext_vector_type(2)));
typedef float v4f __attribute__((ext_vector_type(4)));
typedef short v4s __attribute__((ext_vector_type(4)));
typedef __bf16 v2bf __attribute__((ext_vector_type(2)));

constexpr int DH = 192;
constexpr int NH = 32;
constexpr int SKV = 32768;
constexpr int WPB = 8;
constexpr int NT = WPB * 64;
constexpr int NWG = 256;
constexpr int NSL = 8, SLC = 24;
constexpr int RS = NSL * 16;
static_assert(SKV == 2 * 64 * NWG, "two 64-token groups per workgroup and head");
constexpr int NX = 4;
constexpr int NRC = 17;
constexpr size_t WS_R = 262144;
constexpr size_t WS_X = WS_R + (size_t)NRC * NWG * RS;
constexpr size_t WS_T = WS_X + (size_t)NWG * 256;
constexpr unsigned BODY16 = (unsigned)(TGT * 100.0 / 64.0 * 65536.0);
constexpr int slot(int n) { return (int)((n * (BODY16 / 12)) >> 16); }

template <typename F, int... I>
__device__ __forceinline__ void sfor_impl(F&& f, std::integer_sequence<int, I...>) {
    (f(std::integral_constant<int, I>{}), ...);
}
template <int N, typename F>
__device__ __forceinline__ void sfor(F&& f) {
    sfor_impl(f, std::make_integer_sequence<int, N>{});
}

__device__ __forceinline__ float bf_lo(unsigned u) { return __uint_as_float(u << 16); }
__device__ __forceinline__ float bf_hi(unsigned u) { return __uint_as_float(u & 0xffff0000u); }
__device__ __forceinline__ float rb(float x) { return (float)(__bf16)x; }
__device__ __forceinline__ unsigned pack(float lo, float hi) {
    return __builtin_bit_cast(unsigned, __builtin_convertvector(v2f{lo, hi}, v2bf));
}
__device__ __forceinline__ float dot2(unsigned a, unsigned b, float c) {
    return __builtin_amdgcn_fdot2_f32_bf16(__builtin_bit_cast(v2bf, a), __builtin_bit_cast(v2bf, b), c, false);
}
__device__ __forceinline__ void mul4(float (&y)[4], unsigned a, unsigned b, unsigned s0, unsigned s1, unsigned s2, unsigned s3) {
    asm("v_dot2_f32_bf16 %0, %4, %6, 0\n\tv_dot2_f32_bf16 %1, %4, %7, 0\n\t"
        "v_dot2_f32_bf16 %2, %5, %8, 0\n\tv_dot2_f32_bf16 %3, %5, %9, 0\n\ts_nop 2"
        : "=&v"(y[0]), "=&v"(y[1]), "=&v"(y[2]), "=&v"(y[3])
        : "v"(a), "v"(b), "v"(s0), "v"(s1), "v"(s2), "v"(s3));
}
__device__ __forceinline__ unsigned clk() { return (unsigned)__builtin_amdgcn_s_memrealtime(); }
__device__ __forceinline__ unsigned inflight() {
    unsigned x;
    asm volatile("s_getreg_b32 %0, hwreg(HW_REG_IB_STS, 0, 4)" : "=s"(x));
    return x;
}
template <int CTRL>
__device__ __forceinline__ unsigned dppu(unsigned x) {
    return (unsigned)__builtin_amdgcn_mov_dpp((int)x, CTRL, 0xf, 0xf, true);
}
template <int CTRL>
__device__ __forceinline__ float dpp(float x) { return __uint_as_float(dppu<CTRL>(__float_as_uint(x))); }
__device__ __forceinline__ float rowsum16(float x) {
    x += dpp<0x128>(x);
    x += dpp<0x124>(x);
    x += dpp<0x122>(x);
    x += dpp<0x121>(x);
    return x;
}
__device__ __forceinline__ v4i ldstream(const void* p) {
    return __builtin_nontemporal_load(reinterpret_cast<const v4i*>(p));
}

__device__ __forceinline__ unsigned lds(const void* p) { return (unsigned)(size_t)(const __attribute__((address_space(3))) char*)p; }
template <int O, bool STREAM>
__device__ __forceinline__ void to_lds(unsigned d, const void* p, unsigned o) {
    if (STREAM) asm volatile("s_mov_b32 m0, %0\n\ts_nop 4\n\tglobal_load_lds_dwordx4 %1, %2 offset:%3 nt" :: "s"(d - O), "v"(o), "s"(p), "n"(O) : "memory");
    else asm volatile("s_mov_b32 m0, %0\n\ts_nop 4\n\tglobal_load_lds_dwordx4 %1, %2 offset:%3" :: "s"(d - O), "v"(o), "s"(p), "n"(O) : "memory");
}
__device__ __forceinline__ void st_rec(void* p, unsigned o, v4i a) {
    asm volatile("s_nop 4\n\tglobal_store_dwordx4 %0, %1, %2 sc1\n\ts_nop 1" :: "v"(o), "v"(a), "s"(p) : "memory");
}
__device__ __forceinline__ v4i ld_rec(const void* p, unsigned o) {
    v4i x;
    asm volatile("s_nop 4\n\tglobal_load_dwordx4 %0, %1, %2 sc1" : "=v"(x) : "v"(o), "s"(p) : "memory");
    return x;
}
typedef unsigned long long u64;
__device__ __forceinline__ u64 ld_dev(const u64* p) { return __hip_atomic_load(p, __ATOMIC_RELAXED, __HIP_MEMORY_SCOPE_AGENT); }
__device__ __forceinline__ void st_dev(u64* p, u64 v) { __hip_atomic_store(p, v, __ATOMIC_RELAXED, __HIP_MEMORY_SCOPE_AGENT); }
__device__ __forceinline__ unsigned ld_wg(const unsigned* p) { return __hip_atomic_load(p, __ATOMIC_RELAXED, __HIP_MEMORY_SCOPE_WORKGROUP); }
__device__ __forceinline__ u64 ld_wg64(const u64* p) { return __hip_atomic_load(p, __ATOMIC_RELAXED, __HIP_MEMORY_SCOPE_WORKGROUP); }
__device__ __forceinline__ void st_wg(unsigned* p, unsigned v) { __hip_atomic_store(p, v, __ATOMIC_RELAXED, __HIP_MEMORY_SCOPE_WORKGROUP); }
__device__ __forceinline__ float fold32(float a, float b) {
    auto s = __builtin_amdgcn_permlane32_swap(__float_as_uint(a), __float_as_uint(b), false, false);
    return __uint_as_float(s[0]) + __uint_as_float(s[1]);
}
__device__ __forceinline__ float fold16(float a, float b) {
    auto s = __builtin_amdgcn_permlane16_swap(__float_as_uint(a), __float_as_uint(b), false, false);
    return __uint_as_float(s[0]) + __uint_as_float(s[1]);
}
__device__ __forceinline__ float x4max(float x) {
    float m;
    asm("s_nop 1\n\tv_max_f32_dpp %1, %1, %1 row_ror:4 row_mask:0xf bank_mask:0xf\n\t"
        "s_nop 1\n\tv_max_f32_dpp %1, %1, %1 row_ror:8 row_mask:0xf bank_mask:0xf\n\t"
        "s_nop 1\n\tv_max_f32_dpp %1, %1, %1 row_bcast:15 row_mask:0xa bank_mask:0xf\n\t"
        "s_nop 1\n\tv_max_f32_dpp %1, %1, %1 row_bcast:31 row_mask:0xc bank_mask:0xf\n\t"
        "s_nop 0\n\tv_readlane_b32 %0, %1, 63\n\ts_nop 1" : "=s"(m), "+v"(x));
    return m;
}

struct __attribute__((aligned(16))) Smem {
    v4i ksz[2][3][NT];
    v4i par[NX][64];
    v4i qt[NH * 24];
    v4i vsz[2][NT];
    v4i rec[NRC - 1][RS / 4];
    float stage[2][WPB][DH];
    unsigned short p[NH][WPB][16];
    unsigned ml[NH];
    float fin[WPB + 1][32];
    u64 epoch;
    unsigned desc[2][2][2 * NH];
    unsigned ready;
    unsigned have;
    unsigned tref;
    unsigned slip;
    unsigned vote[2 * NH];
    unsigned late[WPB];
    unsigned req;
    v2u t0;
    unsigned done;
    unsigned prog;
    unsigned fcnt;
    unsigned dsv;
    unsigned tick;
    unsigned quiet;
    unsigned cnt[NH + NH / 4];
};

__host__ __device__ constexpr int position(int b) { return (b & ~7) | ((b & 3) << 1) | ((b >> 2) & 1); }
constexpr int groupof(int p) {
    const int r0 = p < 85 ? 0 : p < 171 ? 85 : 171, na = p < 171 ? 43 : 42, t = p - r0;
    return r0 + (t < 64 ? ((t >> 4) & 1) * na + (t >> 5) * 16 + (t & 15) : t < na + 32 ? t - 32 : t);
}
struct Order { unsigned g[NWG / 2]; };

__global__ __launch_bounds__(NT + 128, 3) void kivi_stream_kernel(
    const __hip_bfloat16* __restrict__ q, const int* __restrict__ qk, const int* __restrict__ qv,
    const __hip_bfloat16* __restrict__ ks, const __hip_bfloat16* __restrict__ kz,
    const __hip_bfloat16* __restrict__ vs, const __hip_bfloat16* __restrict__ vz,
    float* __restrict__ ws, __hip_bfloat16* __restrict__ out, float scale,
    Order order) {
    __shared__ Smem sm;

    u64 ka0;
    unsigned ka1, ka2;
    asm volatile("s_load_dwordx2 %0, %3, 0x18\n\ts_load_dword %1, %3, %4 offset:0x4c\n\ts_load_dword %2, %3, 0x40"
                 : "=&s"(ka0), "=&s"(ka1), "=&s"(ka2) : "s"(__builtin_amdgcn_kernarg_segment_ptr()), "s"((blockIdx.x & 127) * 4));
    const u64 T0 = __builtin_amdgcn_s_memrealtime();
    const int tid = threadIdx.x;
    typedef const __attribute__((address_space(1))) int* gptr;
    u64 code;
    int codelen;
    asm volatile("s_getpc_b64 %0\nkivi_code_begin:\n\ts_mov_b32 %1, kivi_code_end-kivi_code_begin-4" : "=s"(code), "=s"(codelen));
    const gptr wp = (gptr)(code + min((unsigned)tid * 128u, (unsigned)codelen));
    const int warm = CNT ? __builtin_nontemporal_load(wp) : *wp;
    const int win = blockIdx.x >> 7;
    auto blockof = [&](int it) { return it >> 2 == 10 ? it : (it & ~3) + 2 * win + ((it >> 1) & 1); };
    auto halfof = [&](int it) { return (it >> 2 == 10 ? win : it) & 1; };
    auto group = [&](int it, int x) { return blockof(it) * NWG + ((x >> halfof(it) * 8) & 255); };
    unsigned pb = blockof(tid >> 3) * NWG, ph = halfof(tid >> 3) * 8;
    const int wave = __builtin_amdgcn_readfirstlane(tid >> 6);
    asm volatile("" : "+v"(pb), "+v"(ph));
    SB();
    asm volatile("s_waitcnt lgkmcnt(0)" : "+s"(ka0), "+s"(ka1), "+s"(ka2));
    const int gx = ka1 & 0xffff, px = ka1 >> 16;
    const unsigned pg = pb + ((ka1 >> ph) & 255), po = pg * (DH * 2) + (tid & 7) * 16;
    if (wave < WPB) {
        to_lds<0, true>(lds(&sm.ksz[0][0][wave * 64]), (const void*)ka0, po);
        to_lds<128, true>(lds(&sm.ksz[0][1][wave * 64]), (const void*)ka0, po);
        to_lds<256, true>(lds(&sm.ksz[0][2][wave * 64]), (const void*)ka0, po);
    }
    SB();
    u64 tb64 = __builtin_amdgcn_s_memrealtime() - LATEB;
    tb64 += min(max((int)(tb64 - T0) - XT, 0) * XF / 8, XM);
    const unsigned tb = (unsigned)tb64;
    u64* tstart = reinterpret_cast<u64*>(ws + WS_T);
    u64* tbase = tstart + 3 * NWG + NR * 32 + 64 * 32;
    if (TBL && blockIdx.x >= NWG - 8 && tid >= NT && tid < NT + 8)
        st_rec(tbase + (blockIdx.x & 7) * 32, (tid & 7) * 16, v4i{(int)tb, (int)(tb64 >> 32), (int)tb, (int)(tb64 >> 32)});
    if (tid == NT) st_dev(tstart + blockIdx.x, tb64);
    int wq = 0;
    if (QNT && tid >= NT && tid < NT + 96) wq = __builtin_nontemporal_load((const int*)q + (tid - NT) * 32);
    if (wave == WPB + 1 && (!FT || blockIdx.x < 120 || ((blockIdx.x >> 3) & 3) == 3)) {
        const unsigned k = blockIdx.x >> 3, k4 = min(k, 7u) * 4;
        const u64 p = ((const __attribute__((address_space(4))) u64*)__builtin_amdgcn_kernarg_segment_ptr())[(0x13365444u >> k4) & 7u];
        unsigned x;
        asm volatile("s_load_dword %0, %1, 0x0\n\ts_waitcnt lgkmcnt(0)" : "=s"(x) : "s"(p + (((k - ((0x74443000u >> k4) & 7u)) << 21) + (FT ? k : blockIdx.x & 7) * 256)) : "memory");
    }
    SB();
    const int lane = tid & 63;
    const int r = lane & 15;
    const int g = lane >> 4;
    const int pos = position(blockIdx.x);
    auto tagof = [](u64 e, int j) {
        e *= 0x9e3779b97f4a7c15ull;
        return (unsigned)(j == 3 ? (e ^ (e >> 29)) >> 8 : e >> (j * 20)) << 8;
    };
    auto until = [&](unsigned t) { while ((int)(clk() - t) < 0) __builtin_amdgcn_s_sleep(1); };
    auto behind = [&]() {
        const unsigned tick = ld_wg(&sm.tick), t = clk();
        while (ld_wg(&sm.tick) == tick && clk() - t < 100u) __builtin_amdgcn_s_sleep(1);
    };
    const int quart = (pos >> 5) & 3, tau = (-((pos >> 1) + quart)) & 15;
    auto shared = [&](int b) { return SPL == 4 || ((b ^ quart) & 1) == 0; };
    auto job = [&](int t, int b, u64 pol, u64 ok, unsigned side) {
        const int bd = tau == 10 || (t == 0 && shared(b)) ? b : 2 * tau + (SPL == 4 ? 0 : quart & 1);
        const int reg = (shared(b) ? t : 1) * 32 + (group(2 * bd, pos & 1 ? px : gx) * 3 >> 13) * 4;
        return (((ok >> reg) & 1u && (t || b >= H0) ? side ^ (unsigned)(pol >> reg) : quart >> 1) ^ pos) & 1u;
    };
    auto describe = [&](int t, int it, int j, u64 pol, u64 ok, unsigned side) {
        const int b = it >> 1, n = it * 2 + j;
        const bool tr = it >> 2 == tau && tau > 0, sh = tr && shared(b);
        const unsigned jb = job(t, tr ? b : 2 * tau, pol, ok, side);
        const unsigned x = tr ? ((sh ? j : jb) ^ pos) & 1u : 0u;
        const unsigned gr = group(it, x ? px : gx), c = 6 * gr + 3;
        const unsigned flip = sh ? 0u : ((unsigned)(pol >> t * 32) >> (c >> 14) * 4) & 1u;
        const bool early = !t && b < H0;
        const unsigned okt = (unsigned)(ok >> t * 32);
        const bool known = (okt >> (it >> 2) * 3 / 8 * 4) & (okt >> ((it >> 2) * 3 + 2) / 8 * 4) & 1u;
        const unsigned ex = (early || b < 2 || !known ? (n & 3) == 0 ? EXS(0) : (n & 3) == 1 ? EXS(1) : (n & 3) == 2 ? EXS(2) : EXS(3) : 0) + (early || known ? 0 : n & 1)
                          + (n >= 84 && n < 87 ? G21 : 0) + (t ? n == 0 ? GAP1 : n == NG2 && ((pol ^ (pol >> 32)) & 0x111111u) ? GAP2 : 0 : n >= 3 && n < 3 + 4 * NSLOW ? EXK : 0);
        return gr * (64 * DH * 4) | ex << 4 | (early || known || sh ? 0u : 8u) | x << 2 | flip << 1
             | (((sh ? jb : j ^ win ^ flip) ^ (c >> 8) ^ (c >> 15)) & 1u);
    };

    const int mh = (blockIdx.x / 16 * 2 + (blockIdx.x & 7) / 4 + 20) % NH, msl = (blockIdx.x & 3) * 2 + ((blockIdx.x >> 3) & 1);
    const bool both = mh >> 1 == 10;
    v4i x[2] = {};
    unsigned fa[4], mls, one, oo;
    auto prepare = [&]() {
        fa[0] = lds(&sm.fin[wave][g == 3 ? 24 + (r & 3) * 2 : (r & 3) * 6 + g * 2]);
        fa[1] = lane == 0 ? lds(&sm.fcnt) : lds(&sm.vote[lane]);
        fa[2] = lds(&sm.fin[lane >> 3][(lane & 7) * 4]);
        fa[3] = lds(&sm.fin[lane >> 3][25]);
        mls = (lane & 3) * 8;
        one = tid < NT && lane == 0;
        oo = (mh * DH + msl * SLC + r * 4 + g) * 2;
        asm volatile("" : "+v"(fa[0]), "+v"(fa[1]), "+v"(fa[2]), "+v"(fa[3]), "+v"(mls), "+v"(one), "+v"(oo));
    };
    unsigned d_t1 = 0, d_o[5] = {};
    if (__builtin_expect(wave < WPB, 1)) {
    const int ww = (wave & 3) * 2 + (wave >> 2);
    const int ti = ww * 4 + g;
    const int tev = ti + (ti >= 11 ? 10 : 0) + (ti >= 22 ? 11 : 0), tod = ti + 11 + (ti >= 10 ? 11 : 0) + (ti >= 21 ? 10 : 0);
    const unsigned offe = tev * (DH * 4) + r * 16, offo = tod * (DH * 4) + r * 16;
    unsigned descv[2], dsc[4];
    auto lanes = [=](unsigned d) { return (d & 1u ? offo : offe) + (d & ~0x3fffu); };
    auto fetch = [&](int t) {
        while (__builtin_amdgcn_readfirstlane(ld_wg(&sm.ready)) == 0) __builtin_amdgcn_s_sleep(1);
        asm volatile("" ::: "memory");
#pragma unroll
        for (int j = 0; j < 2; ++j) {
            descv[j] = sm.desc[t][j][lane];
            if (ww >= WPB / 2 && (descv[j] & 8u)) descv[j] ^= 3u;
        }
    };
    const char* dummy = reinterpret_cast<const char*>(qv) + (size_t)(blockIdx.x * WPB + wave) * 4864;

    v4i buf[RING];
    auto burst = [&](const char* p, int i, unsigned o) {
#pragma unroll
        for (int e = 0; e < 3; ++e) { buf[i * 3 + e] = ldstream(p + o + e * 256); SB(); }
    };

    const unsigned phase = ((pos & (NWG / 2 - 1)) * (BODY16 / 512) + ww * WST * (BODY16 / (12 * 256))) >> 16;
    unsigned base = tb + phase + PSH;
    unsigned sched = 0, off = 0, offv = 0;
    int prev = 0, now = 0, hold = 0, big = 0, body = 0;
    unsigned hist = 0;
    unsigned vc = 0;
    int d_l = 0, d_n = 0, d_f = 0, d_s = 0, d_nb = 0;
    unsigned d_bar = 0, d_x = 0;
    auto pace = [&](int due) {
        int now;
        while ((now = (int)(clk() - base)) < due) __builtin_amdgcn_s_sleep(1);
        return now;
    };
    auto follow = [&](unsigned o2) {
        hold = o2 - off > SLIP / 2 ? HOLDB : o2 != off ? HOLD : max(hold - 1, 0);
        big = o2 - off > SLIP / 2 ? HOLDB : max(big - 1, 0);
        if ((int)(o2 - off) >= SLIP) ++d_nb;
        base += o2 - off;
        off = o2;
    };
    auto pacen = [&](auto N) {
        constexpr int n = decltype(N)::value;
        follow(__builtin_amdgcn_readfirstlane(offv));
        now = pace(slot(n));
        vc = inflight();
        hist <<= 1;
        if (__builtin_expect(vc > 3, 0)) {
            hist |= 1u;
            asm volatile("s_waitcnt vmcnt(3)");
            now = (int)(clk() - base);
            if (now > slot(n) + THR && prev < PRV && big == 0 && off + SLIP <= MAXOFF && body >= NOSLK
                && (int)(base + now - __builtin_amdgcn_readfirstlane(ld_wg(&sm.quiet))) >= 0) {
                if (lane == 0 && __hip_atomic_fetch_add(&sm.vote[body], 1u, __ATOMIC_RELAXED, __HIP_MEMORY_SCOPE_WORKGROUP) + 1 >= VOTES)
                    __hip_atomic_fetch_max(&sm.slip, off + SLIP, __ATOMIC_RELAXED, __HIP_MEMORY_SCOPE_WORKGROUP);
                unsigned o2;
                while ((o2 = __builtin_amdgcn_readfirstlane(ld_wg(&sm.slip))) == off && (int)(clk() - base) < now + VWAIT) {}
                if (o2 != off) { follow(o2); now = pace(slot(n)); }
            }
            if (hold == 0 && off + SS <= MAXOFF && body >= NOSM && body % NH != 21 && __builtin_popcount(hist & DBM) >= DBN && lane == 0) st_wg(&sm.req, off + SS);
        }
        if (d_n >= BA && d_n < BB) d_l = max(d_l, now - slot(n));
        if (now - slot(n) > 30 && d_f == 0) d_f = d_n + 1;
        if (off > 0 && d_s == 0) d_s = d_n + 1;
        ++d_n;
    };
    auto check = [&](auto N) {
        constexpr int n = decltype(N)::value;
        prev = now - slot(n);
        if (lane == 0) st_wg(&sm.late[wave], (unsigned)prev);
    };
    auto getslip = [&]() { offv = __hip_atomic_load(&sm.slip, __ATOMIC_RELAXED, __HIP_MEMORY_SCOPE_WORKGROUP); };
    auto arrive = [&](int i) {
        asm volatile("" ::: "memory");
        if (lane == 0) __hip_atomic_fetch_add(&sm.cnt[i], 1u, __ATOMIC_RELAXED, __HIP_MEMORY_SCOPE_WORKGROUP);
    };
    auto next_body = [&]() {
        const unsigned s2 = sched + BODY16;
        base += (s2 >> 16) - (sched >> 16);
        sched = s2;
        ++body;
        if (DIAG == 14 && body == BA) d_bar = base - (sched >> 16) - d_nb * SLIP;
    };

    {
        to_lds<0, false>(lds(&sm.qt[wave * 64]), q, tid * 16);
        if (wave < 4) to_lds<0, false>(lds(&sm.qt[NT + wave * 64]), q, (NT + tid) * 16);
        PST(1); pace(slot(3)); SB();
        to_lds<0, true>(lds(&sm.ksz[1][0][wave * 64]), kz, po);
        to_lds<128, true>(lds(&sm.ksz[1][1][wave * 64]), kz, po);
        to_lds<256, true>(lds(&sm.ksz[1][2][wave * 64]), kz, po);
        SB();
        const bool isk = lane < 48, isz = isk ? lane >= 24 : lane >= 56;
        const char* par = (const char*)(isk ? isz ? kz : ks : isz ? vz : vs) + (isk ? lane % 24 : lane & 7) * 16
                        + group(tau * 4 + (wave & 3), px) * (isk ? DH * 2 : 128);
        asm volatile("" : "+v"(par));
        dsc[0] = describe(0, 0, 0, 0u, 0u, 0u);
        dsc[1] = describe(0, 0, 1, 0u, 0u, 0u);
        PST(2); pace(slot(6)); SB();
        to_lds<0, true>(lds(&sm.vsz[0][wave * 64]), vs, pg * 128 + (tid & 7) * 16);
        to_lds<0, true>(lds(&sm.vsz[1][wave * 64]), vz, pg * 128 + (tid & 7) * 16);
        if (wave >= WPB - NX)
            asm volatile("s_mov_b32 m0, %0\n\ts_nop 0\n\tglobal_load_lds_dwordx4 %1, off nt" :: "s"(lds(&sm.par[wave & 3][0])), "v"(par) : "memory");
        PST(3); pace(slot(8) + KD); SB();
        burst((const char*)qk, 0, lanes(dsc[0]));
        PST(4);
        descv[0] = describe(0, lane, 0, 0u, 0u, 0u);
        descv[1] = describe(0, lane, 1, 0u, 0u, 0u);
        asm volatile("" : "+v"(descv[0]), "+v"(descv[1]));
        if (THRO) asm volatile("" :: "v"(warm));
        PST(5); pace(slot(11) + KD); SB();
        burst((const char*)qk, 1, lanes(dsc[1]));
        PST(6);
    }
    {
        unsigned t0;
        for (;;) {
            const u64 t = ld_wg64((const u64*)&sm.t0);
            t0 = __builtin_amdgcn_readfirstlane((unsigned)t);
            if (t0 == ~__builtin_amdgcn_readfirstlane((unsigned)(t >> 32)) && t0 - tb + 1100u < 2200u) break;
            t0 = tb;
            if (clk() - tb > 1000u) break;
        }
        PST(7);
        PST(8);
        d_bar = clk();
        base = t0 + OFF0 + phase;
    }

    const v2f huge = {0x1p127f, 0x1p127f};

    for (int S = 0; S < NH; ++S) {
        v2f kz2[6];
        unsigned slo[6], shi[6];
        v2u qq[3];
        const char* nsup = (const char*)(S + 1 < NH ? qk : qv);
        const int head = S >> 1 == 10 ? S : (S & ~1) + win;
        float mylogit = 0.f;
        float dot = 0.f;
        sfor<12>([&](auto N) {
            constexpr int n = decltype(N)::value;
            constexpr int i = n / 3, j = n % 3;
            if (n == 6 && (S == H0 - 1 || S == NH - 1)) fetch(S == NH - 1);
            if (j == 0) {
                const unsigned d = dsc[i ^ 2] = __builtin_amdgcn_readlane(descv[i & 1], (2 * S + 1 + (i >> 1)) & 63);
                const unsigned o = lanes(d);
                base += (d >> 4) & 31u;
                pacen(N);
                SB();
                burst(i < 2 ? (const char*)qk : nsup, i ^ 2, o);
                SB();
                if (tid == NT - 1) st_wg(&sm.tick, S * 4 + i + 1);
                check(N);
                if (n == 0 && S == 0) asm volatile("s_waitcnt vmcnt(9)\n\ts_barrier" ::: "memory");
            }
            if (j == 2) getslip();
            if (i % 2 == 0 || ((dsc[i] ^ dsc[i ^ 1]) & 4u)) {
                const int it = S * 2 + (i >> 1);
                const bool other = dsc[i] & 4u;
                const v2u* pk = reinterpret_cast<const v2u*>(other ? &sm.par[it & 3][j * 8] : &sm.ksz[0][j][it * 8]) + r;
                const v2u a = pk[0], b = pk[other ? 48 : 3 * NT * 2];
                slo[j * 2 + 0] = a.x & 0xffffu; shi[j * 2 + 0] = a.x & 0xffff0000u;
                slo[j * 2 + 1] = a.y & 0xffffu; shi[j * 2 + 1] = a.y & 0xffff0000u;
                kz2[j * 2 + 0] = v2f{bf_lo(b.x), bf_hi(b.x)} * -0x1p-22f;
                kz2[j * 2 + 1] = v2f{bf_lo(b.y), bf_hi(b.y)} * -0x1p-22f;
            }
            if (i == 0) qq[j] = reinterpret_cast<const v2u*>(&sm.qt[head * 24])[j * 16 + r];
            if (j == 0) dot = 0.f;
            {
                const v4i x = buf[n % RING];
                const v2f t0 = __builtin_elementwise_fma(v2f{__int_as_float(x.x), __int_as_float(x.y)}, huge, kz2[j * 2]);
                const v2f t1 = __builtin_elementwise_fma(v2f{__int_as_float(x.z), __int_as_float(x.w)}, huge, kz2[j * 2 + 1]);
                float y[4];
                mul4(y, pack(t0.x, t0.y), pack(t1.x, t1.y), slo[j * 2], shi[j * 2], slo[j * 2 + 1], shi[j * 2 + 1]);
                dot = dot2(pack(y[0], y[1]), qq[j].x, dot);
                dot = dot2(pack(y[2], y[3]), qq[j].y, dot);
            }
            if (j == 2) {
                const float d = rowsum16(dot);
                if ((r & 3) == (i ^ ((dsc[i] >> 1) & 1))) mylogit = d;
            }
            asm volatile("" : "+v"(dot), "+v"(mylogit));
            SB();
        });
        if (r < 4) sm.p[S][wave][g * 4 + r] = (unsigned short)pack(rb(mylogit * 0x1p22f) * scale, 0.f);
        if (!THRO) asm volatile("" :: "v"(warm));
        if ((S & 3) == 3) arrive(NH + (S >> 2));
        if (S == NH - 3 && tid == 0) st_wg(&sm.done, 1u);
        next_body();
    }

    for (int S = 0; S < NH; ++S) {
        v2u P;
        unsigned vsl, vsh, vzf;
        v4f acc = {0.f, 0.f, 0.f, 0.f};
        float y0[12];
        unsigned b0[12];
        const bool more = S + 1 < NH;
        unsigned sl, sh;
        v2u A[12];
        v2f z2;
        sfor<12>([&](auto N) {
            constexpr int n = decltype(N)::value;
            constexpr int i = n / 3, j = n % 3;
            if (j == 0) {
                const unsigned d = dsc[i ^ 2] = __builtin_amdgcn_readlane(descv[i & 1], (2 * S + 1 + (i >> 1)) & 63);
                unsigned o = lanes(d);
                const char* src = (const char*)qv;
                if (i >= 2 && !more) {
                    o = DUM ? 0 : i * 768;
                    src = !DUM ? dummy : i == 3 && wave == 0 ? (const char*)out + min(mh, NH - 2) * (DH * 2) : (const char*)ws + (i & 1) * (15 << 18) + blockIdx.x * 768;
                }
                if (i < 2 || more) base += (d >> 4) & 31u;
                if (i < 2 || more) pacen(N);
                SB();
                burst(src, i ^ 2, o);
                SB();
                if (tid == NT - 1) st_wg(&sm.tick, 4 * NH + S * 4 + i + 1);
                check(N);
            }
            if (n == 0) {
                const unsigned need = S < 2 ? 1 : S;
                while (__builtin_amdgcn_readfirstlane(ld_wg(&sm.prog)) < need) {}
                asm volatile("" ::: "memory");
                P = *reinterpret_cast<const v2u*>(&sm.p[S][wave][g * 4]);
                P.x = __builtin_amdgcn_alignbit(P.x, P.x, (dsc[0] & 2u) << 3);
                P.y = __builtin_amdgcn_alignbit(P.y, P.y, (dsc[2] & 2u) << 3);
                const unsigned d3 = __builtin_amdgcn_readlane(descv[1], 2 * S + 1);
                const unsigned x = (((dsc[0] & 7u) | (dsc[1] & 7u) << 8) | ((dsc[2] & 7u) << 16 | (d3 & 7u) << 24)) >> (r & 3) * 8;
                const int it = S * 2 + ((r >> 1) & 1);
                const unsigned short* pv = reinterpret_cast<const unsigned short*>(x & 4u ? &sm.par[it & 3][48] : &sm.vsz[0][it * 8]) + (x & 1u ? tod : tev);
                vsl = pv[0];
                vsh = vsl << 16;
                vzf = __float_as_uint(bf_lo(pv[x & 4u ? 64 : NT * 8]) * -0x1p-22f);
            }
            if (j == 2) getslip();
            if (j == 0) {
                sl = dppu<0x55 * i>(vsl);
                sh = dppu<0x55 * i>(vsh);
                const float zb = __uint_as_float(dppu<0x55 * i>(vzf));
                z2 = v2f{zb, zb};
            }
            float y[4];
            {
                const v4i x = buf[n];
                const v2f t0 = __builtin_elementwise_fma(v2f{__int_as_float(x.x), __int_as_float(x.y)}, huge, z2);
                const v2f t1 = __builtin_elementwise_fma(v2f{__int_as_float(x.z), __int_as_float(x.w)}, huge, z2);
                mul4(y, pack(t0.x, t0.y), pack(t1.x, t1.y), sl, sh, sl, sh);
            }
#pragma unroll
            for (int e = 0; e < 4; ++e) {
                const int idx = j * 4 + e;
                if (i % 2 == 0) {
                    y0[idx] = y[e];
                    asm volatile("" : "+v"(y0[idx]));
                    if (i == 2) {
                        A[idx] = v2u{r == idx ? P.x : 0u, r == idx ? P.y : 0u};
                        if (AH) asm volatile("" : "+v"(A[idx]));
                    }
                } else if (i == 1) {
                    b0[idx] = pack(y0[idx], y[e]);
                    asm volatile("" : "+v"(b0[idx]));
                } else {
                    const v2u B = {b0[idx], pack(y0[idx], y[e])};
                    acc = __builtin_amdgcn_mfma_f32_16x16x16bf16_1k(__builtin_bit_cast(v4s, A[idx]), __builtin_bit_cast(v4s, B), acc, 0, 0, 0);
                }
            }
            if (n == 11) {
                if (S == NH - 2 && tid == 0) sm.tref = base + slot(12) + slot(3);
                if (S == NH - 3 && tid == 0) st_wg(&sm.done, 2u);
                if (lane < 48) *reinterpret_cast<v4f*>(&sm.stage[S & 1][wave][lane * 4]) = acc;
                arrive(S);
            }
            SB();
        });
        next_body();
    }
    if (DST >= 21) d_x = sm.dsv;
    d_t1 = clk();
    d_o[0] = off; d_o[1] = DST ? d_x : d_l; d_o[2] = d_f; d_o[3] = d_bar - phase; d_o[4] = DIAG >= 12 ? d_nb : d_s;
    {
        const unsigned tag = tagof(sm.epoch, lane & 3);
        const float* src = ws + WS_R + ((size_t)((both ? 0 : mh & 1) * (NWG / 2) + wave * 16) * NRC + (mh >> 1) + (mh > 20)) * RS + msl * 16;
        const unsigned so = (lane >> 2) * (NRC * RS * 4) + (lane & 3) * 16;
        prepare();
        until(base - slot(12) + slot(3) - phase + (wave - (WPB - 1)) * STG + (mh >= NH - 2 ? TPL : both ? TPC - TPB : TPC));
        for (;;) {
            x[0] = ld_rec(src, so);
            x[1] = v4i{0, 0, 0, (int)tag};
            if (both) x[1] = ld_rec(src + NWG / 2 * NRC * RS, so);
            asm volatile("s_waitcnt vmcnt(0)" : "+v"(x[0]), "+v"(x[1]));
            if (__all((((unsigned)x[0].w ^ tag) | ((unsigned)x[1].w ^ tag)) < 256u) || clk() - tb >= TMO) break;
            __builtin_amdgcn_s_sleep(PSL);
        }
#pragma unroll
        for (int d = 5; d >= 0; --d) asm volatile("" :: "v"(buf[d]));
        x[0].w ^= tag;
        x[1].w ^= tag;
    }
    } else
    if (wave == WPB) {
        const int l = lane;
        u64* echo = tstart + NWG;
        u64* gofs = tstart + 3 * NWG;
        if (l == 0) {
            if (blockIdx.x < NR) st_dev(gofs + blockIdx.x * 32, tb64 - 100000ull);
            if (blockIdx.x == NR) st_dev(gofs + 16, tb64 - 100000ull);
            sm.slip = 0;
            sm.req = 0;
            sm.done = 0;
            sm.prog = 0;
            sm.ready = 0;
            sm.tick = 0;
            sm.quiet = tb;
            sm.fcnt = 0;
            sm.epoch = tb64;
            sm.have = blockIdx.x == 0;
        }
        if (l < NH + NH / 4) sm.cnt[l] = 0;
        sm.vote[l] = 0;
        until(tb + TGATH);
        unsigned ld;
        if (TBL == 2) {
            for (;;) {
                u64 x[8];
                asm volatile("s_load_dwordx2 %0, %8, 0x0 glc\n\ts_load_dwordx2 %1, %8, 0x100 glc\n\ts_load_dwordx2 %2, %8, 0x200 glc\n\t"
                             "s_load_dwordx2 %3, %8, 0x300 glc\n\ts_load_dwordx2 %4, %8, 0x400 glc\n\ts_load_dwordx2 %5, %8, 0x500 glc\n\t"
                             "s_load_dwordx2 %6, %8, 0x600 glc\n\ts_load_dwordx2 %7, %8, 0x700 glc\n\ts_waitcnt lgkmcnt(0)"
                             : "=&s"(x[0]), "=&s"(x[1]), "=&s"(x[2]), "=&s"(x[3]), "=&s"(x[4]), "=&s"(x[5]), "=&s"(x[6]), "=&s"(x[7]) : "s"(tbase) : "memory");
                bool all = true;
                ld = 0;
#pragma unroll
                for (int u = 0; u < 8; ++u) {
                    const u64 dt = x[u] - tb64 + 1000ull;
                    if (dt < 2000ull) ld = max(ld, (unsigned)dt); else all = false;
                }
                if (all || clk() - tb > (unsigned)TGMAX) break;
            }
        } else {
            for (;;) {
                const u64 dt = ld_dev(TBL ? tbase + (l & 7) * 32 : tstart + NWG - 8 + (l & 7)) - tb64 + 1000ull;
                ld = dt < 2000ull ? (unsigned)dt : 0u;
                if (__all(dt < 2000ull) || clk() - tb > (unsigned)TGMAX) break;
            }
            ld = max(ld, dppu<0x124>(ld));
            ld = max(ld, dppu<0x122>(ld));
            ld = max(ld, dppu<0x121>(ld));
            ld = __builtin_amdgcn_readfirstlane(ld);
        }
        const u64 t64 = ld ? tb64 + ld - 1000ull : tb64;
        if (l == 0) sm.t0 = v2u{(unsigned)t64, ~(unsigned)t64};
        if (l == 0) sm.dsv = clk() - tb;
        u64 to[NWG / 64];
        bool have = blockIdx.x == 0;
        auto chores = [&](bool all) {
            if (blockIdx.x == 0) {
                u64 x[NWG / 64];
#pragma unroll
                for (int u = 0; u < NWG / 64; ++u) x[u] = ld_dev(tstart + u * 64 + l);
#pragma unroll
                for (int u = 0; u < NWG / 64; ++u) {
                    if (all || __any(x[u] != to[u])) st_rec(echo + u * 128, l * 16, v4i{(int)(unsigned)x[u], (int)(unsigned)(x[u] >> 32), (int)tb, (int)(unsigned)(tb64 >> 32)});
                    to[u] = x[u];
                }
            } else if (!have && !all) {
                v4i x = ld_rec(echo + blockIdx.x * 2, 0);
                asm volatile("s_waitcnt vmcnt(0)" : "+v"(x));
                if ((unsigned)x.x == tb && (unsigned)x.y == (unsigned)(tb64 >> 32)) {
                    have = true;
                    if (l == 0) sm.epoch = (u64)(unsigned)x.w << 32 | (unsigned)x.z;
                    asm volatile("s_waitcnt lgkmcnt(0)" ::: "memory");
                    if (l == 0) st_wg(&sm.have, 1u);
                }
            }
        };
        if (EARLY) chores(true);
        asm volatile("s_waitcnt lgkmcnt(0)\n\ts_barrier" ::: "memory");
        asm volatile("" :: "v"(warm), "v"(wq));
        if (!EARLY) chores(true);
        if (!ETCH) {
            until(tb + TECHO);
            behind();
            chores(false);
        }
        const u64* mine = NR > 1 ? gofs + ((blockIdx.x >> 3) % NR) * 32 : gofs + 16;
        unsigned sent = 0, stage = 0, tsh = clk();
        int nt = blockIdx.x < 200, spin = 0;
        while (stage < 2) {
            if (PB > 1 && have) __builtin_amdgcn_s_sleep(PSLEEP);
            behind();
            unsigned tch = 0;
            if (nt < 25) {
                const unsigned pg = (blockIdx.x >> 3) + 32 * (nt % 12);
                const char* tp = (nt < 12 ? (const char*)qk : nt < 24 ? (const char*)qv : (const char*)ws) + ((size_t)pg << 21);
                const unsigned vp = (unsigned)((size_t)tp >> 21);
                const bool first = (vp & (PTL - 1)) == 0 || min(pg & 127u, 128u - (pg & 127u)) < (vp & -vp);
                tp += (((first ? blockIdx.x >> 3 : blockIdx.x) * 29 + nt) & 255) * 256;
                if (nt == 24 && blockIdx.x >= 16) tp = (const char*)out + min((pg - 2) * 4096, NH * DH * 2 - 4u);
                if (ld_wg(&sm.tick) + (first ? TLEAD + 4 : TLEAD) >= (unsigned)max(nt / 12 * 128 + nt % 12 * 4 / 3 * 8 - nt / 24 * 32, TLEAD + TF0)) {
                    unsigned x;
                    if (nt < 24 && !first) { if (VT) asm volatile("s_nop 4\n\tglobal_load_dword %0, %1, %2 nt" : "+v"(tch) : "v"(0), "s"(tp) : "memory"); }
                    else if (nt < 24 || blockIdx.x < 48) asm volatile("s_load_dword %0, %1, 0x0\n\ts_waitcnt lgkmcnt(0)" : "=s"(x) : "s"(tp) : "memory");
                    ++nt;
                }
            }
            u64 gd;
            if (SPOLL) asm volatile("s_load_dwordx2 %0, %1, 0x0 glc\n\ts_waitcnt lgkmcnt(0)" : "=s"(gd) : "s"(mine) : "memory");
            else gd = ld_dev(mine);
            gd -= t64;
            asm volatile("s_waitcnt vmcnt(0)" : "+v"(tch));
            const unsigned go = gd <= (u64)MAXOFF ? (unsigned)gd : 0u;
            const unsigned own = ld_wg(&sm.slip);
            if (go > own && l == 0) __hip_atomic_fetch_max(&sm.slip, go, __ATOMIC_RELAXED, __HIP_MEMORY_SCOPE_WORKGROUP);
            if (own <= go) tsh = clk();
            else if (clk() - tsh > (unsigned)REVT && blockIdx.x >= NSENT) { if (l == 0) st_wg(&sm.slip, go); }
            unsigned req = ld_wg(&sm.req);
            if ((int)(clk() - ld_wg(&sm.quiet)) < 0) { req = 0; if (l == 0) st_wg(&sm.req, 0u); }
            unsigned want = blockIdx.x < NSENT ? own : 0u;
            if (req == go + SS && own == go) {
                unsigned x = ld_wg(&sm.late[l & 7]);
                x += dppu<0x121>(x);
                x += dppu<0x122>(x);
                x += dppu<0x124>(x);
                want = min(req + (__builtin_amdgcn_readfirstlane(x) * LFAC / 64 & ~15u), (unsigned)MAXOFF);
            }
            if (want > go && want > sent) {
                u64 old = 0;
                behind();
                if (l == 0) st_wg(&sm.quiet, clk() + TQA);
                if (l == 0) old = __hip_atomic_fetch_max(gofs + 16, t64 + want, __ATOMIC_RELAXED, __HIP_MEMORY_SCOPE_AGENT);
                if (NR > 1 && __builtin_amdgcn_readfirstlane((unsigned)(old < t64 + want)) && l < NR)
                    __hip_atomic_fetch_max(gofs + l * 32, t64 + want, __ATOMIC_RELAXED, __HIP_MEMORY_SCOPE_AGENT);
                sent = want;
            }
            if (have ? blockIdx.x == 0 && ++spin % 8 == 0 : clk() - tb >= (unsigned)(blockIdx.x - CAL0 < 64u ? TECHO : TECHO2)) { behind(); chores(false); }
            if (ld_wg(&sm.done) > stage) {
                ++stage;
                behind();
                asm volatile("" : "+s"(code));
                int w = 0;
#pragma unroll
                for (int u = 0; u < 4; ++u) {
                    const int a = (u * 64 + l) * 128;
                    const gptr cp = (gptr)(code + (a < codelen ? a : 0));
                    w ^= CNT ? __builtin_nontemporal_load(cp) : *cp;
                }
                asm volatile("" :: "v"(w));
            }
        }
        while (!have && clk() - tb < TMO) chores(false);
        if (l == 0) st_wg(&sm.have, 1u);
        if (blockIdx.x == 0) {
            for (spin = 1; ld_wg(&sm.fcnt) < WPB; ++spin) {
                __builtin_amdgcn_s_sleep(2);
                if (spin % 1024 == 0) chores(false);
            }
            return;
        }
        if (!DRY) return;
        prepare();
    } else {
        u64* table = tstart + 3 * NWG + NR * 32;
        const unsigned ent = blockIdx.x - CAL0;
        int lead = 0, lat = 0;
        if (PRE) {
            v4i y[PRE ? PRE : 1];
#pragma unroll
            for (int u = 0; u < PRE; ++u) y[u] = *(const v4i*)((const char*)(ws + WS_R) + (pos * NRC + NRC - 1 - (PRE > 1 ? u * 2 + (lane >> 5) : 0)) * (RS * 4) + (lane & 31) * 16);
#pragma unroll
            for (int u = 0; u < PRE; ++u) asm volatile("" :: "v"(y[u]));
        }
        __syncthreads();
        if (ent < 64 && (ent & 31) < 24 && (ent & 3) < NMEM) {
            const unsigned t0 = __builtin_amdgcn_readfirstlane(sm.t0.x);
            unsigned page, x[4];
            auto pair = [&](unsigned k) {
                const unsigned h = (t0 + k + (ent & ~3u) * 2) * 0x9e3779b1u;
                page = (ent & 28) * 16 + (k & 1) * 32 + 24 + ((t0 + (k & 1)) * 0x85ebca6bu >> 29);
                return (const char*)(ent < 32 ? +qk : +qv) + ((size_t)page << 21) + (h >> 25) * 16384 + ((h >> 8) & 127) * 64;
            };
            asm volatile("s_load_dword %0, %4, 0x0\n\ts_load_dword %1, %4, 0x2000\n\ts_load_dword %2, %5, 0x0\n\t"
                         "s_load_dword %3, %5, 0x2000\n\ts_waitcnt lgkmcnt(0)"
                         : "=&s"(x[0]), "=&s"(x[1]), "=&s"(x[2]), "=&s"(x[3]) : "s"(pair(ent % 4 * 2)), "s"(pair(ent % 4 * 2 + 1)) : "memory");
            until(t0 + TPRB);
#pragma nounroll
            for (unsigned k = 2; k < 2 * NMEM; ++k) {
                u64 ta, tc, td;
                asm volatile("s_memtime %0\n\ts_waitcnt lgkmcnt(0)\n\ts_load_dword %3, %4, 0x0\n\ts_waitcnt lgkmcnt(0)\n\t"
                             "s_memtime %1\n\ts_waitcnt lgkmcnt(0)\n\ts_load_dword %3, %4, 0x2000\n\ts_waitcnt lgkmcnt(0)\n\t"
                             "s_memtime %2\n\ts_waitcnt lgkmcnt(0)"
                             : "=&s"(ta), "=&s"(tc), "=&s"(td), "=&s"(x[0]) : "s"(pair((ent % 4 * 2 + k) % (2 * NMEM))) : "memory");
                const int d0 = (int)(tc - ta), d1 = (int)(td - tc), d = max(-PCL, min(PCL, d1 - d0));
                if (min(d0, d1) > 350 && d0 + d1 < 1700) lead += (page ^ (page >> 7)) & 1 ? -d : d;
                lat += d0 + d1;
            }
        }
        asm volatile("" :: "v"(warm), "v"(wq));
        while (ld_wg(&sm.have) == 0 && clk() - tb < TMO) __builtin_amdgcn_s_sleep(4);
        asm volatile("" ::: "memory");
        const u64 E = sm.epoch;
        if (ent < 64 && (ent & 31) < 24 && (ent & 3) < NMEM) {
            unsigned xcc;
            asm volatile("s_getreg_b32 %0, hwreg(HW_REG_XCC_ID, 0, 3)" : "=s"(xcc));
            if (xcc & 4) lead = -lead;
            behind();
            if (lane == 0) st_wg(&sm.quiet, clk() + TQ);
            if (lane < 8) st_rec(table + ent * 32, lane * 16, v4i{lead, (int)(xcc - blockIdx.x), (int)(unsigned)E, (int)(unsigned)(E >> 32)});
        }
        unsigned jobs = 0;
        v4i cal;
        until(tb + TTAB);
        for (;;) {
            behind();
            cal = ld_rec(table, ((lane & 31) < 24 && (lane & 3) < NMEM ? lane : 0) * 256);
            asm volatile("s_waitcnt vmcnt(0)" : "+v"(cal));
            if (__all((unsigned)cal.z == (unsigned)E && (unsigned)cal.w == (unsigned)(E >> 32)) || clk() - tb >= TMO) break;
        }
        {
            int sum = (lane & 3) < NMEM ? cal.x : 0;
            sum += (int)dppu<0xB1>(sum);
            sum += (int)dppu<0x4E>(sum);
            const u64 pol = __ballot(sum < 0) ^ FPOL, ok = __ballot(abs(sum) >= PTH) & FMASK;
            const unsigned side = ((unsigned)__builtin_amdgcn_readfirstlane(cal.y) + (blockIdx.x & ~4u)) >> 2;
#pragma unroll
            for (int u = 0; u < 4; ++u) {
                sm.desc[u >> 1][u & 1][lane] = describe(u >> 1, lane, u & 1, pol, ok, side);
                jobs |= (tau > 0 ? job(u >> 1, 2 * tau + (u & 1), pol, ok, side) : pos & 1u) << u;
            }
            asm volatile("" ::: "memory");
            if (lane == 0 && DST == 21) sm.dsv = clk() - tb;
            if (lane == 0 && DST == 22) sm.dsv = (12 - __popcll(ok & 0x11111100111111ull)) * 10;
            if (lane == 0) st_wg(&sm.ready, 1u);
        }
#pragma nounroll
        for (int cc = 0; cc <= NH / 4; ++cc) {
            const int c = cc < NH / 4 ? cc : tau >> 1;
            const bool act = (tau > 0 && c * 2 + (g >> 1) == tau) == (cc == NH / 4);
            while (ld_wg(&sm.cnt[NH + c]) < WPB) __builtin_amdgcn_s_sleep(2);
            asm volatile("" ::: "memory");
            if (cc >= NH / 4 - 1) {
                const bool sw = ((jobs ^ (jobs >> 2)) >> (lane >> 5)) & 1u;
                v2u* mine = reinterpret_cast<v2u*>(&sm.p[2 * tau][0][0]) + lane;
                if (cc < NH / 4) {
                    if (lane == 0) st_wg(&sm.quiet, clk() + TXW + TQ);
                    if (sw) st_rec(ws + WS_X + pos * 256, lane * 16, v4i{(int)mine->x, (int)mine->y, (int)(unsigned)E, (int)(unsigned)(E >> 32)});
                } else if (__any(sw)) {
                    until(clk() + TXW);
                }
                while (cc == NH / 4 && __any(sw)) {
                    v4i x = {};
                    if (sw) asm volatile("s_nop 4\n\tglobal_load_dwordx4 %0, %1, %2 sc1 nt" : "+v"(x) : "v"(lane * 16), "s"(ws + WS_X + (pos ^ 1) * 256) : "memory");
                    asm volatile("s_waitcnt vmcnt(0)" : "+v"(x));
                    if (__all(!sw || ((unsigned)x.z == (unsigned)E && (unsigned)x.w == (unsigned)(E >> 32))) || clk() - tb >= TMO) {
                        if (sw) *mine = v2u{(unsigned)x.x, (unsigned)x.y};
                        break;
                    }
                    __builtin_amdgcn_s_sleep(8);
                }
                asm volatile("" ::: "memory");
            }
            const bool two = c * 2 + (g >> 1) != 10;
            v4i* pp = reinterpret_cast<v4i*>(&sm.p[c * 4][0][0]) + lane;
            const v4i u = *pp;
            float x[8];
#pragma unroll
            for (int e = 0; e < 4; ++e) { x[2 * e] = bf_lo(u[e]); x[2 * e + 1] = bf_hi(u[e]); }
            float M = fmaxf(fmaxf(fmaxf(x[0], x[1]), fmaxf(x[2], x[3])), fmaxf(fmaxf(x[4], x[5]), fmaxf(x[6], x[7])));
            M = fmaxf(M, dpp<0x128>(M));
            M = fmaxf(M, dpp<0x124>(M));
            M = fmaxf(M, dpp<0x122>(M));
            M = fmaxf(M, dpp<0x121>(M));
            {
                auto y = __builtin_amdgcn_permlane16_swap(__float_as_uint(M), __float_as_uint(M), false, false);
                if (two) M = fmaxf(__uint_as_float(y[0]), __uint_as_float(y[1]));
            }
            v4i o;
            float L = 0.f;
#pragma unroll
            for (int e = 0; e < 4; ++e) {
                const unsigned pk = pack(__expf(x[2 * e] - M), __expf(x[2 * e + 1] - M));
                o[e] = (int)pk;
                L += bf_lo(pk) + bf_hi(pk);
            }
            if (act) *pp = o;
            L = rowsum16(L);
            const float L2 = fold16(L, L);
            if (two) L = L2;
            if (r == 0 && act) sm.ml[c * 4 + g] = __float_as_uint(M) | __builtin_bit_cast(unsigned short, (_Float16)L);
            asm volatile("" ::: "memory");
            if (lane == 0) st_wg(&sm.prog, 1u);
        }
        const unsigned tag = tagof(E, lane & 3);
        auto place = [&](int i) {
            const int i0 = tau + (tau > 10);
            const unsigned jb = i == i0 ? jobs >> 2 : i == 11 && tau == 10 ? jobs >> 3 : pos;
            return (((pos & ~1) | (jb & 1)) * NRC + i) * (RS * 4) + (lane & 31) * 16;
        };
        const unsigned last = place(NRC - 1);
        v2f keep[3] = {};
#pragma nounroll
        for (int h = 0; h < NH; ++h) {
            const unsigned mlb = (sm.ml[h] >> (lane & 3) * 8) & 0xffu;
            if (h < NH - 1) { while (ld_wg(&sm.cnt[h]) < WPB) __builtin_amdgcn_s_sleep(4); }
            else { while (ld_wg(&sm.cnt[h]) < WPB) {} }
            asm volatile("" ::: "memory");
            v2f c[4][3];
#pragma unroll
            for (int e = 0; e < 4; ++e)
#pragma unroll
                for (int k = 0; k < 3; ++k) c[e][k] = *reinterpret_cast<const v2f*>(&sm.stage[h & 1][(lane >> 5) * 4 + e][(lane & 31) * 6 + k * 2]);
            v4i x;
            const bool first = (h & 1) == 0 && h != 20;
#pragma unroll
            for (int k = 0; k < 3; ++k) {
                const v2f a = (keep[k] + (c[0][k] + c[1][k])) + (c[2][k] + c[3][k]), b = a * 0x1p22f;
                x[k] = (int)pack(fold32(b.x, b.x), fold32(b.y, b.y));
                keep[k] = first ? a : v2f{0.f, 0.f};
            }
            x[3] = (int)(tag | mlb);
            if (h == NH - 1) {
                if (lane < 32) {
                    if (BNT & 2) asm volatile("s_nop 4\n\tglobal_store_dwordx4 %0, %1, %2 sc1 nt\n\ts_nop 1" :: "v"(last), "v"(x), "s"(ws + WS_R) : "memory");
                    else st_rec(ws + WS_R, last, x);
                }
                break;
            }
            if (!first) sm.rec[(h >> 1) + (h > 20)][lane & 31] = x;
            asm volatile("" ::: "memory");
            if (lane == 0) st_wg(&sm.prog, h + 2);
            if (h == NH - 2) {
                v4i y[NRC / 2];
                unsigned o[NRC / 2];
#pragma unroll
                for (int u = 0; u < NRC / 2; ++u) { y[u] = (&sm.rec[0][0])[u * 64 + lane]; o[u] = place(u * 2 + (lane >> 5)); }
                until(ld_wg(&sm.tref) + TREL);
#pragma unroll
                for (int u = 0; u < NRC / 2; ++u) {
                    if (BNT & 1) asm volatile("s_nop 4\n\tglobal_store_dwordx4 %0, %1, %2 sc1 nt\n\ts_nop 1" :: "v"(o[u]), "v"(y[u]), "s"(ws + WS_R) : "memory");
                    else st_rec(ws + WS_R, o[u], y[u]);
                }
            }
        }
        return;
    }

    {
        asm volatile("" : "+v"(x[0]), "+v"(x[1]));
        auto gather = [&](unsigned w) {
            w <<= mls;
            w |= dppu<0xB1>(w);
            return w | dppu<0x4E>(w);
        };
        auto half = [](unsigned u) { return (float)__builtin_bit_cast(_Float16, (unsigned short)u); };
        const unsigned ml = gather(x[0].w);
        float m = bf_hi(ml), l = half(ml), a[6];
#pragma unroll
        for (int e = 0; e < 3; ++e) { a[2 * e] = bf_lo(x[0][e]); a[2 * e + 1] = bf_hi(x[0][e]); }
        if (both) {
            const unsigned m2 = gather(x[1].w);
            const float mx = fmaxf(m, bf_hi(m2)), w0 = __expf(m - mx), w1 = __expf(bf_hi(m2) - mx);
#pragma unroll
            for (int e = 0; e < 3; ++e) {
                a[2 * e] = a[2 * e] * w0 + bf_lo(x[1][e]) * w1;
                a[2 * e + 1] = a[2 * e + 1] * w0 + bf_hi(x[1][e]) * w1;
            }
            l = l * w0 + half(m2) * w1;
            m = mx;
        }
        const float M = x4max(m), w0 = __expf(m - M);
        l *= w0;
        float c0 = fold16(fold32(a[0] * w0, a[4] * w0), fold32(a[2] * w0, l));
        float c1 = fold16(fold32(a[1] * w0, a[5] * w0), fold32(a[3] * w0, l));
        c0 += dpp<0x128>(c0); c1 += dpp<0x128>(c1);
        c0 += dpp<0x124>(c0); c1 += dpp<0x124>(c1);
        if (g == 3) c1 = M;
        unsigned arr;
        v4f y;
        float mw;
        asm volatile("ds_write_b64 %3, %4\n\tds_add_rtn_u32 %0, %5, %6\n\tds_read_b32 %2, %8\n\tds_read_b128 %1, %7\n\ts_waitcnt lgkmcnt(0)"
                     : "=&v"(arr), "=&v"(y), "=&v"(mw) : "v"(fa[0]), "v"(v2f{c0, c1}), "v"(fa[1]), "v"(one), "v"(fa[2]), "v"(fa[3]) : "memory");
        const float w = __expf(mw - x4max(mw));
        float d = fold16(fold32(y.x * w, y.z * w), fold32(y.y * w, y.w * w));
        d += dpp<0x128>(d);
        const float L = __int_as_float(__builtin_amdgcn_readlane(__float_as_int(d), 6));
        const unsigned o = pack(d * __builtin_amdgcn_rcpf(L), 0.f);
        if (__builtin_amdgcn_readfirstlane(arr) == WPB - 1 && wave < WPB) {
            if (r < 6) *(unsigned short*)((char*)out + oo) = (unsigned short)o;
        }
    }
    if (wave < WPB) {
        unsigned* dg = reinterpret_cast<unsigned*>(ws + WS_T + 655360) + (blockIdx.x * WPB + wave) * 8;
        if (lane == 0) { dg[0] = (unsigned)T0; dg[1] = d_t1; dg[2] = clk(); for (int i = 0; i < 5; ++i) dg[3 + i] = d_o[i]; }
    }
}
__global__ void kivi_end_marker() { asm volatile("kivi_code_end:"); }

__global__ void diag_kernel(const float* ws) {
    const unsigned* dg = reinterpret_cast<const unsigned*>(ws + WS_T + 655360);
    const unsigned ref = dg[0];
    int mn = 1 << 30, x = -(1 << 30), cnt = 0;
    for (int b = 0; b < 8; ++b) {
        const unsigned* e = dg + (b * 256 + threadIdx.x) * 8;
        mn = min(mn, (int)(e[0] - ref));
        const int v = DIAG == 2 ? (int)(e[0] - ref) : DIAG == 3 ? (int)e[3] : DIAG == 4 ? (int)(e[1] - ref - e[3]) : DIAG == 5 ? (int)(e[2] - ref)
                    : DIAG == 12 ? (int)(e[2] - ref - e[7] * SLIP) : DIAG == 13 ? (int)(e[3] - e[7] * SLIP) : DIAG == 6 ? (int)e[4] : DIAG == 7 || DIAG == 14 ? (int)(e[6] - ref) : DIAG == 8 ? (e[5] ? 1000 - (int)e[5] : 0) : DIAG == 9 ? (e[7] ? 1000 - (int)e[7] : 0) : 0;
        x = max(x, v);
        cnt += DIAG == 11 ? (int)e[4] : (int)e[4] > 30;
    }
    __shared__ int smn, smx, sc;
    if (threadIdx.x == 0) { smn = 1 << 30; smx = -(1 << 30); sc = 0; }
    __syncthreads();
    atomicMin(&smn, mn); atomicMax(&smx, x); atomicAdd(&sc, cnt);
    __syncthreads();
    mn = smn; x = smx;
    int X = 0;
    if (DIAG == 2 || DIAG == 7 || DIAG == 14) X = x - mn;
    if (DIAG == 3 || DIAG == 6 || DIAG == 8 || DIAG == 9) X = x;
    if (DIAG == 4 || DIAG == 5 || DIAG == 12) X = x - mn - 20000;
    if (DIAG == 13) X = x;
    if (DIAG == 10) X = sc;
    if (DIAG == 11) X = sc / 2048;
    X = max(0, min(X, 3000));
    while ((int)(clk() - ref) < mn + 25000 + X * 10) __builtin_amdgcn_s_sleep(1);
}
extern "C" void kernel_launch(void* const* d_in, const int* in_sizes, int n_in,
                              void* d_out, int out_size, void* d_ws, size_t ws_size,
                              hipStream_t stream) {
    const __hip_bfloat16* q = (const __hip_bfloat16*)d_in[0];
    const int* qk = (const int*)d_in[1];
    const int* qv = (const int*)d_in[2];
    const __hip_bfloat16* ks = (const __hip_bfloat16*)d_in[3];
    const __hip_bfloat16* kz = (const __hip_bfloat16*)d_in[4];
    const __hip_bfloat16* vs = (const __hip_bfloat16*)d_in[5];
    const __hip_bfloat16* vz = (const __hip_bfloat16*)d_in[6];
    const float scale = (float)(1.0 / sqrt((double)DH));
    Order order;
    for (int b = 0; b < NWG / 2; ++b) {
        const int p = position(b);
        order.g[b] = groupof(p) | groupof(p + 128) << 8 | groupof(p ^ 1) << 16 | groupof((p ^ 1) + 128) << 24;
    }
    kivi_stream_kernel<<<NWG, NT + 128, 0, stream>>>(q, qk, qv, ks, kz, vs, vz, (float*)d_ws, (__hip_bfloat16*)d_out, scale, order);
    diag_kernel<<<1, 256, 0, stream>>>((const float*)d_ws);
}
```

```cpp
#include <hip/hip_runtime.h>
#include <hip/hip_bf16.h>
#include <stdint.h>
#include <utility>

#define TGT 199.25
#define WST 16
#define THR 80
#define PRV 16
#define SLIP 250
#define MAXOFF 3000
#define NSENT 8
#define NR 32
#define TPL 265
#define STG 4
#define PSLEEP 16
#define OFF0 390
#define TGATH 150
#define GAP1 10
#define TREL 110
#define NSLOW 1
#define EXK 7
#define NOSLK 2
#define TMO 100000000u
#define TPC 265
#define PSL 2
#define TGMAX 300
#define PSH 0
#define SS 20
#define HOLD 4
#define VOTES 4
#define HOLDB 8
#define REVT 600
#define VWAIT 30
#define LFAC 4
#define NOSM 2
#define H0 5
#define TGS 203.0
#define SPL 2
#define G21 5
#define EXS(i) ((int)(((i) + 1) * (TGS - TGT) * 100.0 / 256.0 + 0.5) - (int)((i) * (TGS - TGT) * 100.0 / 256.0 + 0.5))
#define CAL0 128
#define PTH (NMEM * (NMEM - 1) * 150)
#define NMEM 2
#define PCL 500
#define TPRB 580
#define DBM 7
#define DBN 2
#define TQA 500
#define PB 1
#define PTL 8
#define EARLY 0
#define LATEB 20
#define MEANF 0
#define PF 100
#define DRY 0
#define TPB 20
#define PRE 0
#define BNT 0
#define PVAR 0
#define PFP 50
#define KD 0
#define ETCH 1
#define TF0 2
#define VT 0
#define TECHO2 900
#define CNT 0
#define QNT 0
#define FT 1
#define TBL 1
#define SPOLL 0
#define THRO 1
#define AH 1
#define DUM 1
#define TLEAD 14
#define GAP2 10
#define NG2 3
#define TECHO 650
#define TQ 300
#define TTAB 1300
#define TXW 200
#define FMASK ~0ull
#define FPOL 0ull
#define RING 12
#define SB() __builtin_amdgcn_sched_barrier(0)
#define PST(i) SB()

typedef int v4i __attribute__((ext_vector_type(4)));
typedef unsigned v2u __attribute__((ext_vector_type(2)));
typedef float v2f __attribute__((ext_vector_type(2)));
typedef float v4f __attribute__((ext_vector_type(4)));
typedef short v4s __attribute__((ext_vector_type(4)));
typedef __bf16 v2bf __attribute__((ext_vector_type(2)));

constexpr int DH = 192;
constexpr int NH = 32;
constexpr int SKV = 32768;
constexpr int WPB = 8;
constexpr int NT = WPB * 64;
constexpr int NWG = 256;
constexpr int NSL = 8, SLC = 24;
constexpr int RS = NSL * 16;
static_assert(SKV == 2 * 64 * NWG, "two 64-token groups per workgroup and head");
constexpr int NX = 4;
constexpr int NRC = 17;
constexpr size_t WS_R = 262144;
constexpr size_t WS_X = WS_R + (size_t)NRC * NWG * RS;
constexpr size_t WS_T = WS_X + (size_t)NWG * 256;
constexpr unsigned BODY16 = (unsigned)(TGT * 100.0 / 64.0 * 65536.0);
constexpr int slot(int n) { return (int)((n * (BODY16 / 12)) >> 16); }

template <typename F, int... I>
__device__ __forceinline__ void sfor_impl(F&& f, std::integer_sequence<int, I...>) {
    (f(std::integral_constant<int, I>{}), ...);
}
template <int N, typename F>
__device__ __forceinline__ void sfor(F&& f) {
    sfor_impl(f, std::make_integer_sequence<int, N>{});
}

__device__ __forceinline__ float bf_lo(unsigned u) { return __uint_as_float(u << 16); }
__device__ __forceinline__ float bf_hi(unsigned u) { return __uint_as_float(u & 0xffff0000u); }
__device__ __forceinline__ float rb(float x) { return (float)(__bf16)x; }
__device__ __forceinline__ unsigned pack(float lo, float hi) {
    return __builtin_bit_cast(unsigned, __builtin_convertvector(v2f{lo, hi}, v2bf));
}
__device__ __forceinline__ float dot2(unsigned a, unsigned b, float c) {
    return __builtin_amdgcn_fdot2_f32_bf16(__builtin_bit_cast(v2bf, a), __builtin_bit_cast(v2bf, b), c, false);
}
__device__ __forceinline__ void mul4(float (&y)[4], unsigned a, unsigned b, unsigned s0, unsigned s1, unsigned s2, unsigned s3) {
    asm("v_dot2_f32_bf16 %0, %4, %6, 0\n\tv_dot2_f32_bf16 %1, %4, %7, 0\n\t"
        "v_dot2_f32_bf16 %2, %5, %8, 0\n\tv_dot2_f32_bf16 %3, %5, %9, 0\n\ts_nop 2"
        : "=&v"(y[0]), "=&v"(y[1]), "=&v"(y[2]), "=&v"(y[3])
        : "v"(a), "v"(b), "v"(s0), "v"(s1), "v"(s2), "v"(s3));
}
__device__ __forceinline__ unsigned clk() { return (unsigned)__builtin_amdgcn_s_memrealtime(); }
__device__ __forceinline__ unsigned inflight() {
    unsigned x;
    asm volatile("s_getreg_b32 %0, hwreg(HW_REG_IB_STS, 0, 4)" : "=s"(x));
    return x;
}
template <int CTRL>
__device__ __forceinline__ unsigned dppu(unsigned x) {
    return (unsigned)__builtin_amdgcn_mov_dpp((int)x, CTRL, 0xf, 0xf, true);
}
template <int CTRL>
__device__ __forceinline__ float dpp(float x) { return __uint_as_float(dppu<CTRL>(__float_as_uint(x))); }
__device__ __forceinline__ float rowsum16(float x) {
    x += dpp<0x128>(x);
    x += dpp<0x124>(x);
    x += dpp<0x122>(x);
    x += dpp<0x121>(x);
    return x;
}
__device__ __forceinline__ v4i ldstream(const void* p) {
    return __builtin_nontemporal_load(reinterpret_cast<const v4i*>(p));
}

__device__ __forceinline__ unsigned lds(const void* p) { return (unsigned)(size_t)(const __attribute__((address_space(3))) char*)p; }
template <int O, bool STREAM>
__device__ __forceinline__ void to_lds(unsigned d, const void* p, unsigned o) {
    if (STREAM) asm volatile("s_mov_b32 m0, %0\n\ts_nop 4\n\tglobal_load_lds_dwordx4 %1, %2 offset:%3 nt" :: "s"(d - O), "v"(o), "s"(p), "n"(O) : "memory");
    else asm volatile("s_mov_b32 m0, %0\n\ts_nop 4\n\tglobal_load_lds_dwordx4 %1, %2 offset:%3" :: "s"(d - O), "v"(o), "s"(p), "n"(O) : "memory");
}
__device__ __forceinline__ void st_rec(void* p, unsigned o, v4i a) {
    asm volatile("s_nop 4\n\tglobal_store_dwordx4 %0, %1, %2 sc1\n\ts_nop 1" :: "v"(o), "v"(a), "s"(p) : "memory");
}
__device__ __forceinline__ v4i ld_rec(const void* p, unsigned o) {
    v4i x;
    asm volatile("s_nop 4\n\tglobal_load_dwordx4 %0, %1, %2 sc1" : "=v"(x) : "v"(o), "s"(p) : "memory");
    return x;
}
typedef unsigned long long u64;
__device__ __forceinline__ u64 ld_dev(const u64* p) { return __hip_atomic_load(p, __ATOMIC_RELAXED, __HIP_MEMORY_SCOPE_AGENT); }
__device__ __forceinline__ void st_dev(u64* p, u64 v) { __hip_atomic_store(p, v, __ATOMIC_RELAXED, __HIP_MEMORY_SCOPE_AGENT); }
__device__ __forceinline__ unsigned ld_wg(const unsigned* p) { return __hip_atomic_load(p, __ATOMIC_RELAXED, __HIP_MEMORY_SCOPE_WORKGROUP); }
__device__ __forceinline__ u64 ld_wg64(const u64* p) { return __hip_atomic_load(p, __ATOMIC_RELAXED, __HIP_MEMORY_SCOPE_WORKGROUP); }
__device__ __forceinline__ void st_wg(unsigned* p, unsigned v) { __hip_atomic_store(p, v, __ATOMIC_RELAXED, __HIP_MEMORY_SCOPE_WORKGROUP); }
__device__ __forceinline__ float fold32(float a, float b) {
    auto s = __builtin_amdgcn_permlane32_swap(__float_as_uint(a), __float_as_uint(b), false, false);
    return __uint_as_float(s[0]) + __uint_as_float(s[1]);
}
__device__ __forceinline__ float fold16(float a, float b) {
    auto s = __builtin_amdgcn_permlane16_swap(__float_as_uint(a), __float_as_uint(b), false, false);
    return __uint_as_float(s[0]) + __uint_as_float(s[1]);
}
__device__ __forceinline__ float x4max(float x) {
    float m;
    asm("s_nop 1\n\tv_max_f32_dpp %1, %1, %1 row_ror:4 row_mask:0xf bank_mask:0xf\n\t"
        "s_nop 1\n\tv_max_f32_dpp %1, %1, %1 row_ror:8 row_mask:0xf bank_mask:0xf\n\t"
        "s_nop 1\n\tv_max_f32_dpp %1, %1, %1 row_bcast:15 row_mask:0xa bank_mask:0xf\n\t"
        "s_nop 1\n\tv_max_f32_dpp %1, %1, %1 row_bcast:31 row_mask:0xc bank_mask:0xf\n\t"
        "s_nop 0\n\tv_readlane_b32 %0, %1, 63\n\ts_nop 1" : "=s"(m), "+v"(x));
    return m;
}

struct __attribute__((aligned(16))) Smem {
    v4i ksz[2][3][NT];
    v4i par[NX][64];
    v4i qt[NH * 24];
    v4i vsz[2][NT];
    v4i rec[NRC - 1][RS / 4];
    float stage[2][WPB][DH];
    unsigned short p[NH][WPB][16];
    unsigned ml[NH];
    float fin[WPB + 1][32];
    u64 epoch;
    unsigned desc[2][2][2 * NH];
    unsigned ready;
    unsigned have;
    unsigned tref;
    unsigned slip;
    unsigned vote[2 * NH];
    unsigned late[WPB];
    unsigned req;
    v2u t0;
    unsigned done;
    unsigned prog;
    unsigned fcnt;
    unsigned tick;
    unsigned quiet;
    unsigned cnt[NH + NH / 4];
};

__host__ __device__ constexpr int position(int b) { return (b & ~7) | ((b & 3) << 1) | ((b >> 2) & 1); }
constexpr int groupof(int p) {
    const int r0 = p < 85 ? 0 : p < 171 ? 85 : 171, na = p < 171 ? 43 : 42, t = p - r0;
    return r0 + (t < 64 ? ((t >> 4) & 1) * na + (t >> 5) * 16 + (t & 15) : t < na + 32 ? t - 32 : t);
}
struct Order { unsigned g[NWG / 2]; };

__global__ __launch_bounds__(NT + 128, 3) void kivi_stream_kernel(
    const __hip_bfloat16* __restrict__ q, const int* __restrict__ qk, const int* __restrict__ qv,
    const __hip_bfloat16* __restrict__ ks, const __hip_bfloat16* __restrict__ kz,
    const __hip_bfloat16* __restrict__ vs, const __hip_bfloat16* __restrict__ vz,
    float* __restrict__ ws, __hip_bfloat16* __restrict__ out, float scale,
    Order order) {
    __shared__ Smem sm;

    u64 ka0;
    unsigned ka1, ka2;
    asm volatile("s_load_dwordx2 %0, %3, 0x18\n\ts_load_dword %1, %3, %4 offset:0x4c\n\ts_load_dword %2, %3, 0x40"
                 : "=&s"(ka0), "=&s"(ka1), "=&s"(ka2) : "s"(__builtin_amdgcn_kernarg_segment_ptr()), "s"((blockIdx.x & 127) * 4));
    const int tid = threadIdx.x;
    typedef const __attribute__((address_space(1))) int* gptr;
    u64 code;
    int codelen;
    asm volatile("s_getpc_b64 %0\nkivi_code_begin:\n\ts_mov_b32 %1, kivi_code_end-kivi_code_begin-4" : "=s"(code), "=s"(codelen));
    const gptr wp = (gptr)(code + min((unsigned)tid * 128u, (unsigned)codelen));
    const int warm = CNT ? __builtin_nontemporal_load(wp) : *wp;
    const int win = blockIdx.x >> 7;
    auto blockof = [&](int it) { return it >> 2 == 10 ? it : (it & ~3) + 2 * win + ((it >> 1) & 1); };
    auto halfof = [&](int it) { return (it >> 2 == 10 ? win : it) & 1; };
    auto group = [&](int it, int x) { return blockof(it) * NWG + ((x >> halfof(it) * 8) & 255); };
    unsigned pb = blockof(tid >> 3) * NWG, ph = halfof(tid >> 3) * 8;
    const int wave = __builtin_amdgcn_readfirstlane(tid >> 6);
    asm volatile("" : "+v"(pb), "+v"(ph));
    SB();
    asm volatile("s_waitcnt lgkmcnt(0)" : "+s"(ka0), "+s"(ka1), "+s"(ka2));
    const int gx = ka1 & 0xffff, px = ka1 >> 16;
    const unsigned pg = pb + ((ka1 >> ph) & 255), po = pg * (DH * 2) + (tid & 7) * 16;
    if (wave < WPB) {
        to_lds<0, true>(lds(&sm.ksz[0][0][wave * 64]), (const void*)ka0, po);
        to_lds<128, true>(lds(&sm.ksz[0][1][wave * 64]), (const void*)ka0, po);
        to_lds<256, true>(lds(&sm.ksz[0][2][wave * 64]), (const void*)ka0, po);
    }
    SB();
    const u64 tb64 = __builtin_amdgcn_s_memrealtime() - LATEB;
    const unsigned tb = (unsigned)tb64;
    u64* tstart = reinterpret_cast<u64*>(ws + WS_T);
    u64* tbase = tstart + 3 * NWG + NR * 32 + 64 * 32;
    if (TBL && blockIdx.x >= NWG - 8 && tid >= NT && tid < NT + 8)
        st_rec(tbase + (blockIdx.x & 7) * 32, (tid & 7) * 16, v4i{(int)tb, (int)(tb64 >> 32), (int)tb, (int)(tb64 >> 32)});
    if (tid == NT) st_dev(tstart + blockIdx.x, tb64);
    int wq = 0;
    if (QNT && tid >= NT && tid < NT + 96) wq = __builtin_nontemporal_load((const int*)q + (tid - NT) * 32);
    if (wave == WPB + 1 && (!FT || blockIdx.x < 120 || ((blockIdx.x >> 3) & 3) == 3)) {
        const unsigned k = blockIdx.x >> 3, k4 = min(k, 7u) * 4;
        const u64 p = ((const __attribute__((address_space(4))) u64*)__builtin_amdgcn_kernarg_segment_ptr())[(0x13365444u >> k4) & 7u];
        unsigned x;
        asm volatile("s_load_dword %0, %1, 0x0\n\ts_waitcnt lgkmcnt(0)" : "=s"(x) : "s"(p + (((k - ((0x74443000u >> k4) & 7u)) << 21) + (FT ? k : blockIdx.x & 7) * 256)) : "memory");
    }
    SB();
    const int lane = tid & 63;
    const int r = lane & 15;
    const int g = lane >> 4;
    const int pos = position(blockIdx.x);
    auto tagof = [](u64 e, int j) {
        e *= 0x9e3779b97f4a7c15ull;
        return (unsigned)(j == 3 ? (e ^ (e >> 29)) >> 8 : e >> (j * 20)) << 8;
    };
    auto until = [&](unsigned t) { while ((int)(clk() - t) < 0) __builtin_amdgcn_s_sleep(1); };
    auto behind = [&]() {
        const unsigned tick = ld_wg(&sm.tick), t = clk();
        while (ld_wg(&sm.tick) == tick && clk() - t < 100u) __builtin_amdgcn_s_sleep(1);
    };
    const int quart = (pos >> 5) & 3, tau = (-((pos >> 1) + quart)) & 15;
    auto shared = [&](int b) { return SPL == 4 || ((b ^ quart) & 1) == 0; };
    auto job = [&](int t, int b, u64 pol, u64 ok, unsigned side) {
        const int bd = tau == 10 || (t == 0 && shared(b)) ? b : 2 * tau + (SPL == 4 ? 0 : quart & 1);
        const int reg = (shared(b) ? t : 1) * 32 + (group(2 * bd, pos & 1 ? px : gx) * 3 >> 13) * 4;
        return (((ok >> reg) & 1u && (t || b >= H0) ? side ^ (unsigned)(pol >> reg) : quart >> 1) ^ pos) & 1u;
    };
    auto describe = [&](int t, int it, int j, u64 pol, u64 ok, unsigned side) {
        const int b = it >> 1, n = it * 2 + j;
        const bool tr = it >> 2 == tau && tau > 0, sh = tr && shared(b);
        const unsigned jb = job(t, tr ? b : 2 * tau, pol, ok, side);
        const unsigned x = tr ? ((sh ? j : jb) ^ pos) & 1u : 0u;
        const unsigned gr = group(it, x ? px : gx), c = 6 * gr + 3;
        const unsigned flip = sh ? 0u : ((unsigned)(pol >> t * 32) >> (c >> 14) * 4) & 1u;
        const bool early = !t && b < H0;
        const unsigned okt = (unsigned)(ok >> t * 32);
        const bool known = (okt >> (it >> 2) * 3 / 8 * 4) & (okt >> ((it >> 2) * 3 + 2) / 8 * 4) & 1u;
        const unsigned ex = (early || b < 2 || !known ? (n & 3) == 0 ? EXS(0) : (n & 3) == 1 ? EXS(1) : (n & 3) == 2 ? EXS(2) : EXS(3) : 0) + (early || known ? 0 : n & 1)
                          + (n >= 84 && n < 87 ? G21 : 0) + (t ? n == 0 ? GAP1 : n == NG2 && ((pol ^ (pol >> 32)) & 0x111111u) ? GAP2 : 0 : n >= 3 && n < 3 + 4 * NSLOW ? EXK : 0);
        return gr * (64 * DH * 4) | ex << 4 | (early || known || sh ? 0u : 8u) | x << 2 | flip << 1
             | (((sh ? jb : j ^ win ^ flip) ^ (c >> 8) ^ (c >> 15)) & 1u);
    };

    const int mh = (blockIdx.x / 16 * 2 + (blockIdx.x & 7) / 4 + 20) % NH, msl = (blockIdx.x & 3) * 2 + ((blockIdx.x >> 3) & 1);
    const bool both = mh >> 1 == 10;
    v4i x[2] = {};
    unsigned fa[4], mls, one, oo;
    auto prepare = [&]() {
        fa[0] = lds(&sm.fin[wave][g == 3 ? 24 + (r & 3) * 2 : (r & 3) * 6 + g * 2]);
        fa[1] = lane == 0 ? lds(&sm.fcnt) : lds(&sm.vote[lane]);
        fa[2] = lds(&sm.fin[lane >> 3][(lane & 7) * 4]);
        fa[3] = lds(&sm.fin[lane >> 3][25]);
        mls = (lane & 3) * 8;
        one = tid < NT && lane == 0;
        oo = (mh * DH + msl * SLC + r * 4 + g) * 2;
        asm volatile("" : "+v"(fa[0]), "+v"(fa[1]), "+v"(fa[2]), "+v"(fa[3]), "+v"(mls), "+v"(one), "+v"(oo));
    };
    if (__builtin_expect(wave < WPB, 1)) {
    const int ww = (wave & 3) * 2 + (wave >> 2);
    const int ti = ww * 4 + g;
    const int tev = ti + (ti >= 11 ? 10 : 0) + (ti >= 22 ? 11 : 0), tod = ti + 11 + (ti >= 10 ? 11 : 0) + (ti >= 21 ? 10 : 0);
    const unsigned offe = tev * (DH * 4) + r * 16, offo = tod * (DH * 4) + r * 16;
    unsigned descv[2], dsc[4];
    auto lanes = [=](unsigned d) { return (d & 1u ? offo : offe) + (d & ~0x3fffu); };
    auto fetch = [&](int t) {
        while (__builtin_amdgcn_readfirstlane(ld_wg(&sm.ready)) == 0) __builtin_amdgcn_s_sleep(1);
        asm volatile("" ::: "memory");
#pragma unroll
        for (int j = 0; j < 2; ++j) {
            descv[j] = sm.desc[t][j][lane];
            if (ww >= WPB / 2 && (descv[j] & 8u)) descv[j] ^= 3u;
        }
    };
    const char* dummy = reinterpret_cast<const char*>(qv) + (size_t)(blockIdx.x * WPB + wave) * 4864;

    v4i buf[RING];
    auto burst = [&](const char* p, int i, unsigned o) {
#pragma unroll
        for (int e = 0; e < 3; ++e) { buf[i * 3 + e] = ldstream(p + o + e * 256); SB(); }
    };

    const unsigned phase = ((pos & (NWG / 2 - 1)) * (BODY16 / 512) + ww * WST * (BODY16 / (12 * 256))) >> 16;
    unsigned base = tb + phase + PSH;
    unsigned sched = 0, off = 0, offv = 0;
    int prev = 0, now = 0, hold = 0, big = 0, body = 0;
    unsigned hist = 0;
    unsigned vc = 0;
    auto pace = [&](int due) {
        int now;
        while ((now = (int)(clk() - base)) < due) __builtin_amdgcn_s_sleep(1);
        return now;
    };
    auto follow = [&](unsigned o2) {
        hold = o2 - off > SLIP / 2 ? HOLDB : o2 != off ? HOLD : max(hold - 1, 0);
        big = o2 - off > SLIP / 2 ? HOLDB : max(big - 1, 0);
        base += o2 - off;
        off = o2;
    };
    auto pacen = [&](auto N) {
        constexpr int n = decltype(N)::value;
        follow(__builtin_amdgcn_readfirstlane(offv));
        now = pace(slot(n));
        vc = inflight();
        hist <<= 1;
        if (__builtin_expect(vc > 3, 0)) {
            hist |= 1u;
            asm volatile("s_waitcnt vmcnt(3)");
            now = (int)(clk() - base);
            if (now > slot(n) + THR && prev < PRV && big == 0 && off + SLIP <= MAXOFF && body >= NOSLK
                && (int)(base + now - __builtin_amdgcn_readfirstlane(ld_wg(&sm.quiet))) >= 0) {
                if (lane == 0 && __hip_atomic_fetch_add(&sm.vote[body], 1u, __ATOMIC_RELAXED, __HIP_MEMORY_SCOPE_WORKGROUP) + 1 >= VOTES)
                    __hip_atomic_fetch_max(&sm.slip, off + SLIP, __ATOMIC_RELAXED, __HIP_MEMORY_SCOPE_WORKGROUP);
                unsigned o2;
                while ((o2 = __builtin_amdgcn_readfirstlane(ld_wg(&sm.slip))) == off && (int)(clk() - base) < now + VWAIT) {}
                if (o2 != off) { follow(o2); now = pace(slot(n)); }
            }
            if (hold == 0 && off + SS <= MAXOFF && body >= NOSM && body % NH != 21 && __builtin_popcount(hist & DBM) >= DBN && lane == 0) st_wg(&sm.req, off + SS);
        }
    };
    auto check = [&](auto N) {
        constexpr int n = decltype(N)::value;
        prev = now - slot(n);
        if (lane == 0) st_wg(&sm.late[wave], (unsigned)prev);
    };
    auto getslip = [&]() { offv = __hip_atomic_load(&sm.slip, __ATOMIC_RELAXED, __HIP_MEMORY_SCOPE_WORKGROUP); };
    auto arrive = [&](int i) {
        asm volatile("" ::: "memory");
        if (lane == 0) __hip_atomic_fetch_add(&sm.cnt[i], 1u, __ATOMIC_RELAXED, __HIP_MEMORY_SCOPE_WORKGROUP);
    };
    auto next_body = [&]() {
        const unsigned s2 = sched + BODY16;
        base += (s2 >> 16) - (sched >> 16);
        sched = s2;
        ++body;
    };

    {
        to_lds<0, false>(lds(&sm.qt[wave * 64]), q, tid * 16);
        if (wave < 4) to_lds<0, false>(lds(&sm.qt[NT + wave * 64]), q, (NT + tid) * 16);
        PST(1); pace(slot(3)); SB();
        to_lds<0, true>(lds(&sm.ksz[1][0][wave * 64]), kz, po);
        to_lds<128, true>(lds(&sm.ksz[1][1][wave * 64]), kz, po);
        to_lds<256, true>(lds(&sm.ksz[1][2][wave * 64]), kz, po);
        SB();
        const bool isk = lane < 48, isz = isk ? lane >= 24 : lane >= 56;
        const char* par = (const char*)(isk ? isz ? kz : ks : isz ? vz : vs) + (isk ? lane % 24 : lane & 7) * 16
                        + group(tau * 4 + (wave & 3), px) * (isk ? DH * 2 : 128);
        asm volatile("" : "+v"(par));
        dsc[0] = describe(0, 0, 0, 0u, 0u, 0u);
        dsc[1] = describe(0, 0, 1, 0u, 0u, 0u);
        PST(2); pace(slot(6)); SB();
        to_lds<0, true>(lds(&sm.vsz[0][wave * 64]), vs, pg * 128 + (tid & 7) * 16);
        to_lds<0, true>(lds(&sm.vsz[1][wave * 64]), vz, pg * 128 + (tid & 7) * 16);
        if (wave >= WPB - NX)
            asm volatile("s_mov_b32 m0, %0\n\ts_nop 0\n\tglobal_load_lds_dwordx4 %1, off nt" :: "s"(lds(&sm.par[wave & 3][0])), "v"(par) : "memory");
        PST(3); pace(slot(8) + KD); SB();
        burst((const char*)qk, 0, lanes(dsc[0]));
        PST(4);
        descv[0] = describe(0, lane, 0, 0u, 0u, 0u);
        descv[1] = describe(0, lane, 1, 0u, 0u, 0u);
        asm volatile("" : "+v"(descv[0]), "+v"(descv[1]));
        if (THRO) asm volatile("" :: "v"(warm));
        PST(5); pace(slot(11) + KD); SB();
        burst((const char*)qk, 1, lanes(dsc[1]));
        PST(6);
    }
    {
        unsigned t0;
        for (;;) {
            const u64 t = ld_wg64((const u64*)&sm.t0);
            t0 = __builtin_amdgcn_readfirstlane((unsigned)t);
            if (t0 == ~__builtin_amdgcn_readfirstlane((unsigned)(t >> 32)) && t0 - tb + 1100u < 2200u) break;
            t0 = tb;
            if (clk() - tb > 1000u) break;
        }
        PST(7);
        PST(8);
        base = t0 + OFF0 + phase;
    }

    const v2f huge = {0x1p127f, 0x1p127f};

    for (int S = 0; S < NH; ++S) {
        v2f kz2[6];
        unsigned slo[6], shi[6];
        v2u qq[3];
        const char* nsup = (const char*)(S + 1 < NH ? qk : qv);
        const int head = S >> 1 == 10 ? S : (S & ~1) + win;
        float mylogit = 0.f;
        float dot = 0.f;
        sfor<12>([&](auto N) {
            constexpr int n = decltype(N)::value;
            constexpr int i = n / 3, j = n % 3;
            if (n == 6 && (S == H0 - 1 || S == NH - 1)) fetch(S == NH - 1);
            if (j == 0) {
                const unsigned d = dsc[i ^ 2] = __builtin_amdgcn_readlane(descv[i & 1], (2 * S + 1 + (i >> 1)) & 63);
                const unsigned o = lanes(d);
                base += (d >> 4) & 31u;
                pacen(N);
                SB();
                burst(i < 2 ? (const char*)qk : nsup, i ^ 2, o);
                SB();
                if (tid == NT - 1) st_wg(&sm.tick, S * 4 + i + 1);
                check(N);
                if (n == 0 && S == 0) asm volatile("s_waitcnt vmcnt(9)\n\ts_barrier" ::: "memory");
            }
            if (j == 2) getslip();
            if (i % 2 == 0 || ((dsc[i] ^ dsc[i ^ 1]) & 4u)) {
                const int it = S * 2 + (i >> 1);
                const bool other = dsc[i] & 4u;
                const v2u* pk = reinterpret_cast<const v2u*>(other ? &sm.par[it & 3][j * 8] : &sm.ksz[0][j][it * 8]) + r;
                const v2u a = pk[0], b = pk[other ? 48 : 3 * NT * 2];
                slo[j * 2 + 0] = a.x & 0xffffu; shi[j * 2 + 0] = a.x & 0xffff0000u;
                slo[j * 2 + 1] = a.y & 0xffffu; shi[j * 2 + 1] = a.y & 0xffff0000u;
                kz2[j * 2 + 0] = v2f{bf_lo(b.x), bf_hi(b.x)} * -0x1p-22f;
                kz2[j * 2 + 1] = v2f{bf_lo(b.y), bf_hi(b.y)} * -0x1p-22f;
            }
            if (i == 0) qq[j] = reinterpret_cast<const v2u*>(&sm.qt[head * 24])[j * 16 + r];
            if (j == 0) dot = 0.f;
            {
                const v4i x = buf[n % RING];
                const v2f t0 = __builtin_elementwise_fma(v2f{__int_as_float(x.x), __int_as_float(x.y)}, huge, kz2[j * 2]);
                const v2f t1 = __builtin_elementwise_fma(v2f{__int_as_float(x.z), __int_as_float(x.w)}, huge, kz2[j * 2 + 1]);
                float y[4];
                mul4(y, pack(t0.x, t0.y), pack(t1.x, t1.y), slo[j * 2], shi[j * 2], slo[j * 2 + 1], shi[j * 2 + 1]);
                dot = dot2(pack(y[0], y[1]), qq[j].x, dot);
                dot = dot2(pack(y[2], y[3]), qq[j].y, dot);
            }
            if (j == 2) {
                const float d = rowsum16(dot);
                if ((r & 3) == (i ^ ((dsc[i] >> 1) & 1))) mylogit = d;
            }
            asm volatile("" : "+v"(dot), "+v"(mylogit));
            SB();
        });
        if (r < 4) sm.p[S][wave][g * 4 + r] = (unsigned short)pack(rb(mylogit * 0x1p22f) * scale, 0.f);
        if (!THRO) asm volatile("" :: "v"(warm));
        if ((S & 3) == 3) arrive(NH + (S >> 2));
        if (S == NH - 3 && tid == 0) st_wg(&sm.done, 1u);
        next_body();
    }

    for (int S = 0; S < NH; ++S) {
        v2u P;
        unsigned vsl, vsh, vzf;
        v4f acc = {0.f, 0.f, 0.f, 0.f};
        float y0[12];
        unsigned b0[12];
        const bool more = S + 1 < NH;
        unsigned sl, sh;
        v2u A[12];
        v2f z2;
        sfor<12>([&](auto N) {
            constexpr int n = decltype(N)::value;
            constexpr int i = n / 3, j = n % 3;
            if (j == 0) {
                const unsigned d = dsc[i ^ 2] = __builtin_amdgcn_readlane(descv[i & 1], (2 * S + 1 + (i >> 1)) & 63);
                unsigned o = lanes(d);
                const char* src = (const char*)qv;
                if (i >= 2 && !more) {
                    o = DUM ? 0 : i * 768;
                    src = !DUM ? dummy : i == 3 && wave == 0 ? (const char*)out + min(mh, NH - 2) * (DH * 2) : (const char*)ws + (i & 1) * (15 << 18) + blockIdx.x * 768;
                }
                if (i < 2 || more) base += (d >> 4) & 31u;
                if (i < 2 || more) pacen(N);
                SB();
                burst(src, i ^ 2, o);
                SB();
                if (tid == NT - 1) st_wg(&sm.tick, 4 * NH + S * 4 + i + 1);
                check(N);
            }
            if (n == 0) {
                const unsigned need = S < 2 ? 1 : S;
                while (__builtin_amdgcn_readfirstlane(ld_wg(&sm.prog)) < need) {}
                asm volatile("" ::: "memory");
                P = *reinterpret_cast<const v2u*>(&sm.p[S][wave][g * 4]);
                P.x = __builtin_amdgcn_alignbit(P.x, P.x, (dsc[0] & 2u) << 3);
                P.y = __builtin_amdgcn_alignbit(P.y, P.y, (dsc[2] & 2u) << 3);
                const unsigned d3 = __builtin_amdgcn_readlane(descv[1], 2 * S + 1);
                const unsigned x = (((dsc[0] & 7u) | (dsc[1] & 7u) << 8) | ((dsc[2] & 7u) << 16 | (d3 & 7u) << 24)) >> (r & 3) * 8;
                const int it = S * 2 + ((r >> 1) & 1);
                const unsigned short* pv = reinterpret_cast<const unsigned short*>(x & 4u ? &sm.par[it & 3][48] : &sm.vsz[0][it * 8]) + (x & 1u ? tod : tev);
                vsl = pv[0];
                vsh = vsl << 16;
                vzf = __float_as_uint(bf_lo(pv[x & 4u ? 64 : NT * 8]) * -0x1p-22f);
            }
            if (j == 2) getslip();
            if (j == 0) {
                sl = dppu<0x55 * i>(vsl);
                sh = dppu<0x55 * i>(vsh);
                const float zb = __uint_as_float(dppu<0x55 * i>(vzf));
                z2 = v2f{zb, zb};
            }
            float y[4];
            {
                const v4i x = buf[n];
                const v2f t0 = __builtin_elementwise_fma(v2f{__int_as_float(x.x), __int_as_float(x.y)}, huge, z2);
                const v2f t1 = __builtin_elementwise_fma(v2f{__int_as_float(x.z), __int_as_float(x.w)}, huge, z2);
                mul4(y, pack(t0.x, t0.y), pack(t1.x, t1.y), sl, sh, sl, sh);
            }
#pragma unroll
            for (int e = 0; e < 4; ++e) {
                const int idx = j * 4 + e;
                if (i % 2 == 0) {
                    y0[idx] = y[e];
                    asm volatile("" : "+v"(y0[idx]));
                    if (i == 2) {
                        A[idx] = v2u{r == idx ? P.x : 0u, r == idx ? P.y : 0u};
                        if (AH) asm volatile("" : "+v"(A[idx]));
                    }
                } else if (i == 1) {
                    b0[idx] = pack(y0[idx], y[e]);
                    asm volatile("" : "+v"(b0[idx]));
                } else {
                    const v2u B = {b0[idx], pack(y0[idx], y[e])};
                    acc = __builtin_amdgcn_mfma_f32_16x16x16bf16_1k(__builtin_bit_cast(v4s, A[idx]), __builtin_bit_cast(v4s, B), acc, 0, 0, 0);
                }
            }
            if (n == 11) {
                if (S == NH - 2 && tid == 0) sm.tref = base + slot(12) + slot(3);
                if (S == NH - 3 && tid == 0) st_wg(&sm.done, 2u);
                if (lane < 48) *reinterpret_cast<v4f*>(&sm.stage[S & 1][wave][lane * 4]) = acc;
                arrive(S);
            }
            SB();
        });
        next_body();
    }
    {
        const unsigned tag = tagof(sm.epoch, lane & 3);
        const float* src = ws + WS_R + ((size_t)((both ? 0 : mh & 1) * (NWG / 2) + wave * 16) * NRC + (mh >> 1) + (mh > 20)) * RS + msl * 16;
        const unsigned so = (lane >> 2) * (NRC * RS * 4) + (lane & 3) * 16;
        prepare();
        until(base - slot(12) + slot(3) - phase + (wave - (WPB - 1)) * STG + (mh >= NH - 2 ? TPL : both ? TPC - TPB : TPC));
        for (;;) {
            x[0] = ld_rec(src, so);
            x[1] = v4i{0, 0, 0, (int)tag};
            if (both) x[1] = ld_rec(src + NWG / 2 * NRC * RS, so);
            asm volatile("s_waitcnt vmcnt(0)" : "+v"(x[0]), "+v"(x[1]));
            if (__all((((unsigned)x[0].w ^ tag) | ((unsigned)x[1].w ^ tag)) < 256u) || clk() - tb >= TMO) break;
            __builtin_amdgcn_s_sleep(PSL);
        }
#pragma unroll
        for (int d = 5; d >= 0; --d) asm volatile("" :: "v"(buf[d]));
        x[0].w ^= tag;
        x[1].w ^= tag;
    }
    } else
    if (wave == WPB) {
        const int l = lane;
        u64* echo = tstart + NWG;
        u64* gofs = tstart + 3 * NWG;
        if (l == 0) {
            if (blockIdx.x < NR) st_dev(gofs + blockIdx.x * 32, tb64 - 100000ull);
            if (blockIdx.x == NR) st_dev(gofs + 16, tb64 - 100000ull);
            sm.slip = 0;
            sm.req = 0;
            sm.done = 0;
            sm.prog = 0;
            sm.ready = 0;
            sm.tick = 0;
            sm.quiet = tb;
            sm.fcnt = 0;
            sm.epoch = tb64;
            sm.have = blockIdx.x == 0;
        }
        if (l < NH + NH / 4) sm.cnt[l] = 0;
        sm.vote[l] = 0;
        until(tb + TGATH);
        unsigned ld;
        if (TBL == 2) {
            for (;;) {
                u64 x[8];
                asm volatile("s_load_dwordx2 %0, %8, 0x0 glc\n\ts_load_dwordx2 %1, %8, 0x100 glc\n\ts_load_dwordx2 %2, %8, 0x200 glc\n\t"
                             "s_load_dwordx2 %3, %8, 0x300 glc\n\ts_load_dwordx2 %4, %8, 0x400 glc\n\ts_load_dwordx2 %5, %8, 0x500 glc\n\t"
                             "s_load_dwordx2 %6, %8, 0x600 glc\n\ts_load_dwordx2 %7, %8, 0x700 glc\n\ts_waitcnt lgkmcnt(0)"
                             : "=&s"(x[0]), "=&s"(x[1]), "=&s"(x[2]), "=&s"(x[3]), "=&s"(x[4]), "=&s"(x[5]), "=&s"(x[6]), "=&s"(x[7]) : "s"(tbase) : "memory");
                bool all = true;
                ld = 0;
#pragma unroll
                for (int u = 0; u < 8; ++u) {
                    const u64 dt = x[u] - tb64 + 1000ull;
                    if (dt < 2000ull) ld = max(ld, (unsigned)dt); else all = false;
                }
                if (all || clk() - tb > (unsigned)TGMAX) break;
            }
        } else {
            for (;;) {
                const u64 dt = ld_dev(TBL ? tbase + (l & 7) * 32 : tstart + NWG - 8 + (l & 7)) - tb64 + 1000ull;
                ld = dt < 2000ull ? (unsigned)dt : 0u;
                if (__all(dt < 2000ull) || clk() - tb > (unsigned)TGMAX) break;
            }
            ld = max(ld, dppu<0x124>(ld));
            ld = max(ld, dppu<0x122>(ld));
            ld = max(ld, dppu<0x121>(ld));
            ld = __builtin_amdgcn_readfirstlane(ld);
        }
        const u64 t64 = ld ? tb64 + ld - 1000ull : tb64;
        if (l == 0) sm.t0 = v2u{(unsigned)t64, ~(unsigned)t64};
        u64 to[NWG / 64];
        bool have = blockIdx.x == 0;
        auto chores = [&](bool all) {
            if (blockIdx.x == 0) {
                u64 x[NWG / 64];
#pragma unroll
                for (int u = 0; u < NWG / 64; ++u) x[u] = ld_dev(tstart + u * 64 + l);
#pragma unroll
                for (int u = 0; u < NWG / 64; ++u) {
                    if (all || __any(x[u] != to[u])) st_rec(echo + u * 128, l * 16, v4i{(int)(unsigned)x[u], (int)(unsigned)(x[u] >> 32), (int)tb, (int)(unsigned)(tb64 >> 32)});
                    to[u] = x[u];
                }
            } else if (!have && !all) {
                v4i x = ld_rec(echo + blockIdx.x * 2, 0);
                asm volatile("s_waitcnt vmcnt(0)" : "+v"(x));
                if ((unsigned)x.x == tb && (unsigned)x.y == (unsigned)(tb64 >> 32)) {
                    have = true;
                    if (l == 0) sm.epoch = (u64)(unsigned)x.w << 32 | (unsigned)x.z;
                    asm volatile("s_waitcnt lgkmcnt(0)" ::: "memory");
                    if (l == 0) st_wg(&sm.have, 1u);
                }
            }
        };
        if (EARLY) chores(true);
        asm volatile("s_waitcnt lgkmcnt(0)\n\ts_barrier" ::: "memory");
        asm volatile("" :: "v"(warm), "v"(wq));
        if (!EARLY) chores(true);
        if (!ETCH) {
            until(tb + TECHO);
            behind();
            chores(false);
        }
        const u64* mine = NR > 1 ? gofs + ((blockIdx.x >> 3) % NR) * 32 : gofs + 16;
        unsigned sent = 0, stage = 0, tsh = clk();
        int nt = blockIdx.x < 200, spin = 0;
        while (stage < 2) {
            if (PB > 1 && have) __builtin_amdgcn_s_sleep(PSLEEP);
            behind();
            unsigned tch = 0;
            if (nt < 25) {
                const unsigned pg = (blockIdx.x >> 3) + 32 * (nt % 12);
                const char* tp = (nt < 12 ? (const char*)qk : nt < 24 ? (const char*)qv : (const char*)ws) + ((size_t)pg << 21);
                const unsigned vp = (unsigned)((size_t)tp >> 21);
                const bool first = (vp & (PTL - 1)) == 0 || min(pg & 127u, 128u - (pg & 127u)) < (vp & -vp);
                tp += (((first ? blockIdx.x >> 3 : blockIdx.x) * 29 + nt) & 255) * 256;
                if (nt == 24 && blockIdx.x >= 16) tp = (const char*)out + min((pg - 2) * 4096, NH * DH * 2 - 4u);
                if (ld_wg(&sm.tick) + (first ? TLEAD + 4 : TLEAD) >= (unsigned)max(nt / 12 * 128 + nt % 12 * 4 / 3 * 8 - nt / 24 * 32, TLEAD + TF0)) {
                    unsigned x;
                    if (nt < 24 && !first) { if (VT) asm volatile("s_nop 4\n\tglobal_load_dword %0, %1, %2 nt" : "+v"(tch) : "v"(0), "s"(tp) : "memory"); }
                    else if (nt < 24 || blockIdx.x < 48) asm volatile("s_load_dword %0, %1, 0x0\n\ts_waitcnt lgkmcnt(0)" : "=s"(x) : "s"(tp) : "memory");
                    ++nt;
                }
            }
            u64 gd;
            if (SPOLL) asm volatile("s_load_dwordx2 %0, %1, 0x0 glc\n\ts_waitcnt lgkmcnt(0)" : "=s"(gd) : "s"(mine) : "memory");
            else gd = ld_dev(mine);
            gd -= t64;
            asm volatile("s_waitcnt vmcnt(0)" : "+v"(tch));
            const unsigned go = gd <= (u64)MAXOFF ? (unsigned)gd : 0u;
            const unsigned own = ld_wg(&sm.slip);
            if (go > own && l == 0) __hip_atomic_fetch_max(&sm.slip, go, __ATOMIC_RELAXED, __HIP_MEMORY_SCOPE_WORKGROUP);
            if (own <= go) tsh = clk();
            else if (clk() - tsh > (unsigned)REVT && blockIdx.x >= NSENT) { if (l == 0) st_wg(&sm.slip, go); }
            unsigned req = ld_wg(&sm.req);
            if ((int)(clk() - ld_wg(&sm.quiet)) < 0) { req = 0; if (l == 0) st_wg(&sm.req, 0u); }
            unsigned want = blockIdx.x < NSENT ? own : 0u;
            if (req == go + SS && own == go) {
                unsigned x = ld_wg(&sm.late[l & 7]);
                x += dppu<0x121>(x);
                x += dppu<0x122>(x);
                x += dppu<0x124>(x);
                want = min(req + (__builtin_amdgcn_readfirstlane(x) * LFAC / 64 & ~15u), (unsigned)MAXOFF);
            }
            if (want > go && want > sent) {
                u64 old = 0;
                behind();
                if (l == 0) st_wg(&sm.quiet, clk() + TQA);
                if (l == 0) old = __hip_atomic_fetch_max(gofs + 16, t64 + want, __ATOMIC_RELAXED, __HIP_MEMORY_SCOPE_AGENT);
                if (NR > 1 && __builtin_amdgcn_readfirstlane((unsigned)(old < t64 + want)) && l < NR)
                    __hip_atomic_fetch_max(gofs + l * 32, t64 + want, __ATOMIC_RELAXED, __HIP_MEMORY_SCOPE_AGENT);
                sent = want;
            }
            if (have ? blockIdx.x == 0 && ++spin % 8 == 0 : clk() - tb >= (unsigned)(blockIdx.x - CAL0 < 64u ? TECHO : TECHO2)) { behind(); chores(false); }
            if (ld_wg(&sm.done) > stage) {
                ++stage;
                behind();
                asm volatile("" : "+s"(code));
                int w = 0;
#pragma unroll
                for (int u = 0; u < 4; ++u) {
                    const int a = (u * 64 + l) * 128;
                    const gptr cp = (gptr)(code + (a < codelen ? a : 0));
                    w ^= CNT ? __builtin_nontemporal_load(cp) : *cp;
                }
                asm volatile("" :: "v"(w));
            }
        }
        while (!have && clk() - tb < TMO) chores(false);
        if (l == 0) st_wg(&sm.have, 1u);
        if (blockIdx.x == 0) {
            for (spin = 1; ld_wg(&sm.fcnt) < WPB; ++spin) {
                __builtin_amdgcn_s_sleep(2);
                if (spin % 1024 == 0) chores(false);
            }
            return;
        }
        if (!DRY) return;
        prepare();
    } else {
        u64* table = tstart + 3 * NWG + NR * 32;
        const unsigned ent = blockIdx.x - CAL0;
        int lead = 0, lat = 0;
        if (PRE) {
            v4i y[PRE ? PRE : 1];
#pragma unroll
            for (int u = 0; u < PRE; ++u) y[u] = *(const v4i*)((const char*)(ws + WS_R) + (pos * NRC + NRC - 1 - (PRE > 1 ? u * 2 + (lane >> 5) : 0)) * (RS * 4) + (lane & 31) * 16);
#pragma unroll
            for (int u = 0; u < PRE; ++u) asm volatile("" :: "v"(y[u]));
        }
        __syncthreads();
        if (ent < 64 && (ent & 31) < 24 && (ent & 3) < NMEM) {
            const unsigned t0 = __builtin_amdgcn_readfirstlane(sm.t0.x);
            unsigned page, x[4];
            auto pair = [&](unsigned k) {
                const unsigned h = (t0 + k + (ent & ~3u) * 2) * 0x9e3779b1u;
                page = (ent & 28) * 16 + (k & 1) * 32 + 24 + ((t0 + (k & 1)) * 0x85ebca6bu >> 29);
                return (const char*)(ent < 32 ? +qk : +qv) + ((size_t)page << 21) + (h >> 25) * 16384 + ((h >> 8) & 127) * 64;
            };
            asm volatile("s_load_dword %0, %4, 0x0\n\ts_load_dword %1, %4, 0x2000\n\ts_load_dword %2, %5, 0x0\n\t"
                         "s_load_dword %3, %5, 0x2000\n\ts_waitcnt lgkmcnt(0)"
                         : "=&s"(x[0]), "=&s"(x[1]), "=&s"(x[2]), "=&s"(x[3]) : "s"(pair(ent % 4 * 2)), "s"(pair(ent % 4 * 2 + 1)) : "memory");
            until(t0 + TPRB);
#pragma nounroll
            for (unsigned k = 2; k < 2 * NMEM; ++k) {
                u64 ta, tc, td;
                asm volatile("s_memtime %0\n\ts_waitcnt lgkmcnt(0)\n\ts_load_dword %3, %4, 0x0\n\ts_waitcnt lgkmcnt(0)\n\t"
                             "s_memtime %1\n\ts_waitcnt lgkmcnt(0)\n\ts_load_dword %3, %4, 0x2000\n\ts_waitcnt lgkmcnt(0)\n\t"
                             "s_memtime %2\n\ts_waitcnt lgkmcnt(0)"
                             : "=&s"(ta), "=&s"(tc), "=&s"(td), "=&s"(x[0]) : "s"(pair((ent % 4 * 2 + k) % (2 * NMEM))) : "memory");
                const int d0 = (int)(tc - ta), d1 = (int)(td - tc), d = max(-PCL, min(PCL, d1 - d0));
                if (min(d0, d1) > 350 && d0 + d1 < 1700) lead += (page ^ (page >> 7)) & 1 ? -d : d;
                lat += d0 + d1;
            }
        }
        asm volatile("" :: "v"(warm), "v"(wq));
        while (ld_wg(&sm.have) == 0 && clk() - tb < TMO) __builtin_amdgcn_s_sleep(4);
        asm volatile("" ::: "memory");
        const u64 E = sm.epoch;
        if (ent < 64 && (ent & 31) < 24 && (ent & 3) < NMEM) {
            unsigned xcc;
            asm volatile("s_getreg_b32 %0, hwreg(HW_REG_XCC_ID, 0, 3)" : "=s"(xcc));
            if (xcc & 4) lead = -lead;
            behind();
            if (lane == 0) st_wg(&sm.quiet, clk() + TQ);
            if (lane < 8) st_rec(table + ent * 32, lane * 16, v4i{lead, (int)(xcc - blockIdx.x), (int)(unsigned)E, (int)(unsigned)(E >> 32)});
        }
        unsigned jobs = 0;
        v4i cal;
        until(tb + TTAB);
        for (;;) {
            behind();
            cal = ld_rec(table, ((lane & 31) < 24 && (lane & 3) < NMEM ? lane : 0) * 256);
            asm volatile("s_waitcnt vmcnt(0)" : "+v"(cal));
            if (__all((unsigned)cal.z == (unsigned)E && (unsigned)cal.w == (unsigned)(E >> 32)) || clk() - tb >= TMO) break;
        }
        {
            int sum = (lane & 3) < NMEM ? cal.x : 0;
            sum += (int)dppu<0xB1>(sum);
            sum += (int)dppu<0x4E>(sum);
            const u64 pol = __ballot(sum < 0) ^ FPOL, ok = __ballot(abs(sum) >= PTH) & FMASK;
            const unsigned side = ((unsigned)__builtin_amdgcn_readfirstlane(cal.y) + (blockIdx.x & ~4u)) >> 2;
#pragma unroll
            for (int u = 0; u < 4; ++u) {
                sm.desc[u >> 1][u & 1][lane] = describe(u >> 1, lane, u & 1, pol, ok, side);
                jobs |= (tau > 0 ? job(u >> 1, 2 * tau + (u & 1), pol, ok, side) : pos & 1u) << u;
            }
            asm volatile("" ::: "memory");
            if (lane == 0) st_wg(&sm.ready, 1u);
        }
#pragma nounroll
        for (int cc = 0; cc <= NH / 4; ++cc) {
            const int c = cc < NH / 4 ? cc : tau >> 1;
            const bool act = (tau > 0 && c * 2 + (g >> 1) == tau) == (cc == NH / 4);
            while (ld_wg(&sm.cnt[NH + c]) < WPB) __builtin_amdgcn_s_sleep(2);
            asm volatile("" ::: "memory");
            if (cc >= NH / 4 - 1) {
                const bool sw = ((jobs ^ (jobs >> 2)) >> (lane >> 5)) & 1u;
                v2u* mine = reinterpret_cast<v2u*>(&sm.p[2 * tau][0][0]) + lane;
                if (cc < NH / 4) {
                    if (lane == 0) st_wg(&sm.quiet, clk() + TXW + TQ);
                    if (sw) st_rec(ws + WS_X + pos * 256, lane * 16, v4i{(int)mine->x, (int)mine->y, (int)(unsigned)E, (int)(unsigned)(E >> 32)});
                } else if (__any(sw)) {
                    until(clk() + TXW);
                }
                while (cc == NH / 4 && __any(sw)) {
                    v4i x = {};
                    if (sw) asm volatile("s_nop 4\n\tglobal_load_dwordx4 %0, %1, %2 sc1 nt" : "+v"(x) : "v"(lane * 16), "s"(ws + WS_X + (pos ^ 1) * 256) : "memory");
                    asm volatile("s_waitcnt vmcnt(0)" : "+v"(x));
                    if (__all(!sw || ((unsigned)x.z == (unsigned)E && (unsigned)x.w == (unsigned)(E >> 32))) || clk() - tb >= TMO) {
                        if (sw) *mine = v2u{(unsigned)x.x, (unsigned)x.y};
                        break;
                    }
                    __builtin_amdgcn_s_sleep(8);
                }
                asm volatile("" ::: "memory");
            }
            const bool two = c * 2 + (g >> 1) != 10;
            v4i* pp = reinterpret_cast<v4i*>(&sm.p[c * 4][0][0]) + lane;
            const v4i u = *pp;
            float x[8];
#pragma unroll
            for (int e = 0; e < 4; ++e) { x[2 * e] = bf_lo(u[e]); x[2 * e + 1] = bf_hi(u[e]); }
            float M = fmaxf(fmaxf(fmaxf(x[0], x[1]), fmaxf(x[2], x[3])), fmaxf(fmaxf(x[4], x[5]), fmaxf(x[6], x[7])));
            M = fmaxf(M, dpp<0x128>(M));
            M = fmaxf(M, dpp<0x124>(M));
            M = fmaxf(M, dpp<0x122>(M));
            M = fmaxf(M, dpp<0x121>(M));
            {
                auto y = __builtin_amdgcn_permlane16_swap(__float_as_uint(M), __float_as_uint(M), false, false);
                if (two) M = fmaxf(__uint_as_float(y[0]), __uint_as_float(y[1]));
            }
            v4i o;
            float L = 0.f;
#pragma unroll
            for (int e = 0; e < 4; ++e) {
                const unsigned pk = pack(__expf(x[2 * e] - M), __expf(x[2 * e + 1] - M));
                o[e] = (int)pk;
                L += bf_lo(pk) + bf_hi(pk);
            }
            if (act) *pp = o;
            L = rowsum16(L);
            const float L2 = fold16(L, L);
            if (two) L = L2;
            if (r == 0 && act) sm.ml[c * 4 + g] = __float_as_uint(M) | __builtin_bit_cast(unsigned short, (_Float16)L);
            asm volatile("" ::: "memory");
            if (lane == 0) st_wg(&sm.prog, 1u);
        }
        const unsigned tag = tagof(E, lane & 3);
        auto place = [&](int i) {
            const int i0 = tau + (tau > 10);
            const unsigned jb = i == i0 ? jobs >> 2 : i == 11 && tau == 10 ? jobs >> 3 : pos;
            return (((pos & ~1) | (jb & 1)) * NRC + i) * (RS * 4) + (lane & 31) * 16;
        };
        const unsigned last = place(NRC - 1);
        v2f keep[3] = {};
#pragma nounroll
        for (int h = 0; h < NH; ++h) {
            const unsigned mlb = (sm.ml[h] >> (lane & 3) * 8) & 0xffu;
            if (h < NH - 1) { while (ld_wg(&sm.cnt[h]) < WPB) __builtin_amdgcn_s_sleep(4); }
            else { while (ld_wg(&sm.cnt[h]) < WPB) {} }
            asm volatile("" ::: "memory");
            v2f c[4][3];
#pragma unroll
            for (int e = 0; e < 4; ++e)
#pragma unroll
                for (int k = 0; k < 3; ++k) c[e][k] = *reinterpret_cast<const v2f*>(&sm.stage[h & 1][(lane >> 5) * 4 + e][(lane & 31) * 6 + k * 2]);
            v4i x;
            const bool first = (h & 1) == 0 && h != 20;
#pragma unroll
            for (int k = 0; k < 3; ++k) {
                const v2f a = (keep[k] + (c[0][k] + c[1][k])) + (c[2][k] + c[3][k]), b = a * 0x1p22f;
                x[k] = (int)pack(fold32(b.x, b.x), fold32(b.y, b.y));
                keep[k] = first ? a : v2f{0.f, 0.f};
            }
            x[3] = (int)(tag | mlb);
            if (h == NH - 1) {
                if (lane < 32) {
                    if (BNT & 2) asm volatile("s_nop 4\n\tglobal_store_dwordx4 %0, %1, %2 sc1 nt\n\ts_nop 1" :: "v"(last), "v"(x), "s"(ws + WS_R) : "memory");
                    else st_rec(ws + WS_R, last, x);
                }
                break;
            }
            if (!first) sm.rec[(h >> 1) + (h > 20)][lane & 31] = x;
            asm volatile("" ::: "memory");
            if (lane == 0) st_wg(&sm.prog, h + 2);
            if (h == NH - 2) {
                v4i y[NRC / 2];
                unsigned o[NRC / 2];
#pragma unroll
                for (int u = 0; u < NRC / 2; ++u) { y[u] = (&sm.rec[0][0])[u * 64 + lane]; o[u] = place(u * 2 + (lane >> 5)); }
                until(ld_wg(&sm.tref) + TREL);
#pragma unroll
                for (int u = 0; u < NRC / 2; ++u) {
                    if (BNT & 1) asm volatile("s_nop 4\n\tglobal_store_dwordx4 %0, %1, %2 sc1 nt\n\ts_nop 1" :: "v"(o[u]), "v"(y[u]), "s"(ws + WS_R) : "memory");
                    else st_rec(ws + WS_R, o[u], y[u]);
                }
            }
        }
        return;
    }

    {
        asm volatile("" : "+v"(x[0]), "+v"(x[1]));
        auto gather = [&](unsigned w) {
            w <<= mls;
            w |= dppu<0xB1>(w);
            return w | dppu<0x4E>(w);
        };
        auto half = [](unsigned u) { return (float)__builtin_bit_cast(_Float16, (unsigned short)u); };
        const unsigned ml = gather(x[0].w);
        float m = bf_hi(ml), l = half(ml), a[6];
#pragma unroll
        for (int e = 0; e < 3; ++e) { a[2 * e] = bf_lo(x[0][e]); a[2 * e + 1] = bf_hi(x[0][e]); }
        if (both) {
            const unsigned m2 = gather(x[1].w);
            const float mx = fmaxf(m, bf_hi(m2)), w0 = __expf(m - mx), w1 = __expf(bf_hi(m2) - mx);
#pragma unroll
            for (int e = 0; e < 3; ++e) {
                a[2 * e] = a[2 * e] * w0 + bf_lo(x[1][e]) * w1;
                a[2 * e + 1] = a[2 * e + 1] * w0 + bf_hi(x[1][e]) * w1;
            }
            l = l * w0 + half(m2) * w1;
            m = mx;
        }
        const float M = x4max(m), w0 = __expf(m - M);
        l *= w0;
        float c0 = fold16(fold32(a[0] * w0, a[4] * w0), fold32(a[2] * w0, l));
        float c1 = fold16(fold32(a[1] * w0, a[5] * w0), fold32(a[3] * w0, l));
        c0 += dpp<0x128>(c0); c1 += dpp<0x128>(c1);
        c0 += dpp<0x124>(c0); c1 += dpp<0x124>(c1);
        if (g == 3) c1 = M;
        unsigned arr;
        v4f y;
        float mw;
        asm volatile("ds_write_b64 %3, %4\n\tds_add_rtn_u32 %0, %5, %6\n\tds_read_b32 %2, %8\n\tds_read_b128 %1, %7\n\ts_waitcnt lgkmcnt(0)"
                     : "=&v"(arr), "=&v"(y), "=&v"(mw) : "v"(fa[0]), "v"(v2f{c0, c1}), "v"(fa[1]), "v"(one), "v"(fa[2]), "v"(fa[3]) : "memory");
        const float w = __expf(mw - x4max(mw));
        float d = fold16(fold32(y.x * w, y.z * w), fold32(y.y * w, y.w * w));
        d += dpp<0x128>(d);
        const float L = __int_as_float(__builtin_amdgcn_readlane(__float_as_int(d), 6));
        const unsigned o = pack(d * __builtin_amdgcn_rcpf(L), 0.f);
        if (__builtin_amdgcn_readfirstlane(arr) == WPB - 1 && wave < WPB) {
            if (r < 6) *(unsigned short*)((char*)out + oo) = (unsigned short)o;
        }
    }
}
__global__ void kivi_end_marker() { asm volatile("kivi_code_end:"); }

extern "C" void kernel_launch(void* const* d_in, const int* in_sizes, int n_in,
                              void* d_out, int out_size, void* d_ws, size_t ws_size,
                              hipStream_t stream) {
    const __hip_bfloat16* q = (const __hip_bfloat16*)d_in[0];
    const int* qk = (const int*)d_in[1];
    const int* qv = (const int*)d_in[2];
    const __hip_bfloat16* ks = (const __hip_bfloat16*)d_in[3];
    const __hip_bfloat16* kz = (const __hip_bfloat16*)d_in[4];
    const __hip_bfloat16* vs = (const __hip_bfloat16*)d_in[5];
    const __hip_bfloat16* vz = (const __hip_bfloat16*)d_in[6];
    const float scale = (float)(1.0 / sqrt((double)DH));
    Order order;
    for (int b = 0; b < NWG / 2; ++b) {
        const int p = position(b);
        order.g[b] = groupof(p) | groupof(p + 128) << 8 | groupof(p ^ 1) << 16 | groupof((p ^ 1) + 128) << 24;
    }
    kivi_stream_kernel<<<NWG, NT + 128, 0, stream>>>(q, qk, qv, ks, kz, vs, vz, (float*)d_ws, (__hip_bfloat16*)d_out, scale, order);
}
```
